```python
import torch

def _softmax(x, axis=-1):
    return torch.softmax(x, dim=axis)

def _gelu(x):
    return torch.nn.functional.gelu(x, approximate='tanh')

def _silu(x):
    return torch.nn.functional.silu(x)

def _sigmoid(x):
    return torch.sigmoid(x)

def _layernorm(x, axis=-1, eps=1e-05):
    m = x.mean(dim=axis, keepdim=True)
    v = x.var(dim=axis, keepdim=True, unbiased=False)
    return (x - m) / torch.sqrt(v + eps)

def _scatter_add(tgt, idx, src, axis=0):
    return tgt.clone().index_add_(axis, idx, src)

def _segment_sum(src, seg, n):
    out = torch.zeros((n,) + src.shape[1:], dtype=src.dtype)
    return out.index_add_(0, seg, src)

def _pad(x, pw):
    flat = tuple((v for pair in reversed(pw) for v in pair))
    return torch.nn.functional.pad(x, flat)
kernel_name = 'concat_heads__ssm_conv_s5__memory_aug_recurrent_mem_tr__prompt'

def setup_inputs():

    _w = lambda a: (lambda t: t.to(torch.bfloat16) if t.is_floating_point() else t)(torch.as_tensor(a))

    def _A_setup():
        B, S, D = (1, 2048, 1024)
        P = D
        Lambda_re = -0.5 * (lambda _x: torch.exp(torch.as_tensor(_x)))((lambda *s: torch.randn(*s, dtype=torch.bfloat16) * 0.8)(P))
        Lambda_im = (lambda *s: torch.randn(*s, dtype=torch.bfloat16) * 0.8)(P) * 1.0
        B_re = (lambda *s: torch.randn(*s, dtype=torch.bfloat16) * 0.8)(P, D) * D ** (-0.5)
        B_im = (lambda *s: torch.randn(*s, dtype=torch.bfloat16) * 0.8)(P, D) * D ** (-0.5)
        C_re = (lambda *s: torch.randn(*s, dtype=torch.bfloat16) * 0.8)(D, P) * P ** (-0.5)
        C_im = (lambda *s: torch.randn(*s, dtype=torch.bfloat16) * 0.8)(D, P) * P ** (-0.5)
        log_dt = (lambda *s: torch.randn(*s, dtype=torch.bfloat16) * 0.8)(P) * 0.3 - 2.0
        Dskip = (lambda *s: torch.randn(*s, dtype=torch.bfloat16) * 0.8)(D)
        return {'x': _w((lambda *s: torch.randn(*s, dtype=torch.bfloat16) * 0.05)(B, S, D)), 'Lambda_re': _w(Lambda_re), 'Lambda_im': _w(Lambda_im), 'B_re': _w(B_re), 'B_im': _w(B_im), 'C_re': _w(C_re), 'C_im': _w(C_im), 'log_dt': _w(log_dt), 'Dskip': _w(Dskip)}
    _A_in = _A_setup()

    def _B_setup():
        B, S, Dm = (1, 2048, 1024)
        H, Dh = (32, 128)
        M, seg = (8, 32)
        seg = min(seg, S)
        S = S // seg * seg
        return {'x': _w((lambda *s: torch.randn(*s, dtype=torch.bfloat16) * 0.05)(B, S, Dm)), 'mem0': _w((lambda *s: torch.randn(*s, dtype=torch.bfloat16) * 0.8)(B, M, Dm)), 'Wq': _w((lambda *s: torch.randn(*s, dtype=torch.bfloat16) * 3.2)(Dm, H, Dh) * Dm ** (-0.5)), 'Wk': _w((lambda *s: torch.randn(*s, dtype=torch.bfloat16) * 3.2)(Dm, H, Dh) * Dm ** (-0.5)), 'Wv': _w((lambda *s: torch.randn(*s, dtype=torch.bfloat16) * 0.8)(Dm, H, Dh) * Dm ** (-0.5)), 'Wo': _w((lambda *s: torch.randn(*s, dtype=torch.bfloat16) * 0.8)(H, Dh, Dm) * (H * Dh) ** (-0.5)), 'seg_len': seg}
    _B_in = _B_setup()
    return {'A_x': _A_in['x'], 'A_Lambda_re': _A_in['Lambda_re'], 'A_Lambda_im': _A_in['Lambda_im'], 'A_B_re': _A_in['B_re'], 'A_B_im': _A_in['B_im'], 'A_C_re': _A_in['C_re'], 'A_C_im': _A_in['C_im'], 'A_log_dt': _A_in['log_dt'], 'A_Dskip': _A_in['Dskip'], 'B_x': _B_in['x'], 'B_mem0': _B_in['mem0'], 'B_Wq': _B_in['Wq'], 'B_Wk': _B_in['Wk'], 'B_Wv': _B_in['Wv'], 'B_Wo': _B_in['Wo'], 'B_seg_len': _B_in['seg_len']}

def reference(A_x, A_Lambda_re, A_Lambda_im, A_B_re, A_B_im, A_C_re, A_C_im, A_log_dt, A_Dskip, B_x, B_mem0, B_Wq, B_Wk, B_Wv, B_Wo, B_seg_len):

    def _A_ref(x, Lambda_re, Lambda_im, B_re, B_im, C_re, C_im, log_dt, Dskip):
        Bsz, L, H = x.shape
        Lam = Lambda_re + 1j * Lambda_im
        Bmat = B_re + 1j * B_im
        Cmat = C_re + 1j * C_im
        dt = torch.exp(log_dt)
        Lam_bar = torch.exp(dt * Lam)
        B_bar = ((Lam_bar - 1.0) / Lam)[:, None] * Bmat
        Bu = torch.einsum('bld,pd->blp', x + 0j, B_bar)
        h = torch.zeros_like(Bu[:, 0, :])
        hs = []
        for t in range(L):
            h = Lam_bar[None, :] * h + Bu[:, t, :]
            hs.append(h)
        hseq = torch.stack(hs, 1)
        y = 2.0 * torch.einsum('blp,dp->bld', hseq, Cmat).real + x * Dskip[None, None, :]
        return y

    def _B_ref(x, mem0, Wq, Wk, Wv, Wo, seg_len):
        B, S, Dm = x.shape
        H, Dh = (Wq.shape[1], Wq.shape[2])
        M = mem0.shape[1]
        scale = Dh ** (-0.5)
        n_seg = S // seg_len
        L = M + seg_len
        idx = torch.arange(L)
        mask = (idx[:, None] < M) | (idx[:, None] >= idx[None, :])
        mem = mem0
        outs = []
        for s in range(n_seg):
            xs = x[:, s * seg_len:(s + 1) * seg_len, :].clone()
            h = torch.concatenate([mem, xs], axis=1)
            q = torch.einsum('btd,dhe->bhte', h, Wq)
            k = torch.einsum('btd,dhe->bhte', h, Wk)
            v = torch.einsum('btd,dhe->bhte', h, Wv)
            scores = torch.einsum('bhqe,bhke->bhqk', q, k) * scale
            scores = torch.where(mask[None, None], scores, -1000000000.0)
            p = _softmax(scores, axis=-1)
            o = torch.einsum('bhqk,bhke->bhqe', p, v)
            o = torch.einsum('bhte,hed->btd', o, Wo)
            mem = o[:, :M, :].clone()
            outs.append(o[:, M:, :])
        return torch.concatenate(outs, axis=1)
    a = _A_ref(A_x, A_Lambda_re, A_Lambda_im, A_B_re, A_B_im, A_C_re, A_C_im, A_log_dt, A_Dskip)
    b = _B_ref(B_x, B_mem0, B_Wq, B_Wk, B_Wv, B_Wo, B_seg_len)
    return torch.concatenate([a, b], axis=-1)
```

```cpp
#include <hip/hip_runtime.h>
#include <hip/hip_bf16.h>

namespace {

typedef unsigned short u16;
typedef float f32x4 __attribute__((ext_vector_type(4)));
typedef float f32x2 __attribute__((ext_vector_type(2)));
typedef unsigned v2u __attribute__((ext_vector_type(2)));
typedef unsigned v4u __attribute__((ext_vector_type(4)));
typedef unsigned v8u __attribute__((ext_vector_type(8)));
typedef short s16x4 __attribute__((ext_vector_type(4)));
typedef __bf16 bf16x2 __attribute__((ext_vector_type(2)));
typedef __amdgpu_buffer_rsrc_t rsrc_t;
#define LAS __attribute__((address_space(3)))
#define GAS __attribute__((address_space(1)))
#define DEV __device__ __forceinline__
typedef LAS unsigned char* lptr;

constexpr unsigned RING = 2u << 20, CU = RING - 16384, DONE = CU - 32768, CTL = DONE - 4096, ACK = CTL - 32768, REQ = ACK - 32768;
constexpr unsigned MSG = CU, REP = CU + 1024, RDY = CU + 2048, F_P = CU + 4096;
constexpr unsigned F_O = CU, F_H = CU + 4096;
constexpr unsigned R_MEM = RING, R_K = RING + 128 * 1024, R_O = RING + 256 * 1024, R_V = RING + 384 * 1024;
constexpr unsigned NOWHERE = 0x80000000u;
constexpr unsigned BLK = 4u << 20, B_H = BLK + 512 * 1024, W_DBG = 132u << 20;
constexpr unsigned WS_USED = 134u << 20;
constexpr unsigned ARMED = 0xFFFFFFFFu;
constexpr int SC1 = 16;
constexpr int LDS_BYTES = 160 * 1024;
constexpr unsigned LEADER = 128;

struct Params {
  const u16 *xA, *lre, *lim, *Bre, *Bim, *Cre, *Cim, *ldt, *dsk, *xB, *mem0, *Wq, *Wk, *Wv, *Wo;
  float* out;
  unsigned char* ws;
};

typedef const __attribute__((address_space(4))) Params& PRM;

DEV float bf2f(unsigned b) { return __uint_as_float(b << 16); }
DEV unsigned pk2(float lo, float hi) {
  bf16x2 r;
  r[0] = (__bf16)lo;
  r[1] = (__bf16)hi;
  return __builtin_bit_cast(unsigned, r);
}
DEV rsrc_t mkrs(const void* p, unsigned bytes) { return __builtin_amdgcn_make_buffer_rsrc((void*)p, 0, bytes, 0x00020000); }
DEV v4u mkdesc(const void* p, unsigned bytes) {
  const unsigned long long a = (unsigned long long)p;
  return v4u{(unsigned)a, (unsigned)(a >> 32) & 0xffffu, bytes, 0x00020000u};
}
DEV unsigned phys(unsigned lo) { return ((lo >> 13) << 14) | (lo & 8191u); }
DEV unsigned rotkb(unsigned n, int sh) { return ((n & ~3u) | ((n + (n >> sh)) & 3u)) * 1024u; }
DEV v4u ld128(rsrc_t rs, unsigned vo, unsigned so) { return __builtin_amdgcn_raw_buffer_load_b128(rs, vo, so, SC1); }
DEV unsigned ld32(rsrc_t rs, unsigned vo, unsigned so) { return __builtin_amdgcn_raw_buffer_load_b32(rs, vo, so, SC1); }
DEV void st128(v4u v, rsrc_t rs, unsigned vo, unsigned so) { __builtin_amdgcn_raw_buffer_store_b128(v, rs, vo, so, SC1); }
DEV void st32(unsigned v, rsrc_t rs, unsigned vo, unsigned so) { __builtin_amdgcn_raw_buffer_store_b32(v, rs, vo, so, SC1); }
DEV void drain_bar() { asm volatile("s_waitcnt vmcnt(0) lgkmcnt(0)\n\ts_barrier" ::: "memory"); }
#define SB() __builtin_amdgcn_sched_barrier(0)
DEV unsigned xcc_id() { return __builtin_amdgcn_s_getreg(20 | (3 << 11)) & 7u; }
#define STAMP(s, i)

#define STR_(x) #x
#define STR(x) STR_(x)
#define D_A0 930
#define D_B0 580
#ifndef M_O
#define M_O 0
#endif
#ifndef M_G
#define M_G 20
#endif
#define T_LEAD -60
#define T_LEADA -120
#define T_LEADB -60
#define T_LATE 460
#define T_GO 380
#define T_XS0 120
#define T_XS 120
#define RS_B -80
#define OFF_A 40
#define OFF_V 30
#define BUMP 0
#define ASM_ROLE_V \
    "s_nop 4\n" \
    "s_mov_b32 s98, 0\n" \
    "s_mov_b32 s95, 0\n" \
    "s_add_u32 s97, %[hsa], 32768\n" \
    "buffer_load_dword v31, off, %[ws], s97 sc1\n" \
    "s_add_u32 s97, %[w], 0\n" \
    "s_and_b32 s97, s97, 3\n" \
    "s_sub_u32 s97, s97, 0\n" \
    "s_lshl_b32 s97, s97, 10\n" \
    "v_add_u32 v32, s97, %[vmem]\n" \
    "s_add_u32 s97, %[w], 1\n" \
    "s_and_b32 s97, s97, 3\n" \
    "s_sub_u32 s97, s97, 1\n" \
    "s_lshl_b32 s97, s97, 10\n" \
    "v_add_u32 v33, s97, %[vmem]\n" \
    "s_add_u32 s97, %[w], 2\n" \
    "s_and_b32 s97, s97, 3\n" \
    "s_sub_u32 s97, s97, 2\n" \
    "s_lshl_b32 s97, s97, 10\n" \
    "v_add_u32 v34, s97, %[vmem]\n" \
    "s_add_u32 s97, %[w], 3\n" \
    "s_and_b32 s97, s97, 3\n" \
    "s_sub_u32 s97, s97, 3\n" \
    "s_lshl_b32 s97, s97, 10\n" \
    "v_add_u32 v35, s97, %[vmem]\n" \
    "v_mov_b32 v200, %[fo]\n" \
    "v_xor_b32 v201, 64, %[fo]\n" \
    "v_add_u32 v202, 0x10000, v200\n" \
    "v_add_u32 v203, 0x10000, v201\n" \
    "s_lshr_b32 s61, %[w], 1\n" \
    "s_lshl_b32 s61, s61, 5\n" \
    "s_and_b32 s97, %[w], 1\n" \
    "s_add_u32 s61, s61, s97\n" \
    "s_lshl_b32 s61, s61, 10\n" \
    "s_lshl_b32 s97, %[w], 13\n" \
    "s_add_u32 s61, s61, s97\n" \
    "s_lshl_b32 s60, %[w], 9\n" \
    "s_xor_b32 m0, s61, 0\n" \
    "s_xor_b32 s97, s60, 0\n" \
    "buffer_load_dwordx4 %[vox], %[dx], s97 offen lds\n" \
    "s_xor_b32 m0, s61, 2048\n" \
    "s_xor_b32 s97, s60, 128\n" \
    "buffer_load_dwordx4 %[vox], %[dx], s97 offen lds\n" \
    "s_xor_b32 m0, s61, 4096\n" \
    "s_xor_b32 s97, s60, 256\n" \
    "buffer_load_dwordx4 %[vox], %[dx], s97 offen lds\n" \
    "s_xor_b32 m0, s61, 6144\n" \
    "s_xor_b32 s97, s60, 384\n" \
    "buffer_load_dwordx4 %[vox], %[dx], s97 offen lds\n" \
    "s_xor_b32 m0, s61, 8192\n" \
    "s_xor_b32 s97, s60, 512\n" \
    "buffer_load_dwordx4 %[vox], %[dx], s97 offen lds\n" \
    "s_xor_b32 m0, s61, 10240\n" \
    "s_xor_b32 s97, s60, 640\n" \
    "buffer_load_dwordx4 %[vox], %[dx], s97 offen lds\n" \
    "s_xor_b32 m0, s61, 12288\n" \
    "s_xor_b32 s97, s60, 768\n" \
    "buffer_load_dwordx4 %[vox], %[dx], s97 offen lds\n" \
    "s_xor_b32 m0, s61, 14336\n" \
    "s_xor_b32 s97, s60, 896\n" \
    "buffer_load_dwordx4 %[vox], %[dx], s97 offen lds\n" \
    "s_xor_b32 m0, s61, 16384\n" \
    "s_xor_b32 s97, s60, 1024\n" \
    "buffer_load_dwordx4 %[vox], %[dx], s97 offen lds\n" \
    "s_xor_b32 m0, s61, 18432\n" \
    "s_xor_b32 s97, s60, 1152\n" \
    "buffer_load_dwordx4 %[vox], %[dx], s97 offen lds\n" \
    "s_xor_b32 m0, s61, 20480\n" \
    "s_xor_b32 s97, s60, 1280\n" \
    "buffer_load_dwordx4 %[vox], %[dx], s97 offen lds\n" \
    "s_xor_b32 m0, s61, 22528\n" \
    "s_xor_b32 s97, s60, 1408\n" \
    "buffer_load_dwordx4 %[vox], %[dx], s97 offen lds\n" \
    "s_xor_b32 m0, s61, 24576\n" \
    "s_xor_b32 s97, s60, 1536\n" \
    "buffer_load_dwordx4 %[vox], %[dx], s97 offen lds\n" \
    "s_xor_b32 m0, s61, 26624\n" \
    "s_xor_b32 s97, s60, 1664\n" \
    "buffer_load_dwordx4 %[vox], %[dx], s97 offen lds\n" \
    "s_xor_b32 m0, s61, 28672\n" \
    "s_xor_b32 s97, s60, 1792\n" \
    "buffer_load_dwordx4 %[vox], %[dx], s97 offen lds\n" \
    "s_xor_b32 m0, s61, 30720\n" \
    "s_xor_b32 s97, s60, 1920\n" \
    "buffer_load_dwordx4 %[vox], %[dx], s97 offen lds\n" \
    "s_add_u32 s60, s60, 65536\n" \
    "v_xor_b32 v130, 128, %[vow]\n" \
    "v_mov_b32 v131, %[ra]\n" \
    "v_xor_b32 v132, 32, %[ra]\n" \
    "v_add_u32 v133, 0x10000, v131\n" \
    "v_add_u32 v134, 0x10000, v132\n" \
    "s_lshl_b32 s64, %[w], 13\n" \
    "s_lshl_b32 s65, %[w], 18\n" \
    "s_add_u32 m0, s64, 65536\n" \
    "s_add_u32 s97, s65, 0\n" \
    "buffer_load_dwordx4 %[vow], %[wd], s97 offen nt lds\n" \
    "s_add_u32 m0, s64, 66560\n" \
    "s_add_u32 s97, s65, 65536\n" \
    "buffer_load_dwordx4 v130, %[wd], s97 offen nt lds\n" \
    "s_add_u32 m0, s64, 67584\n" \
    "s_add_u32 s97, s65, 131072\n" \
    "buffer_load_dwordx4 %[vow], %[wd], s97 offen nt lds\n" \
    "s_add_u32 m0, s64, 68608\n" \
    "s_add_u32 s97, s65, 196608\n" \
    "buffer_load_dwordx4 v130, %[wd], s97 offen nt lds\n" \
    "s_add_u32 m0, s64, 69632\n" \
    "s_add_u32 s97, s65, 32768\n" \
    "buffer_load_dwordx4 %[vow], %[wd], s97 offen nt lds\n" \
    "s_add_u32 m0, s64, 70656\n" \
    "s_add_u32 s97, s65, 98304\n" \
    "buffer_load_dwordx4 v130, %[wd], s97 offen nt lds\n" \
    "s_add_u32 m0, s64, 71680\n" \
    "s_add_u32 s97, s65, 163840\n" \
    "buffer_load_dwordx4 %[vow], %[wd], s97 offen nt lds\n" \
    "s_add_u32 m0, s64, 72704\n" \
    "s_add_u32 s97, s65, 229376\n" \
    "buffer_load_dwordx4 v130, %[wd], s97 offen nt lds\n" \
    "s_add_u32 m0, s64, 98304\n" \
    "s_add_u32 s97, s65, 1048576\n" \
    "buffer_load_dwordx4 %[vow], %[wd], s97 offen nt lds\n" \
    "s_add_u32 m0, s64, 99328\n" \
    "s_add_u32 s97, s65, 1114112\n" \
    "buffer_load_dwordx4 v130, %[wd], s97 offen nt lds\n" \
    "s_add_u32 m0, s64, 100352\n" \
    "s_add_u32 s97, s65, 1179648\n" \
    "buffer_load_dwordx4 %[vow], %[wd], s97 offen nt lds\n" \
    "s_add_u32 m0, s64, 101376\n" \
    "s_add_u32 s97, s65, 1245184\n" \
    "buffer_load_dwordx4 v130, %[wd], s97 offen nt lds\n" \
    "s_add_u32 m0, s64, 102400\n" \
    "s_add_u32 s97, s65, 1081344\n" \
    "buffer_load_dwordx4 %[vow], %[wd], s97 offen nt lds\n" \
    "s_add_u32 m0, s64, 103424\n" \
    "s_add_u32 s97, s65, 1146880\n" \
    "buffer_load_dwordx4 v130, %[wd], s97 offen nt lds\n" \
    "s_add_u32 m0, s64, 104448\n" \
    "s_add_u32 s97, s65, 1212416\n" \
    "buffer_load_dwordx4 %[vow], %[wd], s97 offen nt lds\n" \
    "s_add_u32 m0, s64, 105472\n" \
    "s_add_u32 s97, s65, 1277952\n" \
    "buffer_load_dwordx4 v130, %[wd], s97 offen nt lds\n" \
    "s_waitcnt vmcnt(8) lgkmcnt(0)\n" \
    "s_barrier\n" \
    "v_readfirstlane_b32 %[cfg], v31\n" \
    "s_nop 1\n" \
    "s_add_u32 %[cfg], %[cfg], 256\n" \
    "s_and_b32 %[cfg], %[cfg], 0xffffff00\n" \
    "v_mov_b32 v31, %[cfg]\n" \
    "buffer_store_dword v31, off, %[ws], %[hsa] sc1\n" \
    "s_add_u32 m0, s64, 131072\n" \
    "s_add_u32 s97, s65, 2097152\n" \
    "buffer_load_dwordx4 %[vow], %[wd], s97 offen nt lds\n" \
    "s_add_u32 m0, s64, 132096\n" \
    "s_add_u32 s97, s65, 2162688\n" \
    "buffer_load_dwordx4 v130, %[wd], s97 offen nt lds\n" \
    "s_add_u32 m0, s64, 133120\n" \
    "s_add_u32 s97, s65, 2228224\n" \
    "buffer_load_dwordx4 %[vow], %[wd], s97 offen nt lds\n" \
    "s_add_u32 m0, s64, 134144\n" \
    "s_add_u32 s97, s65, 2293760\n" \
    "buffer_load_dwordx4 v130, %[wd], s97 offen nt lds\n" \
    "s_add_u32 m0, s64, 135168\n" \
    "s_add_u32 s97, s65, 2129920\n" \
    "buffer_load_dwordx4 %[vow], %[wd], s97 offen nt lds\n" \
    "s_add_u32 m0, s64, 136192\n" \
    "s_add_u32 s97, s65, 2195456\n" \
    "buffer_load_dwordx4 v130, %[wd], s97 offen nt lds\n" \
    "s_add_u32 m0, s64, 137216\n" \
    "s_add_u32 s97, s65, 2260992\n" \
    "buffer_load_dwordx4 %[vow], %[wd], s97 offen nt lds\n" \
    "s_add_u32 m0, s64, 138240\n" \
    "s_add_u32 s97, s65, 2326528\n" \
    "buffer_load_dwordx4 v130, %[wd], s97 offen nt lds\n" \
    "ds_read_b64_tr_b16 a[0:1], v131 offset:0\n" \
    "ds_read_b64_tr_b16 a[2:3], v131 offset:4096\n" \
    "ds_read_b64_tr_b16 a[8:9], v132 offset:0\n" \
    "ds_read_b64_tr_b16 a[10:11], v132 offset:4096\n" \
    "ds_read_b64_tr_b16 a[4:5], v131 offset:8192\n" \
    "ds_read_b64_tr_b16 a[6:7], v131 offset:12288\n" \
    "ds_read_b64_tr_b16 a[12:13], v132 offset:8192\n" \
    "ds_read_b64_tr_b16 a[14:15], v132 offset:12288\n" \
    "ds_read_b64_tr_b16 a[16:17], v131 offset:16384\n" \
    "ds_read_b64_tr_b16 a[18:19], v131 offset:20480\n" \
    "ds_read_b64_tr_b16 a[24:25], v132 offset:16384\n" \
    "ds_read_b64_tr_b16 a[26:27], v132 offset:20480\n" \
    "ds_read_b64_tr_b16 a[20:21], v131 offset:24576\n" \
    "ds_read_b64_tr_b16 a[22:23], v131 offset:28672\n" \
    "ds_read_b64_tr_b16 a[28:29], v132 offset:24576\n" \
    "ds_read_b64_tr_b16 a[30:31], v132 offset:28672\n" \
    "s_waitcnt vmcnt(8) lgkmcnt(0)\n" \
    "s_barrier\n" \
    "s_add_u32 m0, s64, 65536\n" \
    "s_add_u32 s97, s65, 3145728\n" \
    "buffer_load_dwordx4 %[vow], %[wd], s97 offen nt lds\n" \
    "s_add_u32 m0, s64, 66560\n" \
    "s_add_u32 s97, s65, 3211264\n" \
    "buffer_load_dwordx4 v130, %[wd], s97 offen nt lds\n" \
    "s_add_u32 m0, s64, 67584\n" \
    "s_add_u32 s97, s65, 3276800\n" \
    "buffer_load_dwordx4 %[vow], %[wd], s97 offen nt lds\n" \
    "s_add_u32 m0, s64, 68608\n" \
    "s_add_u32 s97, s65, 3342336\n" \
    "buffer_load_dwordx4 v130, %[wd], s97 offen nt lds\n" \
    "s_add_u32 m0, s64, 69632\n" \
    "s_add_u32 s97, s65, 3178496\n" \
    "buffer_load_dwordx4 %[vow], %[wd], s97 offen nt lds\n" \
    "s_add_u32 m0, s64, 70656\n" \
    "s_add_u32 s97, s65, 3244032\n" \
    "buffer_load_dwordx4 v130, %[wd], s97 offen nt lds\n" \
    "s_add_u32 m0, s64, 71680\n" \
    "s_add_u32 s97, s65, 3309568\n" \
    "buffer_load_dwordx4 %[vow], %[wd], s97 offen nt lds\n" \
    "s_add_u32 m0, s64, 72704\n" \
    "s_add_u32 s97, s65, 3375104\n" \
    "buffer_load_dwordx4 v130, %[wd], s97 offen nt lds\n" \
    "ds_read_b64_tr_b16 a[32:33], v131 offset:32768\n" \
    "ds_read_b64_tr_b16 a[34:35], v131 offset:36864\n" \
    "ds_read_b64_tr_b16 a[40:41], v132 offset:32768\n" \
    "ds_read_b64_tr_b16 a[42:43], v132 offset:36864\n" \
    "ds_read_b64_tr_b16 a[36:37], v131 offset:40960\n" \
    "ds_read_b64_tr_b16 a[38:39], v131 offset:45056\n" \
    "ds_read_b64_tr_b16 a[44:45], v132 offset:40960\n" \
    "ds_read_b64_tr_b16 a[46:47], v132 offset:45056\n" \
    "ds_read_b64_tr_b16 a[48:49], v131 offset:49152\n" \
    "ds_read_b64_tr_b16 a[50:51], v131 offset:53248\n" \
    "ds_read_b64_tr_b16 a[56:57], v132 offset:49152\n" \
    "ds_read_b64_tr_b16 a[58:59], v132 offset:53248\n" \
    "ds_read_b64_tr_b16 a[52:53], v131 offset:57344\n" \
    "ds_read_b64_tr_b16 a[54:55], v131 offset:61440\n" \
    "ds_read_b64_tr_b16 a[60:61], v132 offset:57344\n" \
    "ds_read_b64_tr_b16 a[62:63], v132 offset:61440\n" \
    "s_waitcnt vmcnt(8) lgkmcnt(0)\n" \
    "s_barrier\n" \
    "s_add_u32 m0, s64, 98304\n" \
    "s_add_u32 s97, s65, 4194304\n" \
    "buffer_load_dwordx4 %[vow], %[wd], s97 offen nt lds\n" \
    "s_add_u32 m0, s64, 99328\n" \
    "s_add_u32 s97, s65, 4259840\n" \
    "buffer_load_dwordx4 v130, %[wd], s97 offen nt lds\n" \
    "s_add_u32 m0, s64, 100352\n" \
    "s_add_u32 s97, s65, 4325376\n" \
    "buffer_load_dwordx4 %[vow], %[wd], s97 offen nt lds\n" \
    "s_add_u32 m0, s64, 101376\n" \
    "s_add_u32 s97, s65, 4390912\n" \
    "buffer_load_dwordx4 v130, %[wd], s97 offen nt lds\n" \
    "s_add_u32 m0, s64, 102400\n" \
    "s_add_u32 s97, s65, 4227072\n" \
    "buffer_load_dwordx4 %[vow], %[wd], s97 offen nt lds\n" \
    "s_add_u32 m0, s64, 103424\n" \
    "s_add_u32 s97, s65, 4292608\n" \
    "buffer_load_dwordx4 v130, %[wd], s97 offen nt lds\n" \
    "s_add_u32 m0, s64, 104448\n" \
    "s_add_u32 s97, s65, 4358144\n" \
    "buffer_load_dwordx4 %[vow], %[wd], s97 offen nt lds\n" \
    "s_add_u32 m0, s64, 105472\n" \
    "s_add_u32 s97, s65, 4423680\n" \
    "buffer_load_dwordx4 v130, %[wd], s97 offen nt lds\n" \
    "ds_read_b64_tr_b16 a[64:65], v133 offset:0\n" \
    "ds_read_b64_tr_b16 a[66:67], v133 offset:4096\n" \
    "ds_read_b64_tr_b16 a[72:73], v134 offset:0\n" \
    "ds_read_b64_tr_b16 a[74:75], v134 offset:4096\n" \
    "ds_read_b64_tr_b16 a[68:69], v133 offset:8192\n" \
    "ds_read_b64_tr_b16 a[70:71], v133 offset:12288\n" \
    "ds_read_b64_tr_b16 a[76:77], v134 offset:8192\n" \
    "ds_read_b64_tr_b16 a[78:79], v134 offset:12288\n" \
    "ds_read_b64_tr_b16 a[80:81], v133 offset:16384\n" \
    "ds_read_b64_tr_b16 a[82:83], v133 offset:20480\n" \
    "ds_read_b64_tr_b16 a[88:89], v134 offset:16384\n" \
    "ds_read_b64_tr_b16 a[90:91], v134 offset:20480\n" \
    "ds_read_b64_tr_b16 a[84:85], v133 offset:24576\n" \
    "ds_read_b64_tr_b16 a[86:87], v133 offset:28672\n" \
    "ds_read_b64_tr_b16 a[92:93], v134 offset:24576\n" \
    "ds_read_b64_tr_b16 a[94:95], v134 offset:28672\n" \
    "s_waitcnt vmcnt(8) lgkmcnt(0)\n" \
    "s_barrier\n" \
    "s_add_u32 m0, s64, 131072\n" \
    "s_add_u32 s97, s65, 5242880\n" \
    "buffer_load_dwordx4 %[vow], %[wd], s97 offen nt lds\n" \
    "s_add_u32 m0, s64, 132096\n" \
    "s_add_u32 s97, s65, 5308416\n" \
    "buffer_load_dwordx4 v130, %[wd], s97 offen nt lds\n" \
    "s_add_u32 m0, s64, 133120\n" \
    "s_add_u32 s97, s65, 5373952\n" \
    "buffer_load_dwordx4 %[vow], %[wd], s97 offen nt lds\n" \
    "s_add_u32 m0, s64, 134144\n" \
    "s_add_u32 s97, s65, 5439488\n" \
    "buffer_load_dwordx4 v130, %[wd], s97 offen nt lds\n" \
    "s_add_u32 m0, s64, 135168\n" \
    "s_add_u32 s97, s65, 5275648\n" \
    "buffer_load_dwordx4 %[vow], %[wd], s97 offen nt lds\n" \
    "s_add_u32 m0, s64, 136192\n" \
    "s_add_u32 s97, s65, 5341184\n" \
    "buffer_load_dwordx4 v130, %[wd], s97 offen nt lds\n" \
    "s_add_u32 m0, s64, 137216\n" \
    "s_add_u32 s97, s65, 5406720\n" \
    "buffer_load_dwordx4 %[vow], %[wd], s97 offen nt lds\n" \
    "s_add_u32 m0, s64, 138240\n" \
    "s_add_u32 s97, s65, 5472256\n" \
    "buffer_load_dwordx4 v130, %[wd], s97 offen nt lds\n" \
    "ds_read_b64_tr_b16 a[96:97], v131 offset:0\n" \
    "ds_read_b64_tr_b16 a[98:99], v131 offset:4096\n" \
    "ds_read_b64_tr_b16 a[104:105], v132 offset:0\n" \
    "ds_read_b64_tr_b16 a[106:107], v132 offset:4096\n" \
    "ds_read_b64_tr_b16 a[100:101], v131 offset:8192\n" \
    "ds_read_b64_tr_b16 a[102:103], v131 offset:12288\n" \
    "ds_read_b64_tr_b16 a[108:109], v132 offset:8192\n" \
    "ds_read_b64_tr_b16 a[110:111], v132 offset:12288\n" \
    "ds_read_b64_tr_b16 a[112:113], v131 offset:16384\n" \
    "ds_read_b64_tr_b16 a[114:115], v131 offset:20480\n" \
    "ds_read_b64_tr_b16 a[120:121], v132 offset:16384\n" \
    "ds_read_b64_tr_b16 a[122:123], v132 offset:20480\n" \
    "ds_read_b64_tr_b16 a[116:117], v131 offset:24576\n" \
    "ds_read_b64_tr_b16 a[118:119], v131 offset:28672\n" \
    "ds_read_b64_tr_b16 a[124:125], v132 offset:24576\n" \
    "ds_read_b64_tr_b16 a[126:127], v132 offset:28672\n" \
    "s_waitcnt vmcnt(8) lgkmcnt(0)\n" \
    "s_barrier\n" \
    "s_add_u32 m0, s64, 65536\n" \
    "s_add_u32 s97, s65, 6291456\n" \
    "buffer_load_dwordx4 %[vow], %[wd], s97 offen nt lds\n" \
    "s_add_u32 m0, s64, 66560\n" \
    "s_add_u32 s97, s65, 6356992\n" \
    "buffer_load_dwordx4 v130, %[wd], s97 offen nt lds\n" \
    "s_add_u32 m0, s64, 67584\n" \
    "s_add_u32 s97, s65, 6422528\n" \
    "buffer_load_dwordx4 %[vow], %[wd], s97 offen nt lds\n" \
    "s_add_u32 m0, s64, 68608\n" \
    "s_add_u32 s97, s65, 6488064\n" \
    "buffer_load_dwordx4 v130, %[wd], s97 offen nt lds\n" \
    "s_add_u32 m0, s64, 69632\n" \
    "s_add_u32 s97, s65, 6324224\n" \
    "buffer_load_dwordx4 %[vow], %[wd], s97 offen nt lds\n" \
    "s_add_u32 m0, s64, 70656\n" \
    "s_add_u32 s97, s65, 6389760\n" \
    "buffer_load_dwordx4 v130, %[wd], s97 offen nt lds\n" \
    "s_add_u32 m0, s64, 71680\n" \
    "s_add_u32 s97, s65, 6455296\n" \
    "buffer_load_dwordx4 %[vow], %[wd], s97 offen nt lds\n" \
    "s_add_u32 m0, s64, 72704\n" \
    "s_add_u32 s97, s65, 6520832\n" \
    "buffer_load_dwordx4 v130, %[wd], s97 offen nt lds\n" \
    "ds_read_b64_tr_b16 a[128:129], v131 offset:32768\n" \
    "ds_read_b64_tr_b16 a[130:131], v131 offset:36864\n" \
    "ds_read_b64_tr_b16 a[136:137], v132 offset:32768\n" \
    "ds_read_b64_tr_b16 a[138:139], v132 offset:36864\n" \
    "ds_read_b64_tr_b16 a[132:133], v131 offset:40960\n" \
    "ds_read_b64_tr_b16 a[134:135], v131 offset:45056\n" \
    "ds_read_b64_tr_b16 a[140:141], v132 offset:40960\n" \
    "ds_read_b64_tr_b16 a[142:143], v132 offset:45056\n" \
    "ds_read_b64_tr_b16 a[144:145], v131 offset:49152\n" \
    "ds_read_b64_tr_b16 a[146:147], v131 offset:53248\n" \
    "ds_read_b64_tr_b16 a[152:153], v132 offset:49152\n" \
    "ds_read_b64_tr_b16 a[154:155], v132 offset:53248\n" \
    "ds_read_b64_tr_b16 a[148:149], v131 offset:57344\n" \
    "ds_read_b64_tr_b16 a[150:151], v131 offset:61440\n" \
    "ds_read_b64_tr_b16 a[156:157], v132 offset:57344\n" \
    "ds_read_b64_tr_b16 a[158:159], v132 offset:61440\n" \
    "s_waitcnt vmcnt(8) lgkmcnt(0)\n" \
    "s_barrier\n" \
    "s_add_u32 m0, s64, 98304\n" \
    "s_add_u32 s97, s65, 7340032\n" \
    "buffer_load_dwordx4 %[vow], %[wd], s97 offen nt lds\n" \
    "s_add_u32 m0, s64, 99328\n" \
    "s_add_u32 s97, s65, 7405568\n" \
    "buffer_load_dwordx4 v130, %[wd], s97 offen nt lds\n" \
    "s_add_u32 m0, s64, 100352\n" \
    "s_add_u32 s97, s65, 7471104\n" \
    "buffer_load_dwordx4 %[vow], %[wd], s97 offen nt lds\n" \
    "s_add_u32 m0, s64, 101376\n" \
    "s_add_u32 s97, s65, 7536640\n" \
    "buffer_load_dwordx4 v130, %[wd], s97 offen nt lds\n" \
    "s_add_u32 m0, s64, 102400\n" \
    "s_add_u32 s97, s65, 7372800\n" \
    "buffer_load_dwordx4 %[vow], %[wd], s97 offen nt lds\n" \
    "s_add_u32 m0, s64, 103424\n" \
    "s_add_u32 s97, s65, 7438336\n" \
    "buffer_load_dwordx4 v130, %[wd], s97 offen nt lds\n" \
    "s_add_u32 m0, s64, 104448\n" \
    "s_add_u32 s97, s65, 7503872\n" \
    "buffer_load_dwordx4 %[vow], %[wd], s97 offen nt lds\n" \
    "s_add_u32 m0, s64, 105472\n" \
    "s_add_u32 s97, s65, 7569408\n" \
    "buffer_load_dwordx4 v130, %[wd], s97 offen nt lds\n" \
    "ds_read_b64_tr_b16 a[160:161], v133 offset:0\n" \
    "ds_read_b64_tr_b16 a[162:163], v133 offset:4096\n" \
    "ds_read_b64_tr_b16 a[168:169], v134 offset:0\n" \
    "ds_read_b64_tr_b16 a[170:171], v134 offset:4096\n" \
    "ds_read_b64_tr_b16 a[164:165], v133 offset:8192\n" \
    "ds_read_b64_tr_b16 a[166:167], v133 offset:12288\n" \
    "ds_read_b64_tr_b16 a[172:173], v134 offset:8192\n" \
    "ds_read_b64_tr_b16 a[174:175], v134 offset:12288\n" \
    "ds_read_b64_tr_b16 a[176:177], v133 offset:16384\n" \
    "ds_read_b64_tr_b16 a[178:179], v133 offset:20480\n" \
    "ds_read_b64_tr_b16 a[184:185], v134 offset:16384\n" \
    "ds_read_b64_tr_b16 a[186:187], v134 offset:20480\n" \
    "ds_read_b64_tr_b16 a[180:181], v133 offset:24576\n" \
    "ds_read_b64_tr_b16 a[182:183], v133 offset:28672\n" \
    "ds_read_b64_tr_b16 a[188:189], v134 offset:24576\n" \
    "ds_read_b64_tr_b16 a[190:191], v134 offset:28672\n" \
    "s_waitcnt vmcnt(8) lgkmcnt(0)\n" \
    "s_barrier\n" \
    "s_add_u32 s97, %[hsa], 32768\n" \
    "buffer_load_dword v31, off, %[ws], s97 sc1\n" \
    "ds_read_b64_tr_b16 a[192:193], v131 offset:0\n" \
    "ds_read_b64_tr_b16 a[194:195], v131 offset:4096\n" \
    "ds_read_b64_tr_b16 a[200:201], v132 offset:0\n" \
    "ds_read_b64_tr_b16 a[202:203], v132 offset:4096\n" \
    "ds_read_b64_tr_b16 a[196:197], v131 offset:8192\n" \
    "ds_read_b64_tr_b16 a[198:199], v131 offset:12288\n" \
    "ds_read_b64_tr_b16 a[204:205], v132 offset:8192\n" \
    "ds_read_b64_tr_b16 a[206:207], v132 offset:12288\n" \
    "ds_read_b64_tr_b16 a[208:209], v131 offset:16384\n" \
    "ds_read_b64_tr_b16 a[210:211], v131 offset:20480\n" \
    "ds_read_b64_tr_b16 a[216:217], v132 offset:16384\n" \
    "ds_read_b64_tr_b16 a[218:219], v132 offset:20480\n" \
    "ds_read_b64_tr_b16 a[212:213], v131 offset:24576\n" \
    "ds_read_b64_tr_b16 a[214:215], v131 offset:28672\n" \
    "ds_read_b64_tr_b16 a[220:221], v132 offset:24576\n" \
    "ds_read_b64_tr_b16 a[222:223], v132 offset:28672\n" \
    "s_waitcnt vmcnt(0) lgkmcnt(0)\n" \
    "s_barrier\n" \
    "ds_read_b64_tr_b16 a[224:225], v131 offset:32768\n" \
    "ds_read_b64_tr_b16 a[226:227], v131 offset:36864\n" \
    "ds_read_b64_tr_b16 a[232:233], v132 offset:32768\n" \
    "ds_read_b64_tr_b16 a[234:235], v132 offset:36864\n" \
    "ds_read_b64_tr_b16 a[228:229], v131 offset:40960\n" \
    "ds_read_b64_tr_b16 a[230:231], v131 offset:45056\n" \
    "ds_read_b64_tr_b16 a[236:237], v132 offset:40960\n" \
    "ds_read_b64_tr_b16 a[238:239], v132 offset:45056\n" \
    "ds_read_b64_tr_b16 a[240:241], v131 offset:49152\n" \
    "ds_read_b64_tr_b16 a[242:243], v131 offset:53248\n" \
    "ds_read_b64_tr_b16 a[248:249], v132 offset:49152\n" \
    "ds_read_b64_tr_b16 a[250:251], v132 offset:53248\n" \
    "ds_read_b64_tr_b16 a[244:245], v131 offset:57344\n" \
    "ds_read_b64_tr_b16 a[246:247], v131 offset:61440\n" \
    "ds_read_b64_tr_b16 a[252:253], v132 offset:57344\n" \
    "ds_read_b64_tr_b16 a[254:255], v132 offset:61440\n" \
    "s_waitcnt lgkmcnt(0)\n" \
    "s_branch LV_hsl_%=\n" \
    "LV_hs_%=:\n" \
    "s_add_u32 s97, %[hsa], 32768\n" \
    "buffer_load_dword v31, off, %[ws], s97 sc1\n" \
    "s_waitcnt vmcnt(0)\n" \
    "LV_hsl_%=:\n" \
    "v_readfirstlane_b32 s97, v31\n" \
    "s_nop 1\n" \
    "s_xor_b32 s97, s97, %[cfg]\n" \
    "s_cmp_gt_u32 s97, 255\n" \
    "s_cbranch_scc1 LV_hsr_%=\n" \
    "s_and_b32 s96, s97, 3\n" \
    "s_cmp_lg_u32 s96, %[est]\n" \
    "s_cbranch_scc1 LV_redo_%=\n" \
    "s_bitcmp0_b32 s97, 3\n" \
    "s_cbranch_scc1 LV_hsr_%=\n" \
    "s_mov_b32 %[cfg], s97\n" \
    "LV_hsok_%=:\n" \
    "s_and_b32 %[pol], %[cfg], 4\n" \
    "s_lshl_b32 %[pol], %[pol], 11\n" \
    "s_xor_b32 %[npol], %[pol], 8192\n" \
    "s_waitcnt vmcnt(0)\n" \
    "v_mov_b32 v31, 0\n" \
    "s_add_u32 s97, %[hsa], 69632\n" \
    "buffer_store_dword v31, off, %[ws], s97 sc1\n" \
    "s_mov_b32 s94, %[pol]\n" \
    "s_add_u32 s99, s94, 524288\n" \
    "s_mov_b32 s84, s94\n" \
    "v_mov_b32 v136, -1\n" \
    "v_mov_b32 v137, -1\n" \
    "s_lshl_b32 s57, %[w], 12\n" \
    "s_add_u32 s57, s57, 131072\n" \
    "buffer_store_dwordx2 v[136:137], %[vv], %[ws], s94 offen sc1\n" \
    "s_mov_b32 s66, 0\n" \
    "s_mov_b32 s62, %[npol]\n" \
    "v_mov_b64 v[120:121], 0\n" \
    "v_mov_b64 v[122:123], 0\n" \
    "v_mov_b64 v[124:125], 0\n" \
    "v_mov_b64 v[126:127], 0\n" \
    "s_barrier\n" \
    "s_branch LV_boot0_%=\n" \
    "LV_top_%=:\n" \
    "s_cmp_lg_u32 %[w], 0\n" \
    "s_cbranch_scc1 LV_tmx_%=\n" \
    "LV_tm_%=:\n" \
    "s_memrealtime s[72:73]\n" \
    "s_nop 1\n" \
    "s_memrealtime s[74:75]\n" \
    "s_nop 1\n" \
    "s_memrealtime s[76:77]\n" \
    "s_nop 1\n" \
    "s_memrealtime s[78:79]\n" \
    "s_nop 1\n" \
    "s_memrealtime s[80:81]\n" \
    "s_nop 1\n" \
    "s_memrealtime s[82:83]\n" \
    "s_nop 1\n" \
    "s_memrealtime s[92:93]\n" \
    "s_nop 1\n" \
    "s_memrealtime s[70:71]\n" \
    "s_getpc_b64 s[68:69]\n" \
    "LV_tmp_%=:\n" \
    "s_add_u32 s68, s68, LV_tmx_%= - LV_tmp_%=\n" \
    "s_addc_u32 s69, s69, 0\n" \
    "s_waitcnt lgkmcnt(0)\n" \
    "s_sub_u32 s97, s87, s72\n" \
    "s_sub_u32 s97, s97, s74\n" \
    "s_sub_u32 s97, s97, s76\n" \
    "s_sub_u32 s97, s97, s78\n" \
    "s_sub_u32 s97, s97, s80\n" \
    "s_sub_u32 s97, s97, s82\n" \
    "s_sub_u32 s97, s97, s92\n" \
    "s_sub_u32 s97, s97, s70\n" \
    "s_cmp_gt_u32 s97, 160\n" \
    "s_cbranch_scc1 LV_tmc_%=\n" \
    "s_mul_i32 s97, s97, 47\n" \
    "s_lshr_b32 s97, s97, 4\n" \
    "s_and_b32 s97, s97, -4\n" \
    "s_sub_u32 s68, s68, s97\n" \
    "s_subb_u32 s69, s69, 0\n" \
    "s_setpc_b64 s[68:69]\n" \
    ".rept 118\n" \
    "s_nop 0\n" \
    ".endr\n" \
    "LV_tmx_%=:\n" \
    "s_barrier\n" \
    "buffer_load_dwordx4 v32, %[ws], s94 offen sc1 lds\n" \
    "ds_read_b128 v[144:147], v200 offset:0\n" \
    "ds_read_b128 v[148:151], v200 offset:32768\n" \
    "ds_read_b128 v[152:155], v201 offset:0\n" \
    "ds_read_b128 v[156:159], v201 offset:32768\n" \
    "ds_read_b128 v[160:163], v200 offset:2048\n" \
    "s_xor_b32 s99, s94, 1048576\n" \
    "s_waitcnt lgkmcnt(4)\n" \
    "v_mfma_f32_16x16x32_bf16 v[176:179], v[144:147], a[0:3], 0\n" \
    "ds_read_b128 v[164:167], v200 offset:34816\n" \
    "v_mfma_f32_16x16x32_bf16 v[184:187], v[144:147], a[8:11], 0\n" \
    "buffer_load_dwordx4 v33, %[ws], s94 offen offset:1024 sc1 lds\n" \
    "s_waitcnt lgkmcnt(4)\n" \
    "v_mfma_f32_16x16x32_bf16 v[180:183], v[148:151], a[0:3], 0\n" \
    "ds_read_b128 v[168:171], v201 offset:2048\n" \
    "v_mfma_f32_16x16x32_bf16 v[188:191], v[148:151], a[8:11], 0\n" \
    "s_waitcnt lgkmcnt(4)\n" \
    "v_mfma_f32_16x16x32_bf16 v[176:179], v[152:155], a[4:7], v[176:179]\n" \
    "ds_read_b128 v[172:175], v201 offset:34816\n" \
    "v_mfma_f32_16x16x32_bf16 v[184:187], v[152:155], a[12:15], v[184:187]\n" \
    "s_waitcnt lgkmcnt(4)\n" \
    "v_mfma_f32_16x16x32_bf16 v[180:183], v[156:159], a[4:7], v[180:183]\n" \
    "v_mfma_f32_16x16x32_bf16 v[188:191], v[156:159], a[12:15], v[188:191]\n" \
    "buffer_load_dwordx4 v34, %[ws], s94 offen offset:2048 sc1 lds\n" \
    "s_waitcnt lgkmcnt(3)\n" \
    "v_mfma_f32_16x16x32_bf16 v[176:179], v[160:163], a[16:19], v[176:179]\n" \
    "v_mfma_f32_16x16x32_bf16 v[184:187], v[160:163], a[24:27], v[184:187]\n" \
    "s_waitcnt lgkmcnt(2)\n" \
    "v_mfma_f32_16x16x32_bf16 v[180:183], v[164:167], a[16:19], v[180:183]\n" \
    "v_mfma_f32_16x16x32_bf16 v[188:191], v[164:167], a[24:27], v[188:191]\n" \
    "s_waitcnt lgkmcnt(1)\n" \
    "v_mfma_f32_16x16x32_bf16 v[176:179], v[168:171], a[20:23], v[176:179]\n" \
    "v_mfma_f32_16x16x32_bf16 v[184:187], v[168:171], a[28:31], v[184:187]\n" \
    "buffer_load_dwordx4 v35, %[ws], s94 offen offset:3072 sc1 lds\n" \
    "s_waitcnt lgkmcnt(0)\n" \
    "v_mfma_f32_16x16x32_bf16 v[180:183], v[172:175], a[20:23], v[180:183]\n" \
    "v_mfma_f32_16x16x32_bf16 v[188:191], v[172:175], a[28:31], v[188:191]\n" \
    "s_waitcnt vmcnt(3)\n" \
    "s_barrier\n" \
    "ds_read_b128 v[56:59], %[lr] offset:0\n" \
    "ds_read_b128 v[60:63], %[lr] offset:4096\n" \
    "ds_read_b128 v[64:67], %[lr] offset:8192\n" \
    "ds_read_b128 v[68:71], %[lr] offset:12288\n" \
    "s_waitcnt lgkmcnt(3)\n" \
    "v_smfmac_f32_16x16x64_bf16 v[120:123], v[56:59], a[0:7], %[idx]\n" \
    "v_smfmac_f32_16x16x64_bf16 v[124:127], v[56:59], a[8:15], %[idx]\n" \
    "s_waitcnt lgkmcnt(2)\n" \
    "v_smfmac_f32_16x16x64_bf16 v[120:123], v[60:63], a[64:71], %[idx]\n" \
    "s_waitcnt vmcnt(2)\n" \
    "s_barrier\n" \
    "v_smfmac_f32_16x16x64_bf16 v[124:127], v[60:63], a[72:79], %[idx]\n" \
    "ds_read_b128 v[72:75], %[lr] offset:1024\n" \
    "ds_read_b128 v[76:79], %[lr] offset:5120\n" \
    "s_waitcnt lgkmcnt(3)\n" \
    "v_smfmac_f32_16x16x64_bf16 v[120:123], v[64:67], a[128:135], %[idx]\n" \
    "ds_read_b128 v[80:83], %[lr] offset:9216\n" \
    "ds_read_b128 v[84:87], %[lr] offset:13312\n" \
    "v_smfmac_f32_16x16x64_bf16 v[124:127], v[64:67], a[136:143], %[idx]\n" \
    "s_waitcnt lgkmcnt(4)\n" \
    "v_smfmac_f32_16x16x64_bf16 v[120:123], v[68:71], a[192:199], %[idx]\n" \
    "v_smfmac_f32_16x16x64_bf16 v[124:127], v[68:71], a[200:207], %[idx]\n" \
    "s_waitcnt lgkmcnt(3)\n" \
    "v_smfmac_f32_16x16x64_bf16 v[120:123], v[72:75], a[16:23], %[idx]\n" \
    "v_smfmac_f32_16x16x64_bf16 v[124:127], v[72:75], a[24:31], %[idx]\n" \
    "s_waitcnt lgkmcnt(2)\n" \
    "v_smfmac_f32_16x16x64_bf16 v[120:123], v[76:79], a[80:87], %[idx]\n" \
    "s_waitcnt vmcnt(1)\n" \
    "s_barrier\n" \
    "v_smfmac_f32_16x16x64_bf16 v[124:127], v[76:79], a[88:95], %[idx]\n" \
    "ds_read_b128 v[88:91], %[lr] offset:2048\n" \
    "ds_read_b128 v[92:95], %[lr] offset:6144\n" \
    "s_waitcnt lgkmcnt(3)\n" \
    "v_smfmac_f32_16x16x64_bf16 v[120:123], v[80:83], a[144:151], %[idx]\n" \
    "ds_read_b128 v[96:99], %[lr] offset:10240\n" \
    "ds_read_b128 v[100:103], %[lr] offset:14336\n" \
    "v_smfmac_f32_16x16x64_bf16 v[124:127], v[80:83], a[152:159], %[idx]\n" \
    "s_waitcnt lgkmcnt(4)\n" \
    "v_smfmac_f32_16x16x64_bf16 v[120:123], v[84:87], a[208:215], %[idx]\n" \
    "v_smfmac_f32_16x16x64_bf16 v[124:127], v[84:87], a[216:223], %[idx]\n" \
    "s_waitcnt lgkmcnt(3)\n" \
    "v_smfmac_f32_16x16x64_bf16 v[120:123], v[88:91], a[32:39], %[idx]\n" \
    "v_smfmac_f32_16x16x64_bf16 v[124:127], v[88:91], a[40:47], %[idx]\n" \
    "s_waitcnt lgkmcnt(2)\n" \
    "v_smfmac_f32_16x16x64_bf16 v[120:123], v[92:95], a[96:103], %[idx]\n" \
    "s_waitcnt vmcnt(0)\n" \
    "s_barrier\n" \
    "v_smfmac_f32_16x16x64_bf16 v[124:127], v[92:95], a[104:111], %[idx]\n" \
    "ds_read_b128 v[104:107], %[lr] offset:3072\n" \
    "ds_read_b128 v[108:111], %[lr] offset:7168\n" \
    "s_waitcnt lgkmcnt(3)\n" \
    "v_smfmac_f32_16x16x64_bf16 v[120:123], v[96:99], a[160:167], %[idx]\n" \
    "ds_read_b128 v[112:115], %[lr] offset:11264\n" \
    "ds_read_b128 v[116:119], %[lr] offset:15360\n" \
    "v_smfmac_f32_16x16x64_bf16 v[124:127], v[96:99], a[168:175], %[idx]\n" \
    "s_waitcnt lgkmcnt(4)\n" \
    "v_smfmac_f32_16x16x64_bf16 v[120:123], v[100:103], a[224:231], %[idx]\n" \
    "v_smfmac_f32_16x16x64_bf16 v[124:127], v[100:103], a[232:239], %[idx]\n" \
    "s_waitcnt lgkmcnt(3)\n" \
    "v_smfmac_f32_16x16x64_bf16 v[120:123], v[104:107], a[48:55], %[idx]\n" \
    "v_smfmac_f32_16x16x64_bf16 v[124:127], v[104:107], a[56:63], %[idx]\n" \
    "s_waitcnt lgkmcnt(2)\n" \
    "v_smfmac_f32_16x16x64_bf16 v[120:123], v[108:111], a[112:119], %[idx]\n" \
    "v_smfmac_f32_16x16x64_bf16 v[124:127], v[108:111], a[120:127], %[idx]\n" \
    "s_waitcnt lgkmcnt(1)\n" \
    "v_smfmac_f32_16x16x64_bf16 v[120:123], v[112:115], a[176:183], %[idx]\n" \
    "v_smfmac_f32_16x16x64_bf16 v[124:127], v[112:115], a[184:191], %[idx]\n" \
    "s_waitcnt lgkmcnt(0)\n" \
    "v_smfmac_f32_16x16x64_bf16 v[120:123], v[116:119], a[240:247], %[idx]\n" \
    "ds_read_b128 v[144:147], v200 offset:4096\n" \
    "ds_read_b128 v[148:151], v200 offset:36864\n" \
    "v_smfmac_f32_16x16x64_bf16 v[124:127], v[116:119], a[248:255], %[idx]\n" \
    "ds_read_b128 v[152:155], v201 offset:4096\n" \
    "ds_read_b128 v[156:159], v201 offset:36864\n" \
    "ds_read_b128 v[160:163], v200 offset:6144\n" \
    "s_nop 1\n" \
    "v_add_f32 v130, v120, v122\n" \
    "v_add_f32 v131, v121, v123\n" \
    "v_cmp_u_f32 vcc, v130, v131\n" \
    "v_cvt_pk_bf16_f32 v134, v130, v131\n" \
    "v_add_f32 v132, v124, v126\n" \
    "v_add_f32 v133, v125, v127\n" \
    "v_cvt_pk_bf16_f32 v135, v132, v133\n" \
    "s_cbranch_vccnz LV_memr_%=\n" \
    "LV_memok_%=:\n" \
    "buffer_store_dwordx2 v[134:135], %[vv], %[ws], s94 offen sc1\n" \
    "s_cmp_gt_u32 s66, 63\n" \
    "s_cbranch_scc1 LV_novx_%=\n" \
    "LV_vx_%=:\n" \
    "s_waitcnt lgkmcnt(4)\n" \
    "v_mfma_f32_16x16x32_bf16 v[176:179], v[144:147], a[32:35], v[176:179]\n" \
    "ds_read_b128 v[164:167], v200 offset:38912\n" \
    "s_lshr_b32 s85, s94, 19\n" \
    "v_mfma_f32_16x16x32_bf16 v[184:187], v[144:147], a[40:43], v[184:187]\n" \
    "s_lshl_b32 s85, s85, 8\n" \
    "s_add_u32 s85, s85, 2080768\n" \
    "s_waitcnt lgkmcnt(4)\n" \
    "v_mfma_f32_16x16x32_bf16 v[180:183], v[148:151], a[32:35], v[180:183]\n" \
    "ds_read_b128 v[168:171], v201 offset:6144\n" \
    "s_add_u32 s85, s85, %[npol]\n" \
    "v_mfma_f32_16x16x32_bf16 v[188:191], v[148:151], a[40:43], v[188:191]\n" \
    "buffer_load_dwordx3 v[204:206], off, %[ws], s85 sc1\n" \
    "buffer_store_dwordx2 v[136:137], %[vv], %[ws], s99 offen sc1\n" \
    "s_waitcnt lgkmcnt(4)\n" \
    "v_mfma_f32_16x16x32_bf16 v[176:179], v[152:155], a[36:39], v[176:179]\n" \
    "ds_read_b128 v[172:175], v201 offset:38912\n" \
    "v_mov_b64 v[120:121], 0\n" \
    "v_mfma_f32_16x16x32_bf16 v[184:187], v[152:155], a[44:47], v[184:187]\n" \
    "v_mov_b64 v[122:123], 0\n" \
    "v_mov_b64 v[124:125], 0\n" \
    "s_waitcnt lgkmcnt(4)\n" \
    "v_mfma_f32_16x16x32_bf16 v[180:183], v[156:159], a[36:39], v[180:183]\n" \
    "ds_read_b128 v[144:147], v200 offset:8192\n" \
    "v_mov_b64 v[126:127], 0\n" \
    "v_mfma_f32_16x16x32_bf16 v[188:191], v[156:159], a[44:47], v[188:191]\n" \
    "s_add_u32 s94, s94, 524288\n" \
    "s_and_b32 s94, s94, 2097151\n" \
    "s_waitcnt lgkmcnt(4)\n" \
    "v_mfma_f32_16x16x32_bf16 v[176:179], v[160:163], a[48:51], v[176:179]\n" \
    "ds_read_b128 v[148:151], v200 offset:40960\n" \
    "s_add_u32 s95, s95, 1\n" \
    "v_mfma_f32_16x16x32_bf16 v[184:187], v[160:163], a[56:59], v[184:187]\n" \
    "s_xor_b32 s61, s61, 65536\n" \
    "s_add_u32 s63, s62, 1024\n" \
    "s_waitcnt lgkmcnt(4)\n" \
    "v_mfma_f32_16x16x32_bf16 v[180:183], v[164:167], a[48:51], v[180:183]\n" \
    "ds_read_b128 v[152:155], v201 offset:8192\n" \
    "s_xor_b32 m0, s61, 0\n" \
    "v_mfma_f32_16x16x32_bf16 v[188:191], v[164:167], a[56:59], v[188:191]\n" \
    "s_xor_b32 s97, s60, 0\n" \
    "buffer_load_dwordx4 %[vox], %[dx], s97 offen lds\n" \
    "s_waitcnt lgkmcnt(4)\n" \
    "v_mfma_f32_16x16x32_bf16 v[176:179], v[168:171], a[52:55], v[176:179]\n" \
    "ds_read_b128 v[156:159], v201 offset:40960\n" \
    "s_xor_b32 m0, s61, 2048\n" \
    "v_mfma_f32_16x16x32_bf16 v[184:187], v[168:171], a[60:63], v[184:187]\n" \
    "s_xor_b32 s97, s60, 128\n" \
    "buffer_load_dwordx4 %[vox], %[dx], s97 offen lds\n" \
    "s_waitcnt lgkmcnt(4)\n" \
    "v_mfma_f32_16x16x32_bf16 v[180:183], v[172:175], a[52:55], v[180:183]\n" \
    "ds_read_b128 v[160:163], v200 offset:10240\n" \
    "v_mfma_f32_16x16x32_bf16 v[188:191], v[172:175], a[60:63], v[188:191]\n" \
    "s_waitcnt lgkmcnt(4)\n" \
    "v_mfma_f32_16x16x32_bf16 v[176:179], v[144:147], a[64:67], v[176:179]\n" \
    "ds_read_b128 v[164:167], v200 offset:43008\n" \
    "s_xor_b32 m0, s61, 4096\n" \
    "v_mfma_f32_16x16x32_bf16 v[184:187], v[144:147], a[72:75], v[184:187]\n" \
    "s_xor_b32 s97, s60, 256\n" \
    "buffer_load_dwordx4 %[vox], %[dx], s97 offen lds\n" \
    "s_waitcnt lgkmcnt(4)\n" \
    "v_mfma_f32_16x16x32_bf16 v[180:183], v[148:151], a[64:67], v[180:183]\n" \
    "ds_read_b128 v[168:171], v201 offset:10240\n" \
    "v_mfma_f32_16x16x32_bf16 v[188:191], v[148:151], a[72:75], v[188:191]\n" \
    "s_waitcnt lgkmcnt(4)\n" \
    "v_mfma_f32_16x16x32_bf16 v[176:179], v[152:155], a[68:71], v[176:179]\n" \
    "ds_read_b128 v[172:175], v201 offset:43008\n" \
    "s_xor_b32 m0, s61, 6144\n" \
    "v_mfma_f32_16x16x32_bf16 v[184:187], v[152:155], a[76:79], v[184:187]\n" \
    "s_xor_b32 s97, s60, 384\n" \
    "buffer_load_dwordx4 %[vox], %[dx], s97 offen lds\n" \
    "s_waitcnt lgkmcnt(4)\n" \
    "v_mfma_f32_16x16x32_bf16 v[180:183], v[156:159], a[68:71], v[180:183]\n" \
    "ds_read_b128 v[144:147], v200 offset:12288\n" \
    "v_mfma_f32_16x16x32_bf16 v[188:191], v[156:159], a[76:79], v[188:191]\n" \
    "s_waitcnt lgkmcnt(4)\n" \
    "v_mfma_f32_16x16x32_bf16 v[176:179], v[160:163], a[80:83], v[176:179]\n" \
    "ds_read_b128 v[148:151], v200 offset:45056\n" \
    "s_xor_b32 m0, s61, 8192\n" \
    "v_mfma_f32_16x16x32_bf16 v[184:187], v[160:163], a[88:91], v[184:187]\n" \
    "s_xor_b32 s97, s60, 512\n" \
    "buffer_load_dwordx4 %[vox], %[dx], s97 offen lds\n" \
    "s_waitcnt lgkmcnt(4)\n" \
    "v_mfma_f32_16x16x32_bf16 v[180:183], v[164:167], a[80:83], v[180:183]\n" \
    "ds_read_b128 v[152:155], v201 offset:12288\n" \
    "s_xor_b32 m0, s61, 10240\n" \
    "v_mfma_f32_16x16x32_bf16 v[188:191], v[164:167], a[88:91], v[188:191]\n" \
    "s_xor_b32 s97, s60, 640\n" \
    "buffer_load_dwordx4 %[vox], %[dx], s97 offen lds\n" \
    "s_waitcnt lgkmcnt(4)\n" \
    "v_mfma_f32_16x16x32_bf16 v[176:179], v[168:171], a[84:87], v[176:179]\n" \
    "ds_read_b128 v[156:159], v201 offset:45056\n" \
    "v_mfma_f32_16x16x32_bf16 v[184:187], v[168:171], a[92:95], v[184:187]\n" \
    "s_waitcnt lgkmcnt(4)\n" \
    "v_mfma_f32_16x16x32_bf16 v[180:183], v[172:175], a[84:87], v[180:183]\n" \
    "ds_read_b128 v[160:163], v200 offset:14336\n" \
    "s_xor_b32 m0, s61, 12288\n" \
    "v_mfma_f32_16x16x32_bf16 v[188:191], v[172:175], a[92:95], v[188:191]\n" \
    "s_xor_b32 s97, s60, 768\n" \
    "buffer_load_dwordx4 %[vox], %[dx], s97 offen lds\n" \
    "s_waitcnt lgkmcnt(4)\n" \
    "v_mfma_f32_16x16x32_bf16 v[176:179], v[144:147], a[96:99], v[176:179]\n" \
    "ds_read_b128 v[164:167], v200 offset:47104\n" \
    "v_mfma_f32_16x16x32_bf16 v[184:187], v[144:147], a[104:107], v[184:187]\n" \
    "s_waitcnt lgkmcnt(4)\n" \
    "v_mfma_f32_16x16x32_bf16 v[180:183], v[148:151], a[96:99], v[180:183]\n" \
    "ds_read_b128 v[168:171], v201 offset:14336\n" \
    "s_xor_b32 m0, s61, 14336\n" \
    "v_mfma_f32_16x16x32_bf16 v[188:191], v[148:151], a[104:107], v[188:191]\n" \
    "s_xor_b32 s97, s60, 896\n" \
    "buffer_load_dwordx4 %[vox], %[dx], s97 offen lds\n" \
    "s_waitcnt lgkmcnt(4)\n" \
    "v_mfma_f32_16x16x32_bf16 v[176:179], v[152:155], a[100:103], v[176:179]\n" \
    "ds_read_b128 v[172:175], v201 offset:47104\n" \
    "v_mfma_f32_16x16x32_bf16 v[184:187], v[152:155], a[108:111], v[184:187]\n" \
    "s_waitcnt lgkmcnt(4)\n" \
    "v_mfma_f32_16x16x32_bf16 v[180:183], v[156:159], a[100:103], v[180:183]\n" \
    "ds_read_b128 v[144:147], v200 offset:16384\n" \
    "s_xor_b32 m0, s61, 16384\n" \
    "v_mfma_f32_16x16x32_bf16 v[188:191], v[156:159], a[108:111], v[188:191]\n" \
    "s_xor_b32 s97, s60, 1024\n" \
    "buffer_load_dwordx4 %[vox], %[dx], s97 offen lds\n" \
    "s_waitcnt lgkmcnt(4)\n" \
    "v_mfma_f32_16x16x32_bf16 v[176:179], v[160:163], a[112:115], v[176:179]\n" \
    "ds_read_b128 v[148:151], v200 offset:49152\n" \
    "s_xor_b32 m0, s61, 18432\n" \
    "v_mfma_f32_16x16x32_bf16 v[184:187], v[160:163], a[120:123], v[184:187]\n" \
    "s_xor_b32 s97, s60, 1152\n" \
    "buffer_load_dwordx4 %[vox], %[dx], s97 offen lds\n" \
    "s_waitcnt lgkmcnt(4)\n" \
    "v_mfma_f32_16x16x32_bf16 v[180:183], v[164:167], a[112:115], v[180:183]\n" \
    "ds_read_b128 v[152:155], v201 offset:16384\n" \
    "v_mfma_f32_16x16x32_bf16 v[188:191], v[164:167], a[120:123], v[188:191]\n" \
    "s_waitcnt lgkmcnt(4)\n" \
    "v_mfma_f32_16x16x32_bf16 v[176:179], v[168:171], a[116:119], v[176:179]\n" \
    "ds_read_b128 v[156:159], v201 offset:49152\n" \
    "s_xor_b32 m0, s61, 20480\n" \
    "v_mfma_f32_16x16x32_bf16 v[184:187], v[168:171], a[124:127], v[184:187]\n" \
    "s_xor_b32 s97, s60, 1280\n" \
    "buffer_load_dwordx4 %[vox], %[dx], s97 offen lds\n" \
    "s_waitcnt lgkmcnt(4)\n" \
    "v_mfma_f32_16x16x32_bf16 v[180:183], v[172:175], a[116:119], v[180:183]\n" \
    "ds_read_b128 v[160:163], v200 offset:18432\n" \
    "v_mfma_f32_16x16x32_bf16 v[188:191], v[172:175], a[124:127], v[188:191]\n" \
    "s_waitcnt lgkmcnt(4)\n" \
    "v_mfma_f32_16x16x32_bf16 v[176:179], v[144:147], a[128:131], v[176:179]\n" \
    "ds_read_b128 v[164:167], v200 offset:51200\n" \
    "s_xor_b32 m0, s61, 22528\n" \
    "v_mfma_f32_16x16x32_bf16 v[184:187], v[144:147], a[136:139], v[184:187]\n" \
    "s_xor_b32 s97, s60, 1408\n" \
    "buffer_load_dwordx4 %[vox], %[dx], s97 offen lds\n" \
    "s_waitcnt lgkmcnt(4)\n" \
    "v_mfma_f32_16x16x32_bf16 v[180:183], v[148:151], a[128:131], v[180:183]\n" \
    "ds_read_b128 v[168:171], v201 offset:18432\n" \
    "v_mfma_f32_16x16x32_bf16 v[188:191], v[148:151], a[136:139], v[188:191]\n" \
    "s_waitcnt lgkmcnt(4)\n" \
    "v_mfma_f32_16x16x32_bf16 v[176:179], v[152:155], a[132:135], v[176:179]\n" \
    "ds_read_b128 v[172:175], v201 offset:51200\n" \
    "s_xor_b32 m0, s61, 24576\n" \
    "v_mfma_f32_16x16x32_bf16 v[184:187], v[152:155], a[140:143], v[184:187]\n" \
    "s_xor_b32 s97, s60, 1536\n" \
    "buffer_load_dwordx4 %[vox], %[dx], s97 offen lds\n" \
    "s_waitcnt lgkmcnt(4)\n" \
    "v_mfma_f32_16x16x32_bf16 v[180:183], v[156:159], a[132:135], v[180:183]\n" \
    "ds_read_b128 v[144:147], v200 offset:20480\n" \
    "s_xor_b32 m0, s61, 26624\n" \
    "v_mfma_f32_16x16x32_bf16 v[188:191], v[156:159], a[140:143], v[188:191]\n" \
    "s_xor_b32 s97, s60, 1664\n" \
    "buffer_load_dwordx4 %[vox], %[dx], s97 offen lds\n" \
    "s_waitcnt lgkmcnt(4)\n" \
    "v_mfma_f32_16x16x32_bf16 v[176:179], v[160:163], a[144:147], v[176:179]\n" \
    "ds_read_b128 v[148:151], v200 offset:53248\n" \
    "v_mfma_f32_16x16x32_bf16 v[184:187], v[160:163], a[152:155], v[184:187]\n" \
    "s_waitcnt lgkmcnt(4)\n" \
    "v_mfma_f32_16x16x32_bf16 v[180:183], v[164:167], a[144:147], v[180:183]\n" \
    "ds_read_b128 v[152:155], v201 offset:20480\n" \
    "s_xor_b32 m0, s61, 28672\n" \
    "v_mfma_f32_16x16x32_bf16 v[188:191], v[164:167], a[152:155], v[188:191]\n" \
    "s_xor_b32 s97, s60, 1792\n" \
    "buffer_load_dwordx4 %[vox], %[dx], s97 offen lds\n" \
    "s_waitcnt lgkmcnt(4)\n" \
    "v_mfma_f32_16x16x32_bf16 v[176:179], v[168:171], a[148:151], v[176:179]\n" \
    "ds_read_b128 v[156:159], v201 offset:53248\n" \
    "v_mfma_f32_16x16x32_bf16 v[184:187], v[168:171], a[156:159], v[184:187]\n" \
    "s_waitcnt lgkmcnt(4)\n" \
    "v_mfma_f32_16x16x32_bf16 v[180:183], v[172:175], a[148:151], v[180:183]\n" \
    "ds_read_b128 v[160:163], v200 offset:22528\n" \
    "s_xor_b32 m0, s61, 30720\n" \
    "v_mfma_f32_16x16x32_bf16 v[188:191], v[172:175], a[156:159], v[188:191]\n" \
    "s_xor_b32 s97, s60, 1920\n" \
    "buffer_load_dwordx4 %[vox], %[dx], s97 offen lds\n" \
    "s_waitcnt lgkmcnt(4)\n" \
    "v_mfma_f32_16x16x32_bf16 v[176:179], v[144:147], a[160:163], v[176:179]\n" \
    "ds_read_b128 v[164:167], v200 offset:55296\n" \
    "v_mfma_f32_16x16x32_bf16 v[184:187], v[144:147], a[168:171], v[184:187]\n" \
    "s_waitcnt lgkmcnt(4)\n" \
    "v_mfma_f32_16x16x32_bf16 v[180:183], v[148:151], a[160:163], v[180:183]\n" \
    "ds_read_b128 v[168:171], v201 offset:22528\n" \
    "v_mfma_f32_16x16x32_bf16 v[188:191], v[148:151], a[168:171], v[188:191]\n" \
    "s_waitcnt lgkmcnt(4)\n" \
    "v_mfma_f32_16x16x32_bf16 v[176:179], v[152:155], a[164:167], v[176:179]\n" \
    "ds_read_b128 v[172:175], v201 offset:55296\n" \
    "v_mfma_f32_16x16x32_bf16 v[184:187], v[152:155], a[172:175], v[184:187]\n" \
    "s_waitcnt lgkmcnt(4)\n" \
    "v_mfma_f32_16x16x32_bf16 v[180:183], v[156:159], a[164:167], v[180:183]\n" \
    "ds_read_b128 v[144:147], v200 offset:24576\n" \
    "v_mfma_f32_16x16x32_bf16 v[188:191], v[156:159], a[172:175], v[188:191]\n" \
    "s_waitcnt lgkmcnt(4)\n" \
    "v_mfma_f32_16x16x32_bf16 v[176:179], v[160:163], a[176:179], v[176:179]\n" \
    "ds_read_b128 v[148:151], v200 offset:57344\n" \
    "v_mfma_f32_16x16x32_bf16 v[184:187], v[160:163], a[184:187], v[184:187]\n" \
    "s_waitcnt lgkmcnt(4)\n" \
    "v_mfma_f32_16x16x32_bf16 v[180:183], v[164:167], a[176:179], v[180:183]\n" \
    "ds_read_b128 v[152:155], v201 offset:24576\n" \
    "v_mfma_f32_16x16x32_bf16 v[188:191], v[164:167], a[184:187], v[188:191]\n" \
    "s_waitcnt lgkmcnt(4)\n" \
    "v_mfma_f32_16x16x32_bf16 v[176:179], v[168:171], a[180:183], v[176:179]\n" \
    "ds_read_b128 v[156:159], v201 offset:57344\n" \
    "v_mfma_f32_16x16x32_bf16 v[184:187], v[168:171], a[188:191], v[184:187]\n" \
    "s_waitcnt lgkmcnt(4)\n" \
    "v_mfma_f32_16x16x32_bf16 v[180:183], v[172:175], a[180:183], v[180:183]\n" \
    "ds_read_b128 v[160:163], v200 offset:26624\n" \
    "v_mfma_f32_16x16x32_bf16 v[188:191], v[172:175], a[188:191], v[188:191]\n" \
    "s_waitcnt lgkmcnt(4)\n" \
    "v_mfma_f32_16x16x32_bf16 v[176:179], v[144:147], a[192:195], v[176:179]\n" \
    "ds_read_b128 v[164:167], v200 offset:59392\n" \
    "v_mfma_f32_16x16x32_bf16 v[184:187], v[144:147], a[200:203], v[184:187]\n" \
    "s_waitcnt lgkmcnt(4)\n" \
    "v_mfma_f32_16x16x32_bf16 v[180:183], v[148:151], a[192:195], v[180:183]\n" \
    "ds_read_b128 v[168:171], v201 offset:26624\n" \
    "v_mfma_f32_16x16x32_bf16 v[188:191], v[148:151], a[200:203], v[188:191]\n" \
    "s_waitcnt lgkmcnt(4)\n" \
    "v_mfma_f32_16x16x32_bf16 v[176:179], v[152:155], a[196:199], v[176:179]\n" \
    "ds_read_b128 v[172:175], v201 offset:59392\n" \
    "v_mfma_f32_16x16x32_bf16 v[184:187], v[152:155], a[204:207], v[184:187]\n" \
    "s_waitcnt lgkmcnt(4)\n" \
    "v_mfma_f32_16x16x32_bf16 v[180:183], v[156:159], a[196:199], v[180:183]\n" \
    "ds_read_b128 v[144:147], v200 offset:28672\n" \
    "v_mfma_f32_16x16x32_bf16 v[188:191], v[156:159], a[204:207], v[188:191]\n" \
    "s_waitcnt lgkmcnt(4)\n" \
    "v_mfma_f32_16x16x32_bf16 v[176:179], v[160:163], a[208:211], v[176:179]\n" \
    "ds_read_b128 v[148:151], v200 offset:61440\n" \
    "v_mfma_f32_16x16x32_bf16 v[184:187], v[160:163], a[216:219], v[184:187]\n" \
    "s_waitcnt lgkmcnt(4)\n" \
    "v_mfma_f32_16x16x32_bf16 v[180:183], v[164:167], a[208:211], v[180:183]\n" \
    "ds_read_b128 v[152:155], v201 offset:28672\n" \
    "v_mfma_f32_16x16x32_bf16 v[188:191], v[164:167], a[216:219], v[188:191]\n" \
    "s_waitcnt lgkmcnt(4)\n" \
    "v_mfma_f32_16x16x32_bf16 v[176:179], v[168:171], a[212:215], v[176:179]\n" \
    "ds_read_b128 v[156:159], v201 offset:61440\n" \
    "v_mfma_f32_16x16x32_bf16 v[184:187], v[168:171], a[220:223], v[184:187]\n" \
    "s_waitcnt lgkmcnt(4)\n" \
    "v_mfma_f32_16x16x32_bf16 v[180:183], v[172:175], a[212:215], v[180:183]\n" \
    "ds_read_b128 v[160:163], v200 offset:30720\n" \
    "v_mfma_f32_16x16x32_bf16 v[188:191], v[172:175], a[220:223], v[188:191]\n" \
    "s_waitcnt lgkmcnt(4)\n" \
    "v_mfma_f32_16x16x32_bf16 v[176:179], v[144:147], a[224:227], v[176:179]\n" \
    "ds_read_b128 v[164:167], v200 offset:63488\n" \
    "v_mfma_f32_16x16x32_bf16 v[184:187], v[144:147], a[232:235], v[184:187]\n" \
    "s_waitcnt lgkmcnt(4)\n" \
    "v_mfma_f32_16x16x32_bf16 v[180:183], v[148:151], a[224:227], v[180:183]\n" \
    "ds_read_b128 v[168:171], v201 offset:30720\n" \
    "v_mfma_f32_16x16x32_bf16 v[188:191], v[148:151], a[232:235], v[188:191]\n" \
    "s_waitcnt lgkmcnt(4)\n" \
    "v_mfma_f32_16x16x32_bf16 v[176:179], v[152:155], a[228:231], v[176:179]\n" \
    "ds_read_b128 v[172:175], v201 offset:63488\n" \
    "v_mfma_f32_16x16x32_bf16 v[184:187], v[152:155], a[236:239], v[184:187]\n" \
    "s_waitcnt lgkmcnt(4)\n" \
    "v_mfma_f32_16x16x32_bf16 v[180:183], v[156:159], a[228:231], v[180:183]\n" \
    "v_mfma_f32_16x16x32_bf16 v[188:191], v[156:159], a[236:239], v[188:191]\n" \
    "s_waitcnt lgkmcnt(3)\n" \
    "v_mfma_f32_16x16x32_bf16 v[176:179], v[160:163], a[240:243], v[176:179]\n" \
    "s_mov_b32 m0, s57\n" \
    "v_mfma_f32_16x16x32_bf16 v[184:187], v[160:163], a[248:251], v[184:187]\n" \
    "s_add_u32 s60, s60, 65536\n" \
    "s_and_b32 s60, s60, 0x3fffff\n" \
    "s_waitcnt lgkmcnt(2)\n" \
    "v_mfma_f32_16x16x32_bf16 v[180:183], v[164:167], a[240:243], v[180:183]\n" \
    "v_xor_b32 v200, 0x10000, v200\n" \
    "v_mfma_f32_16x16x32_bf16 v[188:191], v[164:167], a[248:251], v[188:191]\n" \
    "v_xor_b32 v201, 0x10000, v201\n" \
    "s_add_u32 s66, s66, 1\n" \
    "s_waitcnt lgkmcnt(1)\n" \
    "v_mfma_f32_16x16x32_bf16 v[176:179], v[168:171], a[244:247], v[176:179]\n" \
    "v_mfma_f32_16x16x32_bf16 v[184:187], v[168:171], a[252:255], v[184:187]\n" \
    "s_waitcnt lgkmcnt(0)\n" \
    "v_mfma_f32_16x16x32_bf16 v[180:183], v[172:175], a[244:247], v[180:183]\n" \
    "v_mfma_f32_16x16x32_bf16 v[188:191], v[172:175], a[252:255], v[188:191]\n" \
    "v_cvt_pk_bf16_f32 v192, v176, v177\n" \
    "v_cvt_pk_bf16_f32 v193, v178, v179\n" \
    "v_cvt_pk_bf16_f32 v196, v184, v185\n" \
    "v_cvt_pk_bf16_f32 v197, v186, v187\n" \
    "s_nop 0\n" \
    "v_cvt_pk_bf16_f32 v194, v180, v181\n" \
    "v_cvt_pk_bf16_f32 v195, v182, v183\n" \
    "buffer_store_dwordx4 v[192:195], %[voa], %[ws], s62 offen sc1\n" \
    "s_cmp_le_u32 s66, 2\n" \
    "v_cvt_pk_bf16_f32 v198, v188, v189\n" \
    "v_cvt_pk_bf16_f32 v199, v190, v191\n" \
    "buffer_store_dwordx4 v[196:199], %[voa], %[ws], s63 offen sc1\n" \
    "s_cbranch_scc1 LV_boot_%=\n" \
    "s_add_u32 s62, s62, 0x200000\n" \
    "s_xor_b32 s62, s62, 8192\n" \
    "s_waitcnt vmcnt(2)\n" \
    "LV_msg_%=:\n" \
    "v_readfirstlane_b32 s86, v204\n" \
    "v_readfirstlane_b32 s87, v205\n" \
    "v_readfirstlane_b32 s96, v206\n" \
    "s_nop 1\n" \
    "s_max_u32 s97, s86, s87\n" \
    "s_max_u32 s97, s97, s96\n" \
    "s_cmp_eq_u32 s97, -1\n" \
    "s_cbranch_scc1 LV_msgr_%=\n" \
    "s_add_u32 s87, s87, " STR(OFF_V) "\n" \
    "s_branch LV_top_%=\n" \
    "LV_done_%=:\n" \
    "s_branch LV_end_%=\n" \
    "LV_hsr_%=:\n" \
    "s_sleep 2\n" \
    "s_add_u32 s98, s98, 1\n" \
    "s_cmp_gt_u32 s98, 131072\n" \
    "s_cbranch_scc1 LV_fail_%=\n" \
    "s_branch LV_hs_%=\n" \
    "LV_redo_%=:\n" \
    "s_waitcnt vmcnt(0) lgkmcnt(0)\n" \
    "s_barrier\n" \
    "s_or_b32 %[cfg], s97, 256\n" \
    "s_branch LV_end_%=\n" \
    "LV_tmc_%=:\n" \
    "s_cmp_lt_i32 s97, " STR(T_LEADA) "\n" \
    "s_cbranch_scc1 LV_rs_%=\n" \
    "s_cmp_gt_u32 s97, 4000\n" \
    "s_cbranch_scc1 LV_tmx_%=\n" \
    "LV_tmd_%=:\n" \
    "s_memrealtime s[92:93]\n" \
    "s_waitcnt lgkmcnt(0)\n" \
    "s_lshl_b32 s97, s92, 3\n" \
    "s_sub_u32 s97, s87, s97\n" \
    "s_cmp_gt_i32 s97, 96\n" \
    "s_cbranch_scc1 LV_tmd_%=\n" \
    "s_branch LV_tm_%=\n" \
    "LV_t2_%=:\n" \
    "s_memrealtime s[72:73]\n" \
    "s_nop 1\n" \
    "s_memrealtime s[74:75]\n" \
    "s_nop 1\n" \
    "s_memrealtime s[76:77]\n" \
    "s_nop 1\n" \
    "s_memrealtime s[78:79]\n" \
    "s_nop 1\n" \
    "s_memrealtime s[80:81]\n" \
    "s_nop 1\n" \
    "s_memrealtime s[82:83]\n" \
    "s_nop 1\n" \
    "s_memrealtime s[92:93]\n" \
    "s_nop 1\n" \
    "s_memrealtime s[70:71]\n" \
    "s_getpc_b64 s[68:69]\n" \
    "LV_t2p_%=:\n" \
    "s_add_u32 s68, s68, LV_t2x_%= - LV_t2p_%=\n" \
    "s_addc_u32 s69, s69, 0\n" \
    "s_waitcnt lgkmcnt(0)\n" \
    "s_sub_u32 s97, s87, s72\n" \
    "s_sub_u32 s97, s97, s74\n" \
    "s_sub_u32 s97, s97, s76\n" \
    "s_sub_u32 s97, s97, s78\n" \
    "s_sub_u32 s97, s97, s80\n" \
    "s_sub_u32 s97, s97, s82\n" \
    "s_sub_u32 s97, s97, s92\n" \
    "s_sub_u32 s97, s97, s70\n" \
    "s_cmp_gt_u32 s97, 160\n" \
    "s_cbranch_scc1 LV_t2c_%=\n" \
    "s_mul_i32 s97, s97, 47\n" \
    "s_lshr_b32 s97, s97, 4\n" \
    "s_and_b32 s97, s97, -4\n" \
    "s_sub_u32 s68, s68, s97\n" \
    "s_subb_u32 s69, s69, 0\n" \
    "s_setpc_b64 s[68:69]\n" \
    ".rept 118\n" \
    "s_nop 0\n" \
    ".endr\n" \
    "LV_t2x_%=:\n" \
    "s_branch LV_tmx_%=\n" \
    "LV_t2c_%=:\n" \
    "s_cmp_gt_u32 s97, 4000\n" \
    "s_cbranch_scc1 LV_t2x_%=\n" \
    "LV_t2d_%=:\n" \
    "s_memrealtime s[92:93]\n" \
    "s_waitcnt lgkmcnt(0)\n" \
    "s_lshl_b32 s97, s92, 3\n" \
    "s_sub_u32 s97, s87, s97\n" \
    "s_cmp_gt_i32 s97, 96\n" \
    "s_cbranch_scc1 LV_t2d_%=\n" \
    "s_branch LV_t2_%=\n" \
    "LV_memr_%=:\n" \
    "v_max3_u32 v140, v56, v57, v57\n" \
    "v_max3_u32 v140, v140, v58, v59\n" \
    "v_max3_u32 v140, v140, v60, v61\n" \
    "v_max3_u32 v140, v140, v62, v63\n" \
    "v_max3_u32 v140, v140, v64, v65\n" \
    "v_max3_u32 v140, v140, v66, v67\n" \
    "v_max3_u32 v140, v140, v68, v69\n" \
    "v_max3_u32 v140, v140, v70, v71\n" \
    "v_max3_u32 v140, v140, v72, v73\n" \
    "v_max3_u32 v140, v140, v74, v75\n" \
    "v_max3_u32 v140, v140, v76, v77\n" \
    "v_max3_u32 v140, v140, v78, v79\n" \
    "v_max3_u32 v140, v140, v80, v81\n" \
    "v_max3_u32 v140, v140, v82, v83\n" \
    "v_max3_u32 v140, v140, v84, v85\n" \
    "v_max3_u32 v140, v140, v86, v87\n" \
    "v_max3_u32 v140, v140, v88, v89\n" \
    "v_max3_u32 v140, v140, v90, v91\n" \
    "v_max3_u32 v140, v140, v92, v93\n" \
    "v_max3_u32 v140, v140, v94, v95\n" \
    "v_max3_u32 v140, v140, v96, v97\n" \
    "v_max3_u32 v140, v140, v98, v99\n" \
    "v_max3_u32 v140, v140, v100, v101\n" \
    "v_max3_u32 v140, v140, v102, v103\n" \
    "v_max3_u32 v140, v140, v104, v105\n" \
    "v_max3_u32 v140, v140, v106, v107\n" \
    "v_max3_u32 v140, v140, v108, v109\n" \
    "v_max3_u32 v140, v140, v110, v111\n" \
    "v_max3_u32 v140, v140, v112, v113\n" \
    "v_max3_u32 v140, v140, v114, v115\n" \
    "v_max3_u32 v140, v140, v116, v117\n" \
    "v_max3_u32 v140, v140, v118, v119\n" \
    "v_cmp_eq_u32 vcc, -1, v140\n" \
    "s_cbranch_vccz LV_memok_%=\n" \
    "s_waitcnt vmcnt(0) lgkmcnt(0)\n" \
    "s_mov_b32 m0, s57\n" \
    "v_mov_b64 v[120:121], 0\n" \
    "v_mov_b64 v[122:123], 0\n" \
    "v_mov_b64 v[124:125], 0\n" \
    "v_mov_b64 v[126:127], 0\n" \
    "s_add_u32 s98, s98, 1\n" \
    "s_cmp_gt_u32 s98, 131072\n" \
    "s_cbranch_scc1 LV_fail_%=\n" \
    "s_cmp_lg_u32 %[w], 0\n" \
    "s_cbranch_scc1 LV_tmx_%=\n" \
    "LV_rs_%=:\n" \
    "s_add_u32 s98, s98, 1\n" \
    "s_cmp_gt_u32 s98, 131072\n" \
    "s_cbranch_scc1 LV_fail_%=\n" \
    "s_lshr_b32 s85, s94, 19\n" \
    "s_lshl_b32 s85, s85, 8\n" \
    "s_add_u32 s85, s85, 2080768\n" \
    "s_add_u32 s85, s85, %[npol]\n" \
    "buffer_load_dwordx3 v[140:142], off, %[ws], s85 sc1\n" \
    "s_waitcnt vmcnt(0)\n" \
    "v_readfirstlane_b32 s86, v140\n" \
    "v_readfirstlane_b32 s87, v141\n" \
    "v_readfirstlane_b32 s96, v142\n" \
    "s_nop 1\n" \
    "s_max_u32 s97, s86, s87\n" \
    "s_max_u32 s97, s97, s96\n" \
    "s_cmp_eq_u32 s97, -1\n" \
    "s_cbranch_scc1 LV_rs_%=\n" \
    "s_add_u32 s87, s96, " STR(OFF_V) "\n" \
    "s_branch LV_t2_%=\n" \
    "LV_msgr_%=:\n" \
    "buffer_load_dwordx3 v[204:206], off, %[ws], s85 sc1\n" \
    "s_waitcnt vmcnt(0)\n" \
    "s_add_u32 s98, s98, 1\n" \
    "s_cmp_gt_u32 s98, 131072\n" \
    "s_cbranch_scc1 LV_fail_%=\n" \
    "s_branch LV_msg_%=\n" \
    "LV_novx_%=:\n" \
    "s_cmp_eq_u32 s95, 63\n" \
    "s_cbranch_scc1 LV_done_%=\n" \
    "s_lshr_b32 s85, s94, 19\n" \
    "s_lshl_b32 s85, s85, 8\n" \
    "s_add_u32 s85, s85, 2080768\n" \
    "s_add_u32 s85, s85, %[npol]\n" \
    "buffer_load_dwordx3 v[204:206], off, %[ws], s85 sc1\n" \
    "buffer_store_dwordx2 v[136:137], %[vv], %[ws], s99 offen sc1\n" \
    "v_mov_b64 v[120:121], 0\n" \
    "v_mov_b64 v[122:123], 0\n" \
    "v_mov_b64 v[124:125], 0\n" \
    "v_mov_b64 v[126:127], 0\n" \
    "s_add_u32 s94, s94, 524288\n" \
    "s_and_b32 s94, s94, 2097151\n" \
    "s_add_u32 s95, s95, 1\n" \
    "s_waitcnt vmcnt(0) lgkmcnt(0)\n" \
    "s_branch LV_msg_%=\n" \
    "LV_boot_%=:\n" \
    "s_add_u32 s62, s62, 0x200000\n" \
    "s_xor_b32 s62, s62, 8192\n" \
    "s_mov_b32 s94, %[pol]\n" \
    "s_mov_b32 s95, 0\n" \
    "s_cmp_eq_u32 s66, 2\n" \
    "s_cbranch_scc1 LV_go_%=\n" \
    "s_waitcnt vmcnt(0)\n" \
    "s_barrier\n" \
    "v_mov_b32 v130, 63\n" \
    "buffer_store_dword v130, %[vfl], %[ws], %[npol] offen sc1\n" \
    "LV_boot0_%=:\n" \
    "ds_read_b128 v[144:147], v200 offset:0\n" \
    "ds_read_b128 v[148:151], v200 offset:32768\n" \
    "ds_read_b128 v[152:155], v201 offset:0\n" \
    "ds_read_b128 v[156:159], v201 offset:32768\n" \
    "ds_read_b128 v[160:163], v200 offset:2048\n" \
    "s_waitcnt lgkmcnt(4)\n" \
    "v_mfma_f32_16x16x32_bf16 v[176:179], v[144:147], a[0:3], 0\n" \
    "ds_read_b128 v[164:167], v200 offset:34816\n" \
    "v_mfma_f32_16x16x32_bf16 v[184:187], v[144:147], a[8:11], 0\n" \
    "s_waitcnt lgkmcnt(4)\n" \
    "v_mfma_f32_16x16x32_bf16 v[180:183], v[148:151], a[0:3], 0\n" \
    "ds_read_b128 v[168:171], v201 offset:2048\n" \
    "v_mfma_f32_16x16x32_bf16 v[188:191], v[148:151], a[8:11], 0\n" \
    "s_waitcnt lgkmcnt(4)\n" \
    "v_mfma_f32_16x16x32_bf16 v[176:179], v[152:155], a[4:7], v[176:179]\n" \
    "ds_read_b128 v[172:175], v201 offset:34816\n" \
    "v_mfma_f32_16x16x32_bf16 v[184:187], v[152:155], a[12:15], v[184:187]\n" \
    "s_waitcnt lgkmcnt(4)\n" \
    "v_mfma_f32_16x16x32_bf16 v[180:183], v[156:159], a[4:7], v[180:183]\n" \
    "v_mfma_f32_16x16x32_bf16 v[188:191], v[156:159], a[12:15], v[188:191]\n" \
    "s_waitcnt lgkmcnt(3)\n" \
    "v_mfma_f32_16x16x32_bf16 v[176:179], v[160:163], a[16:19], v[176:179]\n" \
    "v_mfma_f32_16x16x32_bf16 v[184:187], v[160:163], a[24:27], v[184:187]\n" \
    "s_waitcnt lgkmcnt(2)\n" \
    "v_mfma_f32_16x16x32_bf16 v[180:183], v[164:167], a[16:19], v[180:183]\n" \
    "v_mfma_f32_16x16x32_bf16 v[188:191], v[164:167], a[24:27], v[188:191]\n" \
    "s_waitcnt lgkmcnt(1)\n" \
    "v_mfma_f32_16x16x32_bf16 v[176:179], v[168:171], a[20:23], v[176:179]\n" \
    "v_mfma_f32_16x16x32_bf16 v[184:187], v[168:171], a[28:31], v[184:187]\n" \
    "s_waitcnt lgkmcnt(0)\n" \
    "v_mfma_f32_16x16x32_bf16 v[180:183], v[172:175], a[20:23], v[180:183]\n" \
    "v_mfma_f32_16x16x32_bf16 v[188:191], v[172:175], a[28:31], v[188:191]\n" \
    "ds_read_b128 v[144:147], v200 offset:4096\n" \
    "ds_read_b128 v[148:151], v200 offset:36864\n" \
    "ds_read_b128 v[152:155], v201 offset:4096\n" \
    "ds_read_b128 v[156:159], v201 offset:36864\n" \
    "ds_read_b128 v[160:163], v200 offset:6144\n" \
    "s_branch LV_vx_%=\n" \
    "LV_go_%=:\n" \
    "s_waitcnt vmcnt(0)\n" \
    "s_barrier\n" \
    "s_add_u32 s97, %[rdy], %[npol]\n" \
    "v_mov_b32 v31, 0\n" \
    "s_cmp_lg_u32 %[w], 0\n" \
    "s_cbranch_scc1 LV_en_%=\n" \
    "buffer_store_dword v31, off, %[ws], s97 sc1\n" \
    "s_branch LV_rs_%=\n" \
    "LV_en_%=:\n" \
    "s_branch LV_tmx_%=\n" \
    "LV_fail_%=:\n" \
    "v_mov_b32 v130, 2043904\n" \
    "v_mov_b32 v131, 1\n" \
    "buffer_store_dword v131, v130, %[ws], 0 offen sc1\n" \
    "s_endpgm\n" \
    "LV_end_%=:\n"
#define ASM_ROLE_B \
    "s_nop 4\n" \
    "s_mov_b32 s98, 0\n" \
    "s_mov_b32 s95, 0\n" \
    "s_add_u32 s97, %[hsa], 32768\n" \
    "buffer_load_dword v31, off, %[ws], s97 sc1\n" \
    "s_lshl_b32 s62, %[w], 21\n" \
    "s_lshl_b32 s63, %[w], 15\n" \
    "v_mov_b32 v32, %[vm0]\n" \
    "v_mov_b32 v33, %[vm1]\n" \
    "v_xor_b32 v34, 32768, %[vmo]\n" \
    "s_add_u32 s57, %[w], 1\n" \
    "s_and_b32 s57, s57, 2\n" \
    "s_xor_b32 s97, %[xr], 0\n" \
    "s_add_u32 s97, %[w], s97\n" \
    "s_and_b32 s97, s97, 3\n" \
    "s_lshl_b32 s97, s97, 10\n" \
    "v_add_u32 v36, s97, %[vo]\n" \
    "s_xor_b32 s97, %[xr], 1\n" \
    "s_add_u32 s97, %[w], s97\n" \
    "s_and_b32 s97, s97, 3\n" \
    "s_lshl_b32 s97, s97, 10\n" \
    "v_add_u32 v37, s97, %[vo]\n" \
    "s_xor_b32 s97, %[xr], 2\n" \
    "s_add_u32 s97, %[w], s97\n" \
    "s_and_b32 s97, s97, 3\n" \
    "s_lshl_b32 s97, s97, 10\n" \
    "v_add_u32 v38, s97, %[vo]\n" \
    "s_xor_b32 s97, %[xr], 3\n" \
    "s_add_u32 s97, %[w], s97\n" \
    "s_and_b32 s97, s97, 3\n" \
    "s_lshl_b32 s97, s97, 10\n" \
    "v_add_u32 v39, s97, %[vo]\n" \
    "s_lshl_b32 s61, %[w], 20\n" \
    "s_lshl_b32 s60, %[xr], 18\n" \
    "s_and_b32 s66, %[xr], 4\n" \
    "s_lshl_b32 s66, s66, 10\n" \
    "v_mov_b32 v40, %[rk]\n" \
    "v_xor_b32 v41, 64, %[rk]\n" \
    "v_add_u32 v42, 0x10000, v40\n" \
    "v_add_u32 v43, 0x10000, v41\n" \
    "v_xor_b32 v46, 128, %[vok]\n" \
    "v_xor_b32 v44, 0, %[rw]\n" \
    "v_xor_b32 v45, 32, %[rw]\n" \
    "global_load_dword v72, %[vse], %[m0p]\n" \
    "s_add_u32 m0, s63, 0\n" \
    "s_add_u32 s97, s62, 0\n" \
    "s_xor_b32 s97, s97, s60\n" \
    "buffer_load_dwordx4 %[vow], %[dw], s97 offen lds\n" \
    "s_add_u32 m0, s63, 1024\n" \
    "s_add_u32 s97, s62, 4096\n" \
    "s_xor_b32 s97, s97, s60\n" \
    "buffer_load_dwordx4 %[vow], %[dw], s97 offen lds\n" \
    "s_add_u32 m0, s63, 2048\n" \
    "s_add_u32 s97, s62, 131072\n" \
    "s_xor_b32 s97, s97, s60\n" \
    "buffer_load_dwordx4 %[vow], %[dw], s97 offen lds\n" \
    "s_add_u32 m0, s63, 3072\n" \
    "s_add_u32 s97, s62, 135168\n" \
    "s_xor_b32 s97, s97, s60\n" \
    "buffer_load_dwordx4 %[vow], %[dw], s97 offen lds\n" \
    "s_add_u32 m0, s63, 4096\n" \
    "s_add_u32 s97, s62, 16384\n" \
    "s_xor_b32 s97, s97, s60\n" \
    "buffer_load_dwordx4 %[vow], %[dw], s97 offen lds\n" \
    "s_add_u32 m0, s63, 5120\n" \
    "s_add_u32 s97, s62, 20480\n" \
    "s_xor_b32 s97, s97, s60\n" \
    "buffer_load_dwordx4 %[vow], %[dw], s97 offen lds\n" \
    "s_add_u32 m0, s63, 6144\n" \
    "s_add_u32 s97, s62, 147456\n" \
    "s_xor_b32 s97, s97, s60\n" \
    "buffer_load_dwordx4 %[vow], %[dw], s97 offen lds\n" \
    "s_add_u32 m0, s63, 7168\n" \
    "s_add_u32 s97, s62, 151552\n" \
    "s_xor_b32 s97, s97, s60\n" \
    "buffer_load_dwordx4 %[vow], %[dw], s97 offen lds\n" \
    "s_add_u32 m0, s63, 8192\n" \
    "s_add_u32 s97, s62, 262144\n" \
    "s_xor_b32 s97, s97, s60\n" \
    "buffer_load_dwordx4 %[vow], %[dw], s97 offen lds\n" \
    "s_add_u32 m0, s63, 9216\n" \
    "s_add_u32 s97, s62, 266240\n" \
    "s_xor_b32 s97, s97, s60\n" \
    "buffer_load_dwordx4 %[vow], %[dw], s97 offen lds\n" \
    "s_add_u32 m0, s63, 10240\n" \
    "s_add_u32 s97, s62, 393216\n" \
    "s_xor_b32 s97, s97, s60\n" \
    "buffer_load_dwordx4 %[vow], %[dw], s97 offen lds\n" \
    "s_add_u32 m0, s63, 11264\n" \
    "s_add_u32 s97, s62, 397312\n" \
    "s_xor_b32 s97, s97, s60\n" \
    "buffer_load_dwordx4 %[vow], %[dw], s97 offen lds\n" \
    "s_add_u32 m0, s63, 12288\n" \
    "s_add_u32 s97, s62, 278528\n" \
    "s_xor_b32 s97, s97, s60\n" \
    "buffer_load_dwordx4 %[vow], %[dw], s97 offen lds\n" \
    "s_add_u32 m0, s63, 13312\n" \
    "s_add_u32 s97, s62, 282624\n" \
    "s_xor_b32 s97, s97, s60\n" \
    "buffer_load_dwordx4 %[vow], %[dw], s97 offen lds\n" \
    "s_add_u32 m0, s63, 14336\n" \
    "s_add_u32 s97, s62, 409600\n" \
    "s_xor_b32 s97, s97, s60\n" \
    "buffer_load_dwordx4 %[vow], %[dw], s97 offen lds\n" \
    "s_add_u32 m0, s63, 15360\n" \
    "s_add_u32 s97, s62, 413696\n" \
    "s_xor_b32 s97, s97, s60\n" \
    "buffer_load_dwordx4 %[vow], %[dw], s97 offen lds\n" \
    "s_add_u32 m0, s63, 16384\n" \
    "s_add_u32 s97, s62, 524288\n" \
    "s_xor_b32 s97, s97, s60\n" \
    "buffer_load_dwordx4 %[vow], %[dw], s97 offen lds\n" \
    "s_add_u32 m0, s63, 17408\n" \
    "s_add_u32 s97, s62, 528384\n" \
    "s_xor_b32 s97, s97, s60\n" \
    "buffer_load_dwordx4 %[vow], %[dw], s97 offen lds\n" \
    "s_add_u32 m0, s63, 18432\n" \
    "s_add_u32 s97, s62, 655360\n" \
    "s_xor_b32 s97, s97, s60\n" \
    "buffer_load_dwordx4 %[vow], %[dw], s97 offen lds\n" \
    "s_add_u32 m0, s63, 19456\n" \
    "s_add_u32 s97, s62, 659456\n" \
    "s_xor_b32 s97, s97, s60\n" \
    "buffer_load_dwordx4 %[vow], %[dw], s97 offen lds\n" \
    "s_add_u32 m0, s63, 20480\n" \
    "s_add_u32 s97, s62, 540672\n" \
    "s_xor_b32 s97, s97, s60\n" \
    "buffer_load_dwordx4 %[vow], %[dw], s97 offen lds\n" \
    "s_add_u32 m0, s63, 21504\n" \
    "s_add_u32 s97, s62, 544768\n" \
    "s_xor_b32 s97, s97, s60\n" \
    "buffer_load_dwordx4 %[vow], %[dw], s97 offen lds\n" \
    "s_add_u32 m0, s63, 22528\n" \
    "s_add_u32 s97, s62, 671744\n" \
    "s_xor_b32 s97, s97, s60\n" \
    "buffer_load_dwordx4 %[vow], %[dw], s97 offen lds\n" \
    "s_add_u32 m0, s63, 23552\n" \
    "s_add_u32 s97, s62, 675840\n" \
    "s_xor_b32 s97, s97, s60\n" \
    "buffer_load_dwordx4 %[vow], %[dw], s97 offen lds\n" \
    "s_add_u32 m0, s63, 24576\n" \
    "s_add_u32 s97, s62, 786432\n" \
    "s_xor_b32 s97, s97, s60\n" \
    "buffer_load_dwordx4 %[vow], %[dw], s97 offen lds\n" \
    "s_add_u32 m0, s63, 25600\n" \
    "s_add_u32 s97, s62, 790528\n" \
    "s_xor_b32 s97, s97, s60\n" \
    "buffer_load_dwordx4 %[vow], %[dw], s97 offen lds\n" \
    "s_add_u32 m0, s63, 26624\n" \
    "s_add_u32 s97, s62, 917504\n" \
    "s_xor_b32 s97, s97, s60\n" \
    "buffer_load_dwordx4 %[vow], %[dw], s97 offen lds\n" \
    "s_add_u32 m0, s63, 27648\n" \
    "s_add_u32 s97, s62, 921600\n" \
    "s_xor_b32 s97, s97, s60\n" \
    "buffer_load_dwordx4 %[vow], %[dw], s97 offen lds\n" \
    "s_add_u32 m0, s63, 28672\n" \
    "s_add_u32 s97, s62, 802816\n" \
    "s_xor_b32 s97, s97, s60\n" \
    "buffer_load_dwordx4 %[vow], %[dw], s97 offen lds\n" \
    "s_add_u32 m0, s63, 29696\n" \
    "s_add_u32 s97, s62, 806912\n" \
    "s_xor_b32 s97, s97, s60\n" \
    "buffer_load_dwordx4 %[vow], %[dw], s97 offen lds\n" \
    "s_add_u32 m0, s63, 30720\n" \
    "s_add_u32 s97, s62, 933888\n" \
    "s_xor_b32 s97, s97, s60\n" \
    "buffer_load_dwordx4 %[vow], %[dw], s97 offen lds\n" \
    "s_add_u32 m0, s63, 31744\n" \
    "s_add_u32 s97, s62, 937984\n" \
    "s_xor_b32 s97, s97, s60\n" \
    "buffer_load_dwordx4 %[vow], %[dw], s97 offen lds\n" \
    "s_waitcnt vmcnt(24)\n" \
    "v_readfirstlane_b32 %[cfg], v31\n" \
    "s_nop 1\n" \
    "s_add_u32 %[cfg], %[cfg], 256\n" \
    "s_and_b32 %[cfg], %[cfg], 0xffffff00\n" \
    "v_mov_b32 v31, %[cfg]\n" \
    "buffer_store_dword v31, off, %[ws], %[hsa] sc1\n" \
    "ds_read_b64_tr_b16 a[0:1], v44 offset:0\n" \
    "ds_read_b64_tr_b16 a[2:3], v44 offset:1024\n" \
    "ds_read_b64_tr_b16 a[4:5], v44 offset:2048\n" \
    "ds_read_b64_tr_b16 a[6:7], v44 offset:3072\n" \
    "ds_read_b64_tr_b16 a[16:17], v45 offset:0\n" \
    "ds_read_b64_tr_b16 a[18:19], v45 offset:1024\n" \
    "ds_read_b64_tr_b16 a[20:21], v45 offset:2048\n" \
    "ds_read_b64_tr_b16 a[22:23], v45 offset:3072\n" \
    "ds_read_b64_tr_b16 a[8:9], v44 offset:4096\n" \
    "ds_read_b64_tr_b16 a[10:11], v44 offset:5120\n" \
    "ds_read_b64_tr_b16 a[12:13], v44 offset:6144\n" \
    "ds_read_b64_tr_b16 a[14:15], v44 offset:7168\n" \
    "ds_read_b64_tr_b16 a[24:25], v45 offset:4096\n" \
    "ds_read_b64_tr_b16 a[26:27], v45 offset:5120\n" \
    "ds_read_b64_tr_b16 a[28:29], v45 offset:6144\n" \
    "ds_read_b64_tr_b16 a[30:31], v45 offset:7168\n" \
    "s_waitcnt lgkmcnt(0)\n" \
    "s_add_u32 m0, s63, 0\n" \
    "s_add_u32 s97, s62, 1048576\n" \
    "s_xor_b32 s97, s97, s60\n" \
    "buffer_load_dwordx4 %[vow], %[dw], s97 offen lds\n" \
    "s_add_u32 m0, s63, 1024\n" \
    "s_add_u32 s97, s62, 1052672\n" \
    "s_xor_b32 s97, s97, s60\n" \
    "buffer_load_dwordx4 %[vow], %[dw], s97 offen lds\n" \
    "s_add_u32 m0, s63, 2048\n" \
    "s_add_u32 s97, s62, 1179648\n" \
    "s_xor_b32 s97, s97, s60\n" \
    "buffer_load_dwordx4 %[vow], %[dw], s97 offen lds\n" \
    "s_add_u32 m0, s63, 3072\n" \
    "s_add_u32 s97, s62, 1183744\n" \
    "s_xor_b32 s97, s97, s60\n" \
    "buffer_load_dwordx4 %[vow], %[dw], s97 offen lds\n" \
    "s_add_u32 m0, s63, 4096\n" \
    "s_add_u32 s97, s62, 1064960\n" \
    "s_xor_b32 s97, s97, s60\n" \
    "buffer_load_dwordx4 %[vow], %[dw], s97 offen lds\n" \
    "s_add_u32 m0, s63, 5120\n" \
    "s_add_u32 s97, s62, 1069056\n" \
    "s_xor_b32 s97, s97, s60\n" \
    "buffer_load_dwordx4 %[vow], %[dw], s97 offen lds\n" \
    "s_add_u32 m0, s63, 6144\n" \
    "s_add_u32 s97, s62, 1196032\n" \
    "s_xor_b32 s97, s97, s60\n" \
    "buffer_load_dwordx4 %[vow], %[dw], s97 offen lds\n" \
    "s_add_u32 m0, s63, 7168\n" \
    "s_add_u32 s97, s62, 1200128\n" \
    "s_xor_b32 s97, s97, s60\n" \
    "buffer_load_dwordx4 %[vow], %[dw], s97 offen lds\n" \
    "s_waitcnt vmcnt(24)\n" \
    "ds_read_b64_tr_b16 a[32:33], v44 offset:8192\n" \
    "ds_read_b64_tr_b16 a[34:35], v44 offset:9216\n" \
    "ds_read_b64_tr_b16 a[36:37], v44 offset:10240\n" \
    "ds_read_b64_tr_b16 a[38:39], v44 offset:11264\n" \
    "ds_read_b64_tr_b16 a[48:49], v45 offset:8192\n" \
    "ds_read_b64_tr_b16 a[50:51], v45 offset:9216\n" \
    "ds_read_b64_tr_b16 a[52:53], v45 offset:10240\n" \
    "ds_read_b64_tr_b16 a[54:55], v45 offset:11264\n" \
    "ds_read_b64_tr_b16 a[40:41], v44 offset:12288\n" \
    "ds_read_b64_tr_b16 a[42:43], v44 offset:13312\n" \
    "ds_read_b64_tr_b16 a[44:45], v44 offset:14336\n" \
    "ds_read_b64_tr_b16 a[46:47], v44 offset:15360\n" \
    "ds_read_b64_tr_b16 a[56:57], v45 offset:12288\n" \
    "ds_read_b64_tr_b16 a[58:59], v45 offset:13312\n" \
    "ds_read_b64_tr_b16 a[60:61], v45 offset:14336\n" \
    "ds_read_b64_tr_b16 a[62:63], v45 offset:15360\n" \
    "s_waitcnt lgkmcnt(0)\n" \
    "s_add_u32 m0, s63, 8192\n" \
    "s_add_u32 s97, s62, 1310720\n" \
    "s_xor_b32 s97, s97, s60\n" \
    "buffer_load_dwordx4 %[vow], %[dw], s97 offen lds\n" \
    "s_add_u32 m0, s63, 9216\n" \
    "s_add_u32 s97, s62, 1314816\n" \
    "s_xor_b32 s97, s97, s60\n" \
    "buffer_load_dwordx4 %[vow], %[dw], s97 offen lds\n" \
    "s_add_u32 m0, s63, 10240\n" \
    "s_add_u32 s97, s62, 1441792\n" \
    "s_xor_b32 s97, s97, s60\n" \
    "buffer_load_dwordx4 %[vow], %[dw], s97 offen lds\n" \
    "s_add_u32 m0, s63, 11264\n" \
    "s_add_u32 s97, s62, 1445888\n" \
    "s_xor_b32 s97, s97, s60\n" \
    "buffer_load_dwordx4 %[vow], %[dw], s97 offen lds\n" \
    "s_add_u32 m0, s63, 12288\n" \
    "s_add_u32 s97, s62, 1327104\n" \
    "s_xor_b32 s97, s97, s60\n" \
    "buffer_load_dwordx4 %[vow], %[dw], s97 offen lds\n" \
    "s_add_u32 m0, s63, 13312\n" \
    "s_add_u32 s97, s62, 1331200\n" \
    "s_xor_b32 s97, s97, s60\n" \
    "buffer_load_dwordx4 %[vow], %[dw], s97 offen lds\n" \
    "s_add_u32 m0, s63, 14336\n" \
    "s_add_u32 s97, s62, 1458176\n" \
    "s_xor_b32 s97, s97, s60\n" \
    "buffer_load_dwordx4 %[vow], %[dw], s97 offen lds\n" \
    "s_add_u32 m0, s63, 15360\n" \
    "s_add_u32 s97, s62, 1462272\n" \
    "s_xor_b32 s97, s97, s60\n" \
    "buffer_load_dwordx4 %[vow], %[dw], s97 offen lds\n" \
    "s_waitcnt vmcnt(24)\n" \
    "ds_read_b64_tr_b16 a[64:65], v44 offset:16384\n" \
    "ds_read_b64_tr_b16 a[66:67], v44 offset:17408\n" \
    "ds_read_b64_tr_b16 a[68:69], v44 offset:18432\n" \
    "ds_read_b64_tr_b16 a[70:71], v44 offset:19456\n" \
    "ds_read_b64_tr_b16 a[80:81], v45 offset:16384\n" \
    "ds_read_b64_tr_b16 a[82:83], v45 offset:17408\n" \
    "ds_read_b64_tr_b16 a[84:85], v45 offset:18432\n" \
    "ds_read_b64_tr_b16 a[86:87], v45 offset:19456\n" \
    "ds_read_b64_tr_b16 a[72:73], v44 offset:20480\n" \
    "ds_read_b64_tr_b16 a[74:75], v44 offset:21504\n" \
    "ds_read_b64_tr_b16 a[76:77], v44 offset:22528\n" \
    "ds_read_b64_tr_b16 a[78:79], v44 offset:23552\n" \
    "ds_read_b64_tr_b16 a[88:89], v45 offset:20480\n" \
    "ds_read_b64_tr_b16 a[90:91], v45 offset:21504\n" \
    "ds_read_b64_tr_b16 a[92:93], v45 offset:22528\n" \
    "ds_read_b64_tr_b16 a[94:95], v45 offset:23552\n" \
    "s_waitcnt lgkmcnt(0)\n" \
    "s_add_u32 m0, s63, 16384\n" \
    "s_add_u32 s97, s62, 1572864\n" \
    "s_xor_b32 s97, s97, s60\n" \
    "buffer_load_dwordx4 %[vow], %[dw], s97 offen lds\n" \
    "s_add_u32 m0, s63, 17408\n" \
    "s_add_u32 s97, s62, 1576960\n" \
    "s_xor_b32 s97, s97, s60\n" \
    "buffer_load_dwordx4 %[vow], %[dw], s97 offen lds\n" \
    "s_add_u32 m0, s63, 18432\n" \
    "s_add_u32 s97, s62, 1703936\n" \
    "s_xor_b32 s97, s97, s60\n" \
    "buffer_load_dwordx4 %[vow], %[dw], s97 offen lds\n" \
    "s_add_u32 m0, s63, 19456\n" \
    "s_add_u32 s97, s62, 1708032\n" \
    "s_xor_b32 s97, s97, s60\n" \
    "buffer_load_dwordx4 %[vow], %[dw], s97 offen lds\n" \
    "s_add_u32 m0, s63, 20480\n" \
    "s_add_u32 s97, s62, 1589248\n" \
    "s_xor_b32 s97, s97, s60\n" \
    "buffer_load_dwordx4 %[vow], %[dw], s97 offen lds\n" \
    "s_add_u32 m0, s63, 21504\n" \
    "s_add_u32 s97, s62, 1593344\n" \
    "s_xor_b32 s97, s97, s60\n" \
    "buffer_load_dwordx4 %[vow], %[dw], s97 offen lds\n" \
    "s_add_u32 m0, s63, 22528\n" \
    "s_add_u32 s97, s62, 1720320\n" \
    "s_xor_b32 s97, s97, s60\n" \
    "buffer_load_dwordx4 %[vow], %[dw], s97 offen lds\n" \
    "s_add_u32 m0, s63, 23552\n" \
    "s_add_u32 s97, s62, 1724416\n" \
    "s_xor_b32 s97, s97, s60\n" \
    "buffer_load_dwordx4 %[vow], %[dw], s97 offen lds\n" \
    "s_waitcnt vmcnt(24)\n" \
    "ds_read_b64_tr_b16 a[96:97], v44 offset:24576\n" \
    "ds_read_b64_tr_b16 a[98:99], v44 offset:25600\n" \
    "ds_read_b64_tr_b16 a[100:101], v44 offset:26624\n" \
    "ds_read_b64_tr_b16 a[102:103], v44 offset:27648\n" \
    "ds_read_b64_tr_b16 a[112:113], v45 offset:24576\n" \
    "ds_read_b64_tr_b16 a[114:115], v45 offset:25600\n" \
    "ds_read_b64_tr_b16 a[116:117], v45 offset:26624\n" \
    "ds_read_b64_tr_b16 a[118:119], v45 offset:27648\n" \
    "ds_read_b64_tr_b16 a[104:105], v44 offset:28672\n" \
    "ds_read_b64_tr_b16 a[106:107], v44 offset:29696\n" \
    "ds_read_b64_tr_b16 a[108:109], v44 offset:30720\n" \
    "ds_read_b64_tr_b16 a[110:111], v44 offset:31744\n" \
    "ds_read_b64_tr_b16 a[120:121], v45 offset:28672\n" \
    "ds_read_b64_tr_b16 a[122:123], v45 offset:29696\n" \
    "ds_read_b64_tr_b16 a[124:125], v45 offset:30720\n" \
    "ds_read_b64_tr_b16 a[126:127], v45 offset:31744\n" \
    "s_waitcnt lgkmcnt(0)\n" \
    "s_add_u32 m0, s63, 24576\n" \
    "s_add_u32 s97, s62, 1835008\n" \
    "s_xor_b32 s97, s97, s60\n" \
    "buffer_load_dwordx4 %[vow], %[dw], s97 offen lds\n" \
    "s_add_u32 m0, s63, 25600\n" \
    "s_add_u32 s97, s62, 1839104\n" \
    "s_xor_b32 s97, s97, s60\n" \
    "buffer_load_dwordx4 %[vow], %[dw], s97 offen lds\n" \
    "s_add_u32 m0, s63, 26624\n" \
    "s_add_u32 s97, s62, 1966080\n" \
    "s_xor_b32 s97, s97, s60\n" \
    "buffer_load_dwordx4 %[vow], %[dw], s97 offen lds\n" \
    "s_add_u32 m0, s63, 27648\n" \
    "s_add_u32 s97, s62, 1970176\n" \
    "s_xor_b32 s97, s97, s60\n" \
    "buffer_load_dwordx4 %[vow], %[dw], s97 offen lds\n" \
    "s_add_u32 m0, s63, 28672\n" \
    "s_add_u32 s97, s62, 1851392\n" \
    "s_xor_b32 s97, s97, s60\n" \
    "buffer_load_dwordx4 %[vow], %[dw], s97 offen lds\n" \
    "s_add_u32 m0, s63, 29696\n" \
    "s_add_u32 s97, s62, 1855488\n" \
    "s_xor_b32 s97, s97, s60\n" \
    "buffer_load_dwordx4 %[vow], %[dw], s97 offen lds\n" \
    "s_add_u32 m0, s63, 30720\n" \
    "s_add_u32 s97, s62, 1982464\n" \
    "s_xor_b32 s97, s97, s60\n" \
    "buffer_load_dwordx4 %[vow], %[dw], s97 offen lds\n" \
    "s_add_u32 m0, s63, 31744\n" \
    "s_add_u32 s97, s62, 1986560\n" \
    "s_xor_b32 s97, s97, s60\n" \
    "buffer_load_dwordx4 %[vow], %[dw], s97 offen lds\n" \
    "s_waitcnt vmcnt(24)\n" \
    "ds_read_b64_tr_b16 a[128:129], v44 offset:0\n" \
    "ds_read_b64_tr_b16 a[130:131], v44 offset:1024\n" \
    "ds_read_b64_tr_b16 a[132:133], v44 offset:2048\n" \
    "ds_read_b64_tr_b16 a[134:135], v44 offset:3072\n" \
    "ds_read_b64_tr_b16 a[144:145], v45 offset:0\n" \
    "ds_read_b64_tr_b16 a[146:147], v45 offset:1024\n" \
    "ds_read_b64_tr_b16 a[148:149], v45 offset:2048\n" \
    "ds_read_b64_tr_b16 a[150:151], v45 offset:3072\n" \
    "ds_read_b64_tr_b16 a[136:137], v44 offset:4096\n" \
    "ds_read_b64_tr_b16 a[138:139], v44 offset:5120\n" \
    "ds_read_b64_tr_b16 a[140:141], v44 offset:6144\n" \
    "ds_read_b64_tr_b16 a[142:143], v44 offset:7168\n" \
    "ds_read_b64_tr_b16 a[152:153], v45 offset:4096\n" \
    "ds_read_b64_tr_b16 a[154:155], v45 offset:5120\n" \
    "ds_read_b64_tr_b16 a[156:157], v45 offset:6144\n" \
    "ds_read_b64_tr_b16 a[158:159], v45 offset:7168\n" \
    "s_waitcnt lgkmcnt(0)\n" \
    "s_add_u32 m0, s63, 0\n" \
    "s_add_u32 s97, s61, 0\n" \
    "buffer_load_dwordx4 %[vok], %[dk], s97 offen lds\n" \
    "s_add_u32 m0, s63, 1024\n" \
    "s_add_u32 s97, s61, 65536\n" \
    "buffer_load_dwordx4 v46, %[dk], s97 offen lds\n" \
    "s_add_u32 m0, s63, 2048\n" \
    "s_add_u32 s97, s61, 131072\n" \
    "buffer_load_dwordx4 %[vok], %[dk], s97 offen lds\n" \
    "s_add_u32 m0, s63, 3072\n" \
    "s_add_u32 s97, s61, 196608\n" \
    "buffer_load_dwordx4 v46, %[dk], s97 offen lds\n" \
    "s_add_u32 m0, s63, 4096\n" \
    "s_add_u32 s97, s61, 32768\n" \
    "buffer_load_dwordx4 %[vok], %[dk], s97 offen lds\n" \
    "s_add_u32 m0, s63, 5120\n" \
    "s_add_u32 s97, s61, 98304\n" \
    "buffer_load_dwordx4 v46, %[dk], s97 offen lds\n" \
    "s_add_u32 m0, s63, 6144\n" \
    "s_add_u32 s97, s61, 163840\n" \
    "buffer_load_dwordx4 %[vok], %[dk], s97 offen lds\n" \
    "s_add_u32 m0, s63, 7168\n" \
    "s_add_u32 s97, s61, 229376\n" \
    "buffer_load_dwordx4 v46, %[dk], s97 offen lds\n" \
    "s_waitcnt vmcnt(24)\n" \
    "ds_read_b64_tr_b16 a[160:161], v44 offset:8192\n" \
    "ds_read_b64_tr_b16 a[162:163], v44 offset:9216\n" \
    "ds_read_b64_tr_b16 a[164:165], v44 offset:10240\n" \
    "ds_read_b64_tr_b16 a[166:167], v44 offset:11264\n" \
    "ds_read_b64_tr_b16 a[176:177], v45 offset:8192\n" \
    "ds_read_b64_tr_b16 a[178:179], v45 offset:9216\n" \
    "ds_read_b64_tr_b16 a[180:181], v45 offset:10240\n" \
    "ds_read_b64_tr_b16 a[182:183], v45 offset:11264\n" \
    "ds_read_b64_tr_b16 a[168:169], v44 offset:12288\n" \
    "ds_read_b64_tr_b16 a[170:171], v44 offset:13312\n" \
    "ds_read_b64_tr_b16 a[172:173], v44 offset:14336\n" \
    "ds_read_b64_tr_b16 a[174:175], v44 offset:15360\n" \
    "ds_read_b64_tr_b16 a[184:185], v45 offset:12288\n" \
    "ds_read_b64_tr_b16 a[186:187], v45 offset:13312\n" \
    "ds_read_b64_tr_b16 a[188:189], v45 offset:14336\n" \
    "ds_read_b64_tr_b16 a[190:191], v45 offset:15360\n" \
    "s_waitcnt lgkmcnt(0)\n" \
    "s_add_u32 m0, s63, 8192\n" \
    "s_add_u32 s97, s61, 262144\n" \
    "buffer_load_dwordx4 %[vok], %[dk], s97 offen lds\n" \
    "s_add_u32 m0, s63, 9216\n" \
    "s_add_u32 s97, s61, 327680\n" \
    "buffer_load_dwordx4 v46, %[dk], s97 offen lds\n" \
    "s_add_u32 m0, s63, 10240\n" \
    "s_add_u32 s97, s61, 393216\n" \
    "buffer_load_dwordx4 %[vok], %[dk], s97 offen lds\n" \
    "s_add_u32 m0, s63, 11264\n" \
    "s_add_u32 s97, s61, 458752\n" \
    "buffer_load_dwordx4 v46, %[dk], s97 offen lds\n" \
    "s_add_u32 m0, s63, 12288\n" \
    "s_add_u32 s97, s61, 294912\n" \
    "buffer_load_dwordx4 %[vok], %[dk], s97 offen lds\n" \
    "s_add_u32 m0, s63, 13312\n" \
    "s_add_u32 s97, s61, 360448\n" \
    "buffer_load_dwordx4 v46, %[dk], s97 offen lds\n" \
    "s_add_u32 m0, s63, 14336\n" \
    "s_add_u32 s97, s61, 425984\n" \
    "buffer_load_dwordx4 %[vok], %[dk], s97 offen lds\n" \
    "s_add_u32 m0, s63, 15360\n" \
    "s_add_u32 s97, s61, 491520\n" \
    "buffer_load_dwordx4 v46, %[dk], s97 offen lds\n" \
    "s_waitcnt vmcnt(24)\n" \
    "ds_read_b64_tr_b16 a[192:193], v44 offset:16384\n" \
    "ds_read_b64_tr_b16 a[194:195], v44 offset:17408\n" \
    "ds_read_b64_tr_b16 a[196:197], v44 offset:18432\n" \
    "ds_read_b64_tr_b16 a[198:199], v44 offset:19456\n" \
    "ds_read_b64_tr_b16 a[208:209], v45 offset:16384\n" \
    "ds_read_b64_tr_b16 a[210:211], v45 offset:17408\n" \
    "ds_read_b64_tr_b16 a[212:213], v45 offset:18432\n" \
    "ds_read_b64_tr_b16 a[214:215], v45 offset:19456\n" \
    "ds_read_b64_tr_b16 a[200:201], v44 offset:20480\n" \
    "ds_read_b64_tr_b16 a[202:203], v44 offset:21504\n" \
    "ds_read_b64_tr_b16 a[204:205], v44 offset:22528\n" \
    "ds_read_b64_tr_b16 a[206:207], v44 offset:23552\n" \
    "ds_read_b64_tr_b16 a[216:217], v45 offset:20480\n" \
    "ds_read_b64_tr_b16 a[218:219], v45 offset:21504\n" \
    "ds_read_b64_tr_b16 a[220:221], v45 offset:22528\n" \
    "ds_read_b64_tr_b16 a[222:223], v45 offset:23552\n" \
    "s_waitcnt lgkmcnt(0)\n" \
    "s_add_u32 m0, s63, 16384\n" \
    "s_add_u32 s97, s61, 524288\n" \
    "buffer_load_dwordx4 %[vok], %[dk], s97 offen lds\n" \
    "s_add_u32 m0, s63, 17408\n" \
    "s_add_u32 s97, s61, 589824\n" \
    "buffer_load_dwordx4 v46, %[dk], s97 offen lds\n" \
    "s_add_u32 m0, s63, 18432\n" \
    "s_add_u32 s97, s61, 655360\n" \
    "buffer_load_dwordx4 %[vok], %[dk], s97 offen lds\n" \
    "s_add_u32 m0, s63, 19456\n" \
    "s_add_u32 s97, s61, 720896\n" \
    "buffer_load_dwordx4 v46, %[dk], s97 offen lds\n" \
    "s_add_u32 m0, s63, 20480\n" \
    "s_add_u32 s97, s61, 557056\n" \
    "buffer_load_dwordx4 %[vok], %[dk], s97 offen lds\n" \
    "s_add_u32 m0, s63, 21504\n" \
    "s_add_u32 s97, s61, 622592\n" \
    "buffer_load_dwordx4 v46, %[dk], s97 offen lds\n" \
    "s_add_u32 m0, s63, 22528\n" \
    "s_add_u32 s97, s61, 688128\n" \
    "buffer_load_dwordx4 %[vok], %[dk], s97 offen lds\n" \
    "s_add_u32 m0, s63, 23552\n" \
    "s_add_u32 s97, s61, 753664\n" \
    "buffer_load_dwordx4 v46, %[dk], s97 offen lds\n" \
    "s_waitcnt vmcnt(24)\n" \
    "ds_read_b64_tr_b16 a[224:225], v44 offset:24576\n" \
    "ds_read_b64_tr_b16 a[226:227], v44 offset:25600\n" \
    "ds_read_b64_tr_b16 a[228:229], v44 offset:26624\n" \
    "ds_read_b64_tr_b16 a[230:231], v44 offset:27648\n" \
    "ds_read_b64_tr_b16 a[240:241], v45 offset:24576\n" \
    "ds_read_b64_tr_b16 a[242:243], v45 offset:25600\n" \
    "ds_read_b64_tr_b16 a[244:245], v45 offset:26624\n" \
    "ds_read_b64_tr_b16 a[246:247], v45 offset:27648\n" \
    "ds_read_b64_tr_b16 a[232:233], v44 offset:28672\n" \
    "ds_read_b64_tr_b16 a[234:235], v44 offset:29696\n" \
    "ds_read_b64_tr_b16 a[236:237], v44 offset:30720\n" \
    "ds_read_b64_tr_b16 a[238:239], v44 offset:31744\n" \
    "ds_read_b64_tr_b16 a[248:249], v45 offset:28672\n" \
    "ds_read_b64_tr_b16 a[250:251], v45 offset:29696\n" \
    "ds_read_b64_tr_b16 a[252:253], v45 offset:30720\n" \
    "ds_read_b64_tr_b16 a[254:255], v45 offset:31744\n" \
    "s_waitcnt lgkmcnt(0)\n" \
    "s_add_u32 m0, s63, 24576\n" \
    "s_add_u32 s97, s61, 786432\n" \
    "buffer_load_dwordx4 %[vok], %[dk], s97 offen lds\n" \
    "s_add_u32 m0, s63, 25600\n" \
    "s_add_u32 s97, s61, 851968\n" \
    "buffer_load_dwordx4 v46, %[dk], s97 offen lds\n" \
    "s_add_u32 m0, s63, 26624\n" \
    "s_add_u32 s97, s61, 917504\n" \
    "buffer_load_dwordx4 %[vok], %[dk], s97 offen lds\n" \
    "s_add_u32 m0, s63, 27648\n" \
    "s_add_u32 s97, s61, 983040\n" \
    "buffer_load_dwordx4 v46, %[dk], s97 offen lds\n" \
    "s_add_u32 m0, s63, 28672\n" \
    "s_add_u32 s97, s61, 819200\n" \
    "buffer_load_dwordx4 %[vok], %[dk], s97 offen lds\n" \
    "s_add_u32 m0, s63, 29696\n" \
    "s_add_u32 s97, s61, 884736\n" \
    "buffer_load_dwordx4 v46, %[dk], s97 offen lds\n" \
    "s_add_u32 m0, s63, 30720\n" \
    "s_add_u32 s97, s61, 950272\n" \
    "buffer_load_dwordx4 %[vok], %[dk], s97 offen lds\n" \
    "s_add_u32 m0, s63, 31744\n" \
    "s_add_u32 s97, s61, 1015808\n" \
    "buffer_load_dwordx4 v46, %[dk], s97 offen lds\n" \
    "s_add_u32 s97, %[hsa], 32768\n" \
    "buffer_load_dword v31, off, %[ws], s97 sc1\n" \
    "s_waitcnt vmcnt(0)\n" \
    "s_barrier\n" \
    "ds_read_b64_tr_b16 v[128:129], v40 offset:0\n" \
    "ds_read_b64_tr_b16 v[130:131], v40 offset:4096\n" \
    "ds_read_b64_tr_b16 v[132:133], v40 offset:8192\n" \
    "ds_read_b64_tr_b16 v[134:135], v40 offset:12288\n" \
    "ds_read_b64_tr_b16 v[136:137], v41 offset:0\n" \
    "ds_read_b64_tr_b16 v[138:139], v41 offset:4096\n" \
    "ds_read_b64_tr_b16 v[140:141], v41 offset:8192\n" \
    "ds_read_b64_tr_b16 v[142:143], v41 offset:12288\n" \
    "ds_read_b64_tr_b16 v[144:145], v40 offset:16384\n" \
    "ds_read_b64_tr_b16 v[146:147], v40 offset:20480\n" \
    "ds_read_b64_tr_b16 v[148:149], v40 offset:24576\n" \
    "ds_read_b64_tr_b16 v[150:151], v40 offset:28672\n" \
    "ds_read_b64_tr_b16 v[152:153], v41 offset:16384\n" \
    "ds_read_b64_tr_b16 v[154:155], v41 offset:20480\n" \
    "ds_read_b64_tr_b16 v[156:157], v41 offset:24576\n" \
    "ds_read_b64_tr_b16 v[158:159], v41 offset:28672\n" \
    "ds_read_b64_tr_b16 v[160:161], v40 offset:32768\n" \
    "ds_read_b64_tr_b16 v[162:163], v40 offset:36864\n" \
    "ds_read_b64_tr_b16 v[164:165], v40 offset:40960\n" \
    "ds_read_b64_tr_b16 v[166:167], v40 offset:45056\n" \
    "ds_read_b64_tr_b16 v[168:169], v41 offset:32768\n" \
    "ds_read_b64_tr_b16 v[170:171], v41 offset:36864\n" \
    "ds_read_b64_tr_b16 v[172:173], v41 offset:40960\n" \
    "ds_read_b64_tr_b16 v[174:175], v41 offset:45056\n" \
    "ds_read_b64_tr_b16 v[176:177], v40 offset:49152\n" \
    "ds_read_b64_tr_b16 v[178:179], v40 offset:53248\n" \
    "ds_read_b64_tr_b16 v[180:181], v40 offset:57344\n" \
    "ds_read_b64_tr_b16 v[182:183], v40 offset:61440\n" \
    "ds_read_b64_tr_b16 v[184:185], v41 offset:49152\n" \
    "ds_read_b64_tr_b16 v[186:187], v41 offset:53248\n" \
    "ds_read_b64_tr_b16 v[188:189], v41 offset:57344\n" \
    "ds_read_b64_tr_b16 v[190:191], v41 offset:61440\n" \
    "ds_read_b64_tr_b16 v[192:193], v42 offset:0\n" \
    "ds_read_b64_tr_b16 v[194:195], v42 offset:4096\n" \
    "ds_read_b64_tr_b16 v[196:197], v42 offset:8192\n" \
    "ds_read_b64_tr_b16 v[198:199], v42 offset:12288\n" \
    "ds_read_b64_tr_b16 v[200:201], v43 offset:0\n" \
    "ds_read_b64_tr_b16 v[202:203], v43 offset:4096\n" \
    "ds_read_b64_tr_b16 v[204:205], v43 offset:8192\n" \
    "ds_read_b64_tr_b16 v[206:207], v43 offset:12288\n" \
    "ds_read_b64_tr_b16 v[208:209], v42 offset:16384\n" \
    "ds_read_b64_tr_b16 v[210:211], v42 offset:20480\n" \
    "ds_read_b64_tr_b16 v[212:213], v42 offset:24576\n" \
    "ds_read_b64_tr_b16 v[214:215], v42 offset:28672\n" \
    "ds_read_b64_tr_b16 v[216:217], v43 offset:16384\n" \
    "ds_read_b64_tr_b16 v[218:219], v43 offset:20480\n" \
    "ds_read_b64_tr_b16 v[220:221], v43 offset:24576\n" \
    "ds_read_b64_tr_b16 v[222:223], v43 offset:28672\n" \
    "ds_read_b64_tr_b16 v[224:225], v42 offset:32768\n" \
    "ds_read_b64_tr_b16 v[226:227], v42 offset:36864\n" \
    "ds_read_b64_tr_b16 v[228:229], v42 offset:40960\n" \
    "ds_read_b64_tr_b16 v[230:231], v42 offset:45056\n" \
    "ds_read_b64_tr_b16 v[232:233], v43 offset:32768\n" \
    "ds_read_b64_tr_b16 v[234:235], v43 offset:36864\n" \
    "ds_read_b64_tr_b16 v[236:237], v43 offset:40960\n" \
    "ds_read_b64_tr_b16 v[238:239], v43 offset:45056\n" \
    "ds_read_b64_tr_b16 v[240:241], v42 offset:49152\n" \
    "ds_read_b64_tr_b16 v[242:243], v42 offset:53248\n" \
    "ds_read_b64_tr_b16 v[244:245], v42 offset:57344\n" \
    "ds_read_b64_tr_b16 v[246:247], v42 offset:61440\n" \
    "ds_read_b64_tr_b16 v[248:249], v43 offset:49152\n" \
    "ds_read_b64_tr_b16 v[250:251], v43 offset:53248\n" \
    "ds_read_b64_tr_b16 v[252:253], v43 offset:57344\n" \
    "ds_read_b64_tr_b16 v[254:255], v43 offset:61440\n" \
    "s_waitcnt lgkmcnt(0)\n" \
    "s_branch LB_hsl_%=\n" \
    "LB_hs_%=:\n" \
    "s_add_u32 s97, %[hsa], 32768\n" \
    "buffer_load_dword v31, off, %[ws], s97 sc1\n" \
    "s_waitcnt vmcnt(0)\n" \
    "LB_hsl_%=:\n" \
    "v_readfirstlane_b32 s97, v31\n" \
    "s_nop 1\n" \
    "s_xor_b32 s97, s97, %[cfg]\n" \
    "s_cmp_gt_u32 s97, 255\n" \
    "s_cbranch_scc1 LB_hsr_%=\n" \
    "s_and_b32 s96, s97, 3\n" \
    "s_cmp_lg_u32 s96, %[est]\n" \
    "s_cbranch_scc1 LB_redo_%=\n" \
    "s_bitcmp0_b32 s97, 3\n" \
    "s_cbranch_scc1 LB_hsr_%=\n" \
    "s_mov_b32 %[cfg], s97\n" \
    "LB_hsok_%=:\n" \
    "s_and_b32 %[pol], %[cfg], 4\n" \
    "s_lshl_b32 %[pol], %[pol], 11\n" \
    "s_xor_b32 %[npol], %[pol], 8192\n" \
    "s_waitcnt vmcnt(0)\n" \
    "v_mov_b32 v31, 0\n" \
    "s_add_u32 s97, %[hsa], 69632\n" \
    "buffer_store_dword v31, off, %[ws], s97 sc1\n" \
    "s_mov_b32 s94, %[pol]\n" \
    "s_add_u32 s99, s94, 524288\n" \
    "s_mov_b32 s84, s94\n" \
    "v_mov_b32 v124, -1\n" \
    "v_mov_b32 v125, -1\n" \
    "v_mov_b32 v126, -1\n" \
    "s_lshl_b32 m0, %[w], 11\n" \
    "s_mov_b32 s40, " STR(D_A0) " * 24\n" \
    "s_mov_b32 s41, " STR(D_B0) " * 8\n" \
    "s_mov_b32 s42, 0\n" \
    "s_mov_b32 s43, -1\n" \
    "s_mov_b32 s46, 0\n" \
    "v_mov_b64 v[96:97], 0\n" \
    "v_mov_b64 v[98:99], 0\n" \
    "v_mov_b64 v[100:101], 0\n" \
    "v_mov_b64 v[102:103], 0\n" \
    "buffer_store_dword v72, %[vmo], %[ws], s94 offen sc1\n" \
    "buffer_store_dword v72, v34, %[ws], s94 offen sc1\n" \
    "buffer_store_dword v124, %[vmo], %[ws], s99 offen sc1\n" \
    "buffer_store_dword v124, v34, %[ws], s99 offen sc1\n" \
    "buffer_store_dwordx2 v[124:125], %[vk], %[ws], s84 offen sc1\n" \
    "s_waitcnt vmcnt(0)\n" \
    "s_barrier\n" \
    "s_add_u32 s97, %[rdy], %[npol]\n" \
    "v_mov_b32 v31, 0\n" \
    "s_cmp_lg_u32 %[w], 1\n" \
    "s_cbranch_scc1 LB_en_%=\n" \
    "buffer_store_dword v31, off, %[ws], s97 sc1\n" \
    "s_branch LB_rs_%=\n" \
    "LB_en_%=:\n" \
    "v_readfirstlane_b32 s97, %[vmsg]\n" \
    "v_lshlrev_b32 v80, 1, %[lr]\n" \
    "s_add_u32 s60, %[npol], 2082816\n" \
    "s_cmp_lg_u32 s97, 0\n" \
    "s_cbranch_scc1 LB_tmx_%=\n" \
    "LB_rd_%=:\n" \
    "s_add_u32 s98, s98, 1\n" \
    "s_cmp_gt_u32 s98, 131072\n" \
    "s_cbranch_scc1 LB_fail_%=\n" \
    "buffer_load_dword v81, v80, %[ws], s60 offen sc1\n" \
    "buffer_load_dword v82, v80, %[ws], s60 offen offset:16 sc1\n" \
    "s_waitcnt vmcnt(0)\n" \
    "s_memrealtime s[64:65]\n" \
    "v_or_b32 v81, v81, v82\n" \
    "v_cmp_ne_u32 vcc, 0, v81\n" \
    "s_waitcnt lgkmcnt(0)\n" \
    "s_cbranch_vccnz LB_rd_%=\n" \
    "s_lshl_b32 s64, s64, 3\n" \
    "s_lshr_b32 s85, s94, 19\n" \
    "s_lshl_b32 s85, s85, 8\n" \
    "s_add_u32 s85, s85, 2080768\n" \
    "s_add_u32 s85, s85, %[npol]\n" \
    "s_add_u32 s97, s64, " STR(T_GO) " + " STR(T_XS0) " + " STR(D_A0) " + " STR(M_O0) "\n" \
    "s_min_u32 s97, s97, -2\n" \
    "v_mov_b32 v72, s97\n" \
    "s_add_u32 s97, s64, " STR(T_GO) " + " STR(T_XS0) " + " STR(D_A0) " + " STR(M_O0) " + " STR(D_B0) " + " STR(M_G0) "\n" \
    "s_min_u32 s97, s97, -2\n" \
    "v_mov_b32 v73, s97\n" \
    "s_add_u32 s97, s64, " STR(T_GO) "\n" \
    "s_min_u32 s97, s97, -2\n" \
    "v_mov_b32 v74, s97\n" \
    "buffer_store_dwordx3 v[72:74], %[vmsg], %[ws], s85 offen sc1\n" \
    "s_branch LB_tmx_%=\n" \
    "LB_tm_%=:\n" \
    "s_memrealtime s[72:73]\n" \
    "s_nop 1\n" \
    "s_memrealtime s[74:75]\n" \
    "s_nop 1\n" \
    "s_memrealtime s[76:77]\n" \
    "s_nop 1\n" \
    "s_memrealtime s[78:79]\n" \
    "s_nop 1\n" \
    "s_memrealtime s[80:81]\n" \
    "s_nop 1\n" \
    "s_memrealtime s[82:83]\n" \
    "s_nop 1\n" \
    "s_memrealtime s[92:93]\n" \
    "s_nop 1\n" \
    "s_memrealtime s[70:71]\n" \
    "s_getpc_b64 s[68:69]\n" \
    "LB_tmp_%=:\n" \
    "s_add_u32 s68, s68, LB_tmx_%= - LB_tmp_%=\n" \
    "s_addc_u32 s69, s69, 0\n" \
    "s_waitcnt lgkmcnt(0)\n" \
    "s_sub_u32 s97, s87, s72\n" \
    "s_sub_u32 s97, s97, s74\n" \
    "s_sub_u32 s97, s97, s76\n" \
    "s_sub_u32 s97, s97, s78\n" \
    "s_sub_u32 s97, s97, s80\n" \
    "s_sub_u32 s97, s97, s82\n" \
    "s_sub_u32 s97, s97, s92\n" \
    "s_sub_u32 s97, s97, s70\n" \
    "s_max_i32 s42, s42, s97\n" \
    "s_cmp_gt_u32 s97, 160\n" \
    "s_cbranch_scc1 LB_tmc_%=\n" \
    "s_mul_i32 s97, s97, 47\n" \
    "s_lshr_b32 s97, s97, 4\n" \
    "s_and_b32 s97, s97, -4\n" \
    "s_sub_u32 s68, s68, s97\n" \
    "s_subb_u32 s69, s69, 0\n" \
    "s_setpc_b64 s[68:69]\n" \
    ".rept 118\n" \
    "s_nop 0\n" \
    ".endr\n" \
    "LB_tmx_%=:\n" \
    "s_barrier\n" \
    "buffer_load_dwordx4 v32, %[ws], s94 offen sc1 lds\n" \
    "s_xor_b32 s99, s94, 1048576\n" \
    "s_lshr_b32 s85, s94, 19\n" \
    "s_lshl_b32 s85, s85, 8\n" \
    "s_add_u32 s85, s85, 2080768\n" \
    "s_add_u32 s85, s85, %[npol]\n" \
    "s_nop 9\n" \
    "buffer_load_dwordx4 v33, %[ws], s94 offen offset:1024 sc1 lds\n" \
    "s_nop 15\n" \
    "s_nop 7\n" \
    "buffer_load_dwordx3 v[88:90], off, %[ws], s85 sc1\n" \
    "buffer_load_dword v85, %[vrl], %[ws], %[npol] offen sc1\n" \
    "s_waitcnt vmcnt(3)\n" \
    "s_barrier\n" \
    "ds_read_b128 v[40:43], %[lr] offset:0\n" \
    "ds_read_b128 v[44:47], %[lr] offset:2048\n" \
    "ds_read_b128 v[48:51], %[lr] offset:4096\n" \
    "ds_read_b128 v[52:55], %[lr] offset:6144\n" \
    "s_waitcnt lgkmcnt(3)\n" \
    "v_smfmac_f32_16x16x64_bf16 v[96:99], v[40:43], v[128:135], %[idxm]\n" \
    "v_smfmac_f32_16x16x64_bf16 v[100:103], v[40:43], v[136:143], %[idxm]\n" \
    "s_waitcnt lgkmcnt(2)\n" \
    "v_smfmac_f32_16x16x64_bf16 v[96:99], v[44:47], v[160:167], %[idxm]\n" \
    "s_waitcnt vmcnt(2)\n" \
    "s_barrier\n" \
    "v_smfmac_f32_16x16x64_bf16 v[100:103], v[44:47], v[168:175], %[idxm]\n" \
    "ds_read_b128 v[56:59], %[lr] offset:1024\n" \
    "ds_read_b128 v[60:63], %[lr] offset:3072\n" \
    "s_waitcnt lgkmcnt(3)\n" \
    "v_smfmac_f32_16x16x64_bf16 v[96:99], v[48:51], v[192:199], %[idxm]\n" \
    "ds_read_b128 v[64:67], %[lr] offset:5120\n" \
    "ds_read_b128 v[68:71], %[lr] offset:7168\n" \
    "v_smfmac_f32_16x16x64_bf16 v[100:103], v[48:51], v[200:207], %[idxm]\n" \
    "s_waitcnt lgkmcnt(4)\n" \
    "v_smfmac_f32_16x16x64_bf16 v[96:99], v[52:55], v[224:231], %[idxm]\n" \
    "v_smfmac_f32_16x16x64_bf16 v[100:103], v[52:55], v[232:239], %[idxm]\n" \
    "s_waitcnt lgkmcnt(3)\n" \
    "v_smfmac_f32_16x16x64_bf16 v[96:99], v[56:59], v[144:151], %[idxm]\n" \
    "v_smfmac_f32_16x16x64_bf16 v[100:103], v[56:59], v[152:159], %[idxm]\n" \
    "s_waitcnt lgkmcnt(2)\n" \
    "v_smfmac_f32_16x16x64_bf16 v[96:99], v[60:63], v[176:183], %[idxm]\n" \
    "v_smfmac_f32_16x16x64_bf16 v[100:103], v[60:63], v[184:191], %[idxm]\n" \
    "s_waitcnt lgkmcnt(1)\n" \
    "v_smfmac_f32_16x16x64_bf16 v[96:99], v[64:67], v[208:215], %[idxm]\n" \
    "v_smfmac_f32_16x16x64_bf16 v[100:103], v[64:67], v[216:223], %[idxm]\n" \
    "s_waitcnt lgkmcnt(0)\n" \
    "v_smfmac_f32_16x16x64_bf16 v[96:99], v[68:71], v[240:247], %[idxm]\n" \
    "v_smfmac_f32_16x16x64_bf16 v[100:103], v[68:71], v[248:255], %[idxm]\n" \
    "s_nop 4\n" \
    "v_add_f32 v72, v96, v98\n" \
    "v_add_f32 v73, v97, v99\n" \
    "v_cmp_u_f32 vcc, v72, v73\n" \
    "v_cvt_pk_bf16_f32 v76, v72, v73\n" \
    "v_add_f32 v74, v100, v102\n" \
    "v_add_f32 v75, v101, v103\n" \
    "v_cvt_pk_bf16_f32 v77, v74, v75\n" \
    "s_cbranch_vccnz LB_memr_%=\n" \
    "LB_memok_%=:\n" \
    "buffer_store_dwordx2 v[76:77], %[vk], %[ws], s84 offen sc1\n" \
    "s_cmp_eq_u32 s95, 63\n" \
    "s_cbranch_scc1 LB_done_%=\n" \
    "s_nop 11\n" \
    "s_xor_b32 s97, s85, 512\n" \
    "s_sub_u32 s96, s43, s42\n" \
    "v_mov_b32 v72, s96\n" \
    "buffer_store_dword v72, %[vrp], %[ws], %[npol] offen sc1\n" \
    "s_xor_b32 s84, s84, 1048576\n" \
    "buffer_store_dwordx2 v[124:125], %[vk], %[ws], s84 offen sc1\n" \
    "buffer_store_dword v124, %[vmo], %[ws], s99 offen sc1\n" \
    "buffer_store_dword v124, v34, %[ws], s99 offen sc1\n" \
    "buffer_store_dwordx3 v[124:126], %[vmsg], %[ws], s97 offen sc1\n" \
    "v_mov_b64 v[96:97], 0\n" \
    "v_mov_b64 v[98:99], 0\n" \
    "v_mov_b64 v[100:101], 0\n" \
    "v_mov_b64 v[102:103], 0\n" \
    "v_mov_b64 v[104:105], 0\n" \
    "v_mov_b64 v[106:107], 0\n" \
    "v_mov_b64 v[108:109], 0\n" \
    "v_mov_b64 v[110:111], 0\n" \
    "v_mov_b64 v[112:113], 0\n" \
    "v_mov_b64 v[114:115], 0\n" \
    "v_mov_b64 v[116:117], 0\n" \
    "v_mov_b64 v[118:119], 0\n" \
    "s_waitcnt vmcnt(6)\n" \
    "LB_msg_%=:\n" \
    "v_readfirstlane_b32 s86, v88\n" \
    "v_readfirstlane_b32 s87, v89\n" \
    "v_readfirstlane_b32 s96, v90\n" \
    "s_nop 1\n" \
    "s_max_u32 s97, s86, s87\n" \
    "s_max_u32 s97, s97, s96\n" \
    "s_cmp_eq_u32 s97, -1\n" \
    "s_cbranch_scc1 LB_msgr_%=\n" \
    "v_readlane_b32 s97, v85, 0\n" \
    "v_readlane_b32 s96, v85, 1\n" \
    "v_readlane_b32 s44, v85, 2\n" \
    "v_readlane_b32 s45, v85, 3\n" \
    "s_nop 1\n" \
    "s_max_i32 s97, s97, s96\n" \
    "s_max_i32 s44, s44, s45\n" \
    "s_max_i32 s97, s97, s44\n" \
    "s_min_i32 s97, s97, 0x100000\n" \
    "s_lshl_b32 s96, s97, 3\n" \
    "s_sub_u32 s96, s96, s40\n" \
    "s_max_i32 s96, s96, -2304\n" \
    "s_min_i32 s96, s96, 384\n" \
    "s_cmp_lt_u32 s95, 7\n" \
    "s_cselect_b32 s44, 1, 2\n" \
    "s_ashr_i32 s96, s96, s44\n" \
    "s_cmp_gt_i32 s97, 0\n" \
    "s_cselect_b32 s96, s96, 0\n" \
    "s_cmp_gt_u32 s95, 1\n" \
    "s_cselect_b32 s96, s96, 0\n" \
    "s_add_u32 s40, s40, s96\n" \
    "v_readlane_b32 s97, v85, 4\n" \
    "v_readlane_b32 s96, v85, 5\n" \
    "v_readlane_b32 s44, v85, 6\n" \
    "v_readlane_b32 s45, v85, 7\n" \
    "s_nop 1\n" \
    "s_max_i32 s97, s97, s96\n" \
    "s_max_i32 s44, s44, s45\n" \
    "s_max_i32 s97, s97, s44\n" \
    "s_min_i32 s97, s97, 0x100000\n" \
    "s_lshl_b32 s96, s97, 3\n" \
    "s_sub_u32 s96, s96, s41\n" \
    "s_max_i32 s96, s96, -768\n" \
    "s_min_i32 s96, s96, 128\n" \
    "s_cmp_lt_u32 s95, 7\n" \
    "s_cselect_b32 s44, 1, 2\n" \
    "s_ashr_i32 s96, s96, s44\n" \
    "s_cmp_gt_i32 s97, 0\n" \
    "s_cselect_b32 s96, s96, 0\n" \
    "s_cmp_gt_u32 s95, 2\n" \
    "s_cselect_b32 s96, s96, 0\n" \
    "s_add_u32 s41, s41, s96\n" \
    "s_mul_i32 s44, s40, 2731\n" \
    "s_lshr_b32 s44, s44, 16\n" \
    "s_add_u32 s44, s44, " STR(M_O) "\n" \
    "s_add_u32 s44, s44, s46\n" \
    "s_lshr_b32 s45, s41, 3\n" \
    "s_add_u32 s45, s45, " STR(M_G) "\n" \
    "s_add_u32 s45, s45, s46\n" \
    "s_add_u32 s45, s45, s44\n" \
    "s_sub_u32 s43, s87, s86\n" \
    "s_mov_b32 s42, -100000\n" \
    "s_add_u32 s60, s94, s66\n" \
    "s_xor_b32 s61, s60, 4096\n" \
    "s_add_u32 s95, s95, 1\n" \
    "s_add_u32 s94, s94, 524288\n" \
    "s_and_b32 s94, s94, 2097151\n" \
    "s_lshr_b32 s85, s94, 19\n" \
    "s_lshl_b32 s85, s85, 8\n" \
    "s_add_u32 s85, s85, 2080768\n" \
    "s_add_u32 s85, s85, %[npol]\n" \
    "s_cmp_lg_u32 %[w], 1\n" \
    "s_cbranch_scc1 LB_tox_%=\n" \
    "LB_to_%=:\n" \
    "s_memrealtime s[72:73]\n" \
    "s_nop 1\n" \
    "s_memrealtime s[74:75]\n" \
    "s_nop 1\n" \
    "s_memrealtime s[76:77]\n" \
    "s_nop 1\n" \
    "s_memrealtime s[78:79]\n" \
    "s_nop 1\n" \
    "s_memrealtime s[80:81]\n" \
    "s_nop 1\n" \
    "s_memrealtime s[82:83]\n" \
    "s_nop 1\n" \
    "s_memrealtime s[92:93]\n" \
    "s_nop 1\n" \
    "s_memrealtime s[70:71]\n" \
    "s_getpc_b64 s[68:69]\n" \
    "LB_top_%=:\n" \
    "s_add_u32 s68, s68, LB_tox_%= - LB_top_%=\n" \
    "s_addc_u32 s69, s69, 0\n" \
    "s_waitcnt lgkmcnt(0)\n" \
    "s_sub_u32 s97, s86, s72\n" \
    "s_sub_u32 s97, s97, s74\n" \
    "s_sub_u32 s97, s97, s76\n" \
    "s_sub_u32 s97, s97, s78\n" \
    "s_sub_u32 s97, s97, s80\n" \
    "s_sub_u32 s97, s97, s82\n" \
    "s_sub_u32 s97, s97, s92\n" \
    "s_sub_u32 s97, s97, s70\n" \
    "s_cmp_gt_u32 s97, 160\n" \
    "s_cbranch_scc1 LB_toc_%=\n" \
    "s_mul_i32 s97, s97, 47\n" \
    "s_lshr_b32 s97, s97, 4\n" \
    "s_and_b32 s97, s97, -4\n" \
    "s_sub_u32 s68, s68, s97\n" \
    "s_subb_u32 s69, s69, 0\n" \
    "s_setpc_b64 s[68:69]\n" \
    ".rept 118\n" \
    "s_nop 0\n" \
    ".endr\n" \
    "LB_tox_%=:\n" \
    "s_barrier\n" \
    "buffer_load_dwordx4 v[40:43], v36, %[ws], s60 offen sc1\n" \
    "s_nop 12\n" \
    "buffer_load_dwordx4 v[44:47], v37, %[ws], s60 offen sc1\n" \
    "s_nop 12\n" \
    "buffer_load_dwordx4 v[48:51], v38, %[ws], s60 offen sc1\n" \
    "s_nop 12\n" \
    "buffer_load_dwordx4 v[52:55], v39, %[ws], s60 offen sc1\n" \
    "s_nop 12\n" \
    "buffer_load_dwordx4 v[56:59], v36, %[ws], s61 offen sc1\n" \
    "s_nop 12\n" \
    "buffer_load_dwordx4 v[60:63], v37, %[ws], s61 offen sc1\n" \
    "s_nop 12\n" \
    "buffer_load_dwordx4 v[64:67], v38, %[ws], s61 offen sc1\n" \
    "s_nop 12\n" \
    "buffer_load_dwordx4 v[68:71], v39, %[ws], s61 offen sc1\n" \
    "LB_om_%=:\n" \
    "s_waitcnt vmcnt(7)\n" \
    "v_smfmac_f32_16x16x64_bf16 v[104:107], v[40:43], a[0:7], %[idxo]\n" \
    "v_smfmac_f32_16x16x64_bf16 v[108:111], v[40:43], a[8:15], %[idxo]\n" \
    "v_smfmac_f32_16x16x64_bf16 v[112:115], v[40:43], a[16:23], %[idxo]\n" \
    "v_smfmac_f32_16x16x64_bf16 v[116:119], v[40:43], a[24:31], %[idxo]\n" \
    "s_waitcnt vmcnt(6)\n" \
    "v_smfmac_f32_16x16x64_bf16 v[104:107], v[44:47], a[32:39], %[idxo]\n" \
    "v_smfmac_f32_16x16x64_bf16 v[108:111], v[44:47], a[40:47], %[idxo]\n" \
    "v_smfmac_f32_16x16x64_bf16 v[112:115], v[44:47], a[48:55], %[idxo]\n" \
    "v_smfmac_f32_16x16x64_bf16 v[116:119], v[44:47], a[56:63], %[idxo]\n" \
    "s_waitcnt vmcnt(5)\n" \
    "v_smfmac_f32_16x16x64_bf16 v[104:107], v[48:51], a[64:71], %[idxo]\n" \
    "v_smfmac_f32_16x16x64_bf16 v[108:111], v[48:51], a[72:79], %[idxo]\n" \
    "v_smfmac_f32_16x16x64_bf16 v[112:115], v[48:51], a[80:87], %[idxo]\n" \
    "v_smfmac_f32_16x16x64_bf16 v[116:119], v[48:51], a[88:95], %[idxo]\n" \
    "s_waitcnt vmcnt(4)\n" \
    "v_smfmac_f32_16x16x64_bf16 v[104:107], v[52:55], a[96:103], %[idxo]\n" \
    "v_smfmac_f32_16x16x64_bf16 v[108:111], v[52:55], a[104:111], %[idxo]\n" \
    "v_smfmac_f32_16x16x64_bf16 v[112:115], v[52:55], a[112:119], %[idxo]\n" \
    "v_smfmac_f32_16x16x64_bf16 v[116:119], v[52:55], a[120:127], %[idxo]\n" \
    "s_waitcnt vmcnt(3)\n" \
    "v_smfmac_f32_16x16x64_bf16 v[104:107], v[56:59], a[128:135], %[idxo]\n" \
    "v_smfmac_f32_16x16x64_bf16 v[108:111], v[56:59], a[136:143], %[idxo]\n" \
    "v_smfmac_f32_16x16x64_bf16 v[112:115], v[56:59], a[144:151], %[idxo]\n" \
    "v_smfmac_f32_16x16x64_bf16 v[116:119], v[56:59], a[152:159], %[idxo]\n" \
    "s_waitcnt vmcnt(2)\n" \
    "v_smfmac_f32_16x16x64_bf16 v[104:107], v[60:63], a[160:167], %[idxo]\n" \
    "v_smfmac_f32_16x16x64_bf16 v[108:111], v[60:63], a[168:175], %[idxo]\n" \
    "v_smfmac_f32_16x16x64_bf16 v[112:115], v[60:63], a[176:183], %[idxo]\n" \
    "v_smfmac_f32_16x16x64_bf16 v[116:119], v[60:63], a[184:191], %[idxo]\n" \
    "s_waitcnt vmcnt(1)\n" \
    "v_smfmac_f32_16x16x64_bf16 v[104:107], v[64:67], a[192:199], %[idxo]\n" \
    "v_smfmac_f32_16x16x64_bf16 v[108:111], v[64:67], a[200:207], %[idxo]\n" \
    "v_smfmac_f32_16x16x64_bf16 v[112:115], v[64:67], a[208:215], %[idxo]\n" \
    "v_smfmac_f32_16x16x64_bf16 v[116:119], v[64:67], a[216:223], %[idxo]\n" \
    "s_waitcnt vmcnt(0)\n" \
    "v_smfmac_f32_16x16x64_bf16 v[104:107], v[68:71], a[224:231], %[idxo]\n" \
    "v_smfmac_f32_16x16x64_bf16 v[108:111], v[68:71], a[232:239], %[idxo]\n" \
    "v_smfmac_f32_16x16x64_bf16 v[112:115], v[68:71], a[240:247], %[idxo]\n" \
    "v_smfmac_f32_16x16x64_bf16 v[116:119], v[68:71], a[248:255], %[idxo]\n" \
    "s_cmp_lg_u32 s57, 0\n" \
    "v_pk_add_f32 v[80:81], v[104:105], v[110:111]\n" \
    "s_nop 0\n" \
    "v_add_f32 v80, v80, v81\n" \
    "ds_write_b32 %[ow], v80\n" \
    "v_add_f32 v82, v112, v113\n" \
    "v_cmp_u_f32 vcc, v80, v80\n" \
    "s_nop 1\n" \
    "v_add_f32 v82, v82, v118\n" \
    "v_add_f32 v82, v82, v119\n" \
    "ds_write_b32 %[ow], v82 offset:64\n" \
    "s_cbranch_vccnz LB_or_%=\n" \
    "LB_ook_%=:\n" \
    "s_waitcnt lgkmcnt(0)\n" \
    "s_barrier\n" \
    "s_cbranch_scc1 LB_nw_%=\n" \
    "ds_read_b64 v[72:73], %[orr] offset:0\n" \
    "ds_read_b64 v[74:75], %[orr] offset:768\n" \
    "ds_read_b64 v[76:77], %[orr] offset:1536\n" \
    "ds_read_b64 v[78:79], %[orr] offset:2304\n" \
    "s_waitcnt lgkmcnt(2)\n" \
    "v_pk_add_f32 v[72:73], v[72:73], v[74:75]\n" \
    "s_waitcnt lgkmcnt(1)\n" \
    "v_pk_add_f32 v[72:73], v[72:73], v[76:77]\n" \
    "s_waitcnt lgkmcnt(0)\n" \
    "v_add_f32 v72, v72, v78\n" \
    "v_add_f32 v73, v73, v79\n" \
    "v_cvt_pk_bf16_f32 v72, v72, v73\n" \
    "buffer_store_dword v72, %[vmo], %[ws], s94 offen sc1\n" \
    "buffer_store_dword v72, v34, %[ws], s94 offen sc1\n" \
    "s_branch LB_tmx_%=\n" \
    "LB_nw_%=:\n" \
    "s_cmp_eq_u32 %[w], 1\n" \
    "s_cbranch_scc1 LB_tm_%=\n" \
    "s_memrealtime s[64:65]\n" \
    "s_waitcnt lgkmcnt(0)\n" \
    "s_lshl_b32 s64, s64, 3\n" \
    "s_sub_u32 s97, s87, s64\n" \
    "s_add_u32 s96, s64, " STR(T_LATE) "\n" \
    "s_cmp_ge_i32 s97, " STR(T_LEAD) "\n" \
    "s_cselect_b32 s96, s87, s96\n" \
    "s_cselect_b32 s64, 0, " STR(T_XS) "\n" \
    "s_cselect_b32 s65, 0, " STR(BUMP) "\n" \
    "s_add_u32 s64, s64, s96\n" \
    "s_min_u32 s96, s96, -2\n" \
    "v_mov_b32 v74, s96\n" \
    "s_add_u32 s97, s64, s44\n" \
    "s_add_u32 s96, s64, s45\n" \
    "s_min_u32 s97, s97, -2\n" \
    "s_min_u32 s96, s96, -2\n" \
    "v_mov_b32 v72, s97\n" \
    "v_mov_b32 v73, s96\n" \
    "buffer_store_dwordx3 v[72:74], %[vmsg], %[ws], s85 offen sc1\n" \
    "s_cmp_gt_u32 s95, 3\n" \
    "s_cselect_b32 s65, s65, 0\n" \
    "s_add_u32 s46, s46, s65\n" \
    "s_branch LB_tmx_%=\n" \
    "LB_done_%=:\n" \
    "s_branch LB_end_%=\n" \
    "LB_hsr_%=:\n" \
    "s_sleep 2\n" \
    "s_add_u32 s98, s98, 1\n" \
    "s_cmp_gt_u32 s98, 131072\n" \
    "s_cbranch_scc1 LB_fail_%=\n" \
    "s_branch LB_hs_%=\n" \
    "LB_redo_%=:\n" \
    "s_waitcnt vmcnt(0) lgkmcnt(0)\n" \
    "s_barrier\n" \
    "s_or_b32 %[cfg], s97, 256\n" \
    "s_branch LB_end_%=\n" \
    "LB_tmc_%=:\n" \
    "s_cmp_lt_i32 s97, " STR(T_LEADB) "\n" \
    "s_cbranch_scc1 LB_rs_%=\n" \
    "s_cmp_gt_u32 s97, 4000\n" \
    "s_cbranch_scc1 LB_tmx_%=\n" \
    "LB_tmd_%=:\n" \
    "s_memrealtime s[92:93]\n" \
    "s_waitcnt lgkmcnt(0)\n" \
    "s_lshl_b32 s97, s92, 3\n" \
    "s_sub_u32 s97, s87, s97\n" \
    "s_cmp_gt_i32 s97, 96\n" \
    "s_cbranch_scc1 LB_tmd_%=\n" \
    "s_branch LB_tm_%=\n" \
    "LB_t2_%=:\n" \
    "s_memrealtime s[72:73]\n" \
    "s_nop 1\n" \
    "s_memrealtime s[74:75]\n" \
    "s_nop 1\n" \
    "s_memrealtime s[76:77]\n" \
    "s_nop 1\n" \
    "s_memrealtime s[78:79]\n" \
    "s_nop 1\n" \
    "s_memrealtime s[80:81]\n" \
    "s_nop 1\n" \
    "s_memrealtime s[82:83]\n" \
    "s_nop 1\n" \
    "s_memrealtime s[92:93]\n" \
    "s_nop 1\n" \
    "s_memrealtime s[70:71]\n" \
    "s_getpc_b64 s[68:69]\n" \
    "LB_t2p_%=:\n" \
    "s_add_u32 s68, s68, LB_t2x_%= - LB_t2p_%=\n" \
    "s_addc_u32 s69, s69, 0\n" \
    "s_waitcnt lgkmcnt(0)\n" \
    "s_sub_u32 s97, s87, s72\n" \
    "s_sub_u32 s97, s97, s74\n" \
    "s_sub_u32 s97, s97, s76\n" \
    "s_sub_u32 s97, s97, s78\n" \
    "s_sub_u32 s97, s97, s80\n" \
    "s_sub_u32 s97, s97, s82\n" \
    "s_sub_u32 s97, s97, s92\n" \
    "s_sub_u32 s97, s97, s70\n" \
    "s_cmp_gt_u32 s97, 160\n" \
    "s_cbranch_scc1 LB_t2c_%=\n" \
    "s_mul_i32 s97, s97, 47\n" \
    "s_lshr_b32 s97, s97, 4\n" \
    "s_and_b32 s97, s97, -4\n" \
    "s_sub_u32 s68, s68, s97\n" \
    "s_subb_u32 s69, s69, 0\n" \
    "s_setpc_b64 s[68:69]\n" \
    ".rept 118\n" \
    "s_nop 0\n" \
    ".endr\n" \
    "LB_t2x_%=:\n" \
    "s_branch LB_tmx_%=\n" \
    "LB_t2c_%=:\n" \
    "s_cmp_gt_u32 s97, 4000\n" \
    "s_cbranch_scc1 LB_t2x_%=\n" \
    "LB_t2d_%=:\n" \
    "s_memrealtime s[92:93]\n" \
    "s_waitcnt lgkmcnt(0)\n" \
    "s_lshl_b32 s97, s92, 3\n" \
    "s_sub_u32 s97, s87, s97\n" \
    "s_cmp_gt_i32 s97, 96\n" \
    "s_cbranch_scc1 LB_t2d_%=\n" \
    "s_branch LB_t2_%=\n" \
    "LB_memr_%=:\n" \
    "v_max3_u32 v120, v40, v41, v41\n" \
    "v_max3_u32 v120, v120, v42, v43\n" \
    "v_max3_u32 v120, v120, v44, v45\n" \
    "v_max3_u32 v120, v120, v46, v47\n" \
    "v_max3_u32 v120, v120, v48, v49\n" \
    "v_max3_u32 v120, v120, v50, v51\n" \
    "v_max3_u32 v120, v120, v52, v53\n" \
    "v_max3_u32 v120, v120, v54, v55\n" \
    "v_max3_u32 v120, v120, v56, v57\n" \
    "v_max3_u32 v120, v120, v58, v59\n" \
    "v_max3_u32 v120, v120, v60, v61\n" \
    "v_max3_u32 v120, v120, v62, v63\n" \
    "v_max3_u32 v120, v120, v64, v65\n" \
    "v_max3_u32 v120, v120, v66, v67\n" \
    "v_max3_u32 v120, v120, v68, v69\n" \
    "v_max3_u32 v120, v120, v70, v71\n" \
    "v_cmp_eq_u32 vcc, -1, v120\n" \
    "s_cbranch_vccz LB_memok_%=\n" \
    "s_waitcnt vmcnt(0) lgkmcnt(0)\n" \
    "s_lshl_b32 m0, %[w], 11\n" \
    "v_mov_b64 v[96:97], 0\n" \
    "v_mov_b64 v[98:99], 0\n" \
    "v_mov_b64 v[100:101], 0\n" \
    "v_mov_b64 v[102:103], 0\n" \
    "s_add_u32 s98, s98, 1\n" \
    "s_cmp_gt_u32 s98, 131072\n" \
    "s_cbranch_scc1 LB_fail_%=\n" \
    "s_cmp_lg_u32 %[w], 1\n" \
    "s_cbranch_scc1 LB_tmx_%=\n" \
    "LB_rs_%=:\n" \
    "s_add_u32 s98, s98, 1\n" \
    "s_cmp_gt_u32 s98, 131072\n" \
    "s_cbranch_scc1 LB_fail_%=\n" \
    "s_lshr_b32 s85, s94, 19\n" \
    "s_lshl_b32 s85, s85, 8\n" \
    "s_add_u32 s85, s85, 2080768\n" \
    "s_add_u32 s85, s85, %[npol]\n" \
    "buffer_load_dwordx3 v[120:122], off, %[ws], s85 sc1\n" \
    "s_waitcnt vmcnt(0)\n" \
    "v_readfirstlane_b32 s86, v120\n" \
    "v_readfirstlane_b32 s87, v121\n" \
    "v_readfirstlane_b32 s96, v122\n" \
    "s_nop 1\n" \
    "s_max_u32 s97, s86, s87\n" \
    "s_max_u32 s97, s97, s96\n" \
    "s_cmp_eq_u32 s97, -1\n" \
    "s_cbranch_scc1 LB_rs_%=\n" \
    "s_add_u32 s87, s96, " STR(RS_B) "\n" \
    "s_branch LB_t2_%=\n" \
    "LB_msgr_%=:\n" \
    "buffer_load_dwordx3 v[88:90], off, %[ws], s85 sc1\n" \
    "s_waitcnt vmcnt(0)\n" \
    "s_add_u32 s98, s98, 1\n" \
    "s_cmp_gt_u32 s98, 131072\n" \
    "s_cbranch_scc1 LB_fail_%=\n" \
    "s_branch LB_msg_%=\n" \
    "LB_toc_%=:\n" \
    "s_cmp_gt_u32 s97, 4000\n" \
    "s_cbranch_scc1 LB_tox_%=\n" \
    "LB_tod_%=:\n" \
    "s_memrealtime s[92:93]\n" \
    "s_waitcnt lgkmcnt(0)\n" \
    "s_lshl_b32 s97, s92, 3\n" \
    "s_sub_u32 s97, s86, s97\n" \
    "s_cmp_gt_i32 s97, 96\n" \
    "s_cbranch_scc1 LB_tod_%=\n" \
    "s_branch LB_to_%=\n" \
    "LB_or_%=:\n" \
    "v_max3_u32 v120, v40, v41, v41\n" \
    "v_max3_u32 v120, v120, v42, v43\n" \
    "v_max3_u32 v120, v120, v44, v45\n" \
    "v_max3_u32 v120, v120, v46, v47\n" \
    "v_max3_u32 v120, v120, v48, v49\n" \
    "v_max3_u32 v120, v120, v50, v51\n" \
    "v_max3_u32 v120, v120, v52, v53\n" \
    "v_max3_u32 v120, v120, v54, v55\n" \
    "v_max3_u32 v120, v120, v56, v57\n" \
    "v_max3_u32 v120, v120, v58, v59\n" \
    "v_max3_u32 v120, v120, v60, v61\n" \
    "v_max3_u32 v120, v120, v62, v63\n" \
    "v_max3_u32 v120, v120, v64, v65\n" \
    "v_max3_u32 v120, v120, v66, v67\n" \
    "v_max3_u32 v120, v120, v68, v69\n" \
    "v_max3_u32 v120, v120, v70, v71\n" \
    "v_cmp_eq_u32 vcc, -1, v120\n" \
    "s_cbranch_vccz LB_ook_%=\n" \
    "s_waitcnt lgkmcnt(0)\n" \
    "v_mov_b64 v[104:105], 0\n" \
    "v_mov_b64 v[106:107], 0\n" \
    "v_mov_b64 v[108:109], 0\n" \
    "v_mov_b64 v[110:111], 0\n" \
    "v_mov_b64 v[112:113], 0\n" \
    "v_mov_b64 v[114:115], 0\n" \
    "v_mov_b64 v[116:117], 0\n" \
    "v_mov_b64 v[118:119], 0\n" \
    "LB_or2_%=:\n" \
    "s_add_u32 s98, s98, 1\n" \
    "s_cmp_gt_u32 s98, 131072\n" \
    "s_cbranch_scc1 LB_fail_%=\n" \
    "buffer_load_dwordx4 v[40:43], v36, %[ws], s60 offen sc1\n" \
    "buffer_load_dwordx4 v[44:47], v37, %[ws], s60 offen sc1\n" \
    "buffer_load_dwordx4 v[48:51], v38, %[ws], s60 offen sc1\n" \
    "buffer_load_dwordx4 v[52:55], v39, %[ws], s60 offen sc1\n" \
    "buffer_load_dwordx4 v[56:59], v36, %[ws], s61 offen sc1\n" \
    "buffer_load_dwordx4 v[60:63], v37, %[ws], s61 offen sc1\n" \
    "buffer_load_dwordx4 v[64:67], v38, %[ws], s61 offen sc1\n" \
    "buffer_load_dwordx4 v[68:71], v39, %[ws], s61 offen sc1\n" \
    "s_waitcnt vmcnt(0)\n" \
    "v_max3_u32 v120, v40, v41, v41\n" \
    "v_max3_u32 v120, v120, v42, v43\n" \
    "v_max3_u32 v120, v120, v44, v45\n" \
    "v_max3_u32 v120, v120, v46, v47\n" \
    "v_max3_u32 v120, v120, v48, v49\n" \
    "v_max3_u32 v120, v120, v50, v51\n" \
    "v_max3_u32 v120, v120, v52, v53\n" \
    "v_max3_u32 v120, v120, v54, v55\n" \
    "v_max3_u32 v120, v120, v56, v57\n" \
    "v_max3_u32 v120, v120, v58, v59\n" \
    "v_max3_u32 v120, v120, v60, v61\n" \
    "v_max3_u32 v120, v120, v62, v63\n" \
    "v_max3_u32 v120, v120, v64, v65\n" \
    "v_max3_u32 v120, v120, v66, v67\n" \
    "v_max3_u32 v120, v120, v68, v69\n" \
    "v_max3_u32 v120, v120, v70, v71\n" \
    "v_cmp_eq_u32 vcc, -1, v120\n" \
    "s_cbranch_vccnz LB_or2_%=\n" \
    "s_branch LB_om_%=\n" \
    "LB_fail_%=:\n" \
    "v_mov_b32 v72, 2043904\n" \
    "v_mov_b32 v73, 1\n" \
    "buffer_store_dword v73, v72, %[ws], 0 offen sc1\n" \
    "s_endpgm\n" \
    "LB_end_%=:\n"
#define ASM_ROLE_A \
    "s_nop 4\n" \
    "s_mov_b32 s98, 0\n" \
    "s_mov_b32 s95, 0\n" \
    "s_add_u32 s97, %[hsa], 32768\n" \
    "buffer_load_dword v31, off, %[ws], s97 sc1\n" \
    "s_add_u32 s97, %[w], 0\n" \
    "s_and_b32 s97, s97, 3\n" \
    "s_sub_u32 s97, s97, 0\n" \
    "s_lshl_b32 s97, s97, 10\n" \
    "v_add_u32 v32, s97, %[vmem]\n" \
    "s_add_u32 s97, %[w], 1\n" \
    "s_and_b32 s97, s97, 3\n" \
    "s_sub_u32 s97, s97, 1\n" \
    "s_lshl_b32 s97, s97, 10\n" \
    "v_add_u32 v33, s97, %[vmem]\n" \
    "s_add_u32 s97, %[w], 2\n" \
    "s_and_b32 s97, s97, 3\n" \
    "s_sub_u32 s97, s97, 2\n" \
    "s_lshl_b32 s97, s97, 10\n" \
    "v_add_u32 v34, s97, %[vmem]\n" \
    "s_add_u32 s97, %[w], 3\n" \
    "s_and_b32 s97, s97, 3\n" \
    "s_sub_u32 s97, s97, 3\n" \
    "s_lshl_b32 s97, s97, 10\n" \
    "v_add_u32 v35, s97, %[vmem]\n" \
    "v_xor_b32 v244, 128, %[vow]\n" \
    "v_mov_b32 v245, %[ra]\n" \
    "v_xor_b32 v246, 32, %[ra]\n" \
    "v_add_u32 v247, 0x10000, v245\n" \
    "v_add_u32 v248, 0x10000, v246\n" \
    "s_lshl_b32 s64, %[w], 13\n" \
    "s_lshl_b32 s65, %[w], 18\n" \
    "s_xor_b32 s97, %[hx], 0\n" \
    "buffer_load_dword v65, %[vh], %[dw], s97 offen\n" \
    "s_xor_b32 s97, %[hx], 262144\n" \
    "buffer_load_dword v65, %[vh], %[dw], s97 offen\n" \
    "s_xor_b32 s97, %[hx], 524288\n" \
    "buffer_load_dword v65, %[vh], %[dw], s97 offen\n" \
    "s_add_u32 m0, s64, 65536\n" \
    "s_add_u32 s97, s65, 0\n" \
    "buffer_load_dwordx4 %[vow], %[wd], s97 offen lds\n" \
    "s_add_u32 m0, s64, 66560\n" \
    "s_add_u32 s97, s65, 65536\n" \
    "buffer_load_dwordx4 v244, %[wd], s97 offen lds\n" \
    "s_add_u32 m0, s64, 67584\n" \
    "s_add_u32 s97, s65, 131072\n" \
    "buffer_load_dwordx4 %[vow], %[wd], s97 offen lds\n" \
    "s_add_u32 m0, s64, 68608\n" \
    "s_add_u32 s97, s65, 196608\n" \
    "buffer_load_dwordx4 v244, %[wd], s97 offen lds\n" \
    "s_add_u32 m0, s64, 69632\n" \
    "s_add_u32 s97, s65, 32768\n" \
    "buffer_load_dwordx4 %[vow], %[wd], s97 offen lds\n" \
    "s_add_u32 m0, s64, 70656\n" \
    "s_add_u32 s97, s65, 98304\n" \
    "buffer_load_dwordx4 v244, %[wd], s97 offen lds\n" \
    "s_add_u32 m0, s64, 71680\n" \
    "s_add_u32 s97, s65, 163840\n" \
    "buffer_load_dwordx4 %[vow], %[wd], s97 offen lds\n" \
    "s_add_u32 m0, s64, 72704\n" \
    "s_add_u32 s97, s65, 229376\n" \
    "buffer_load_dwordx4 v244, %[wd], s97 offen lds\n" \
    "s_add_u32 m0, s64, 98304\n" \
    "s_add_u32 s97, s65, 1048576\n" \
    "buffer_load_dwordx4 %[vow], %[wd], s97 offen lds\n" \
    "s_add_u32 m0, s64, 99328\n" \
    "s_add_u32 s97, s65, 1114112\n" \
    "buffer_load_dwordx4 v244, %[wd], s97 offen lds\n" \
    "s_add_u32 m0, s64, 100352\n" \
    "s_add_u32 s97, s65, 1179648\n" \
    "buffer_load_dwordx4 %[vow], %[wd], s97 offen lds\n" \
    "s_add_u32 m0, s64, 101376\n" \
    "s_add_u32 s97, s65, 1245184\n" \
    "buffer_load_dwordx4 v244, %[wd], s97 offen lds\n" \
    "s_add_u32 m0, s64, 102400\n" \
    "s_add_u32 s97, s65, 1081344\n" \
    "buffer_load_dwordx4 %[vow], %[wd], s97 offen lds\n" \
    "s_add_u32 m0, s64, 103424\n" \
    "s_add_u32 s97, s65, 1146880\n" \
    "buffer_load_dwordx4 v244, %[wd], s97 offen lds\n" \
    "s_add_u32 m0, s64, 104448\n" \
    "s_add_u32 s97, s65, 1212416\n" \
    "buffer_load_dwordx4 %[vow], %[wd], s97 offen lds\n" \
    "s_add_u32 m0, s64, 105472\n" \
    "s_add_u32 s97, s65, 1277952\n" \
    "buffer_load_dwordx4 v244, %[wd], s97 offen lds\n" \
    "s_waitcnt vmcnt(8) lgkmcnt(0)\n" \
    "s_barrier\n" \
    "v_readfirstlane_b32 %[cfg], v31\n" \
    "s_nop 1\n" \
    "s_add_u32 %[cfg], %[cfg], 256\n" \
    "s_and_b32 %[cfg], %[cfg], 0xffffff00\n" \
    "v_mov_b32 v31, %[cfg]\n" \
    "buffer_store_dword v31, off, %[ws], %[hsa] sc1\n" \
    "s_xor_b32 s97, %[hx], 786432\n" \
    "buffer_load_dword v65, %[vh], %[dw], s97 offen\n" \
    "s_add_u32 m0, s64, 131072\n" \
    "s_add_u32 s97, s65, 2097152\n" \
    "buffer_load_dwordx4 %[vow], %[wd], s97 offen lds\n" \
    "s_add_u32 m0, s64, 132096\n" \
    "s_add_u32 s97, s65, 2162688\n" \
    "buffer_load_dwordx4 v244, %[wd], s97 offen lds\n" \
    "s_add_u32 m0, s64, 133120\n" \
    "s_add_u32 s97, s65, 2228224\n" \
    "buffer_load_dwordx4 %[vow], %[wd], s97 offen lds\n" \
    "s_add_u32 m0, s64, 134144\n" \
    "s_add_u32 s97, s65, 2293760\n" \
    "buffer_load_dwordx4 v244, %[wd], s97 offen lds\n" \
    "s_add_u32 m0, s64, 135168\n" \
    "s_add_u32 s97, s65, 2129920\n" \
    "buffer_load_dwordx4 %[vow], %[wd], s97 offen lds\n" \
    "s_add_u32 m0, s64, 136192\n" \
    "s_add_u32 s97, s65, 2195456\n" \
    "buffer_load_dwordx4 v244, %[wd], s97 offen lds\n" \
    "s_add_u32 m0, s64, 137216\n" \
    "s_add_u32 s97, s65, 2260992\n" \
    "buffer_load_dwordx4 %[vow], %[wd], s97 offen lds\n" \
    "s_add_u32 m0, s64, 138240\n" \
    "s_add_u32 s97, s65, 2326528\n" \
    "buffer_load_dwordx4 v244, %[wd], s97 offen lds\n" \
    "ds_read_b64_tr_b16 a[0:1], v245 offset:0\n" \
    "ds_read_b64_tr_b16 a[2:3], v245 offset:4096\n" \
    "ds_read_b64_tr_b16 a[8:9], v246 offset:0\n" \
    "ds_read_b64_tr_b16 a[10:11], v246 offset:4096\n" \
    "ds_read_b64_tr_b16 a[4:5], v245 offset:8192\n" \
    "ds_read_b64_tr_b16 a[6:7], v245 offset:12288\n" \
    "ds_read_b64_tr_b16 a[12:13], v246 offset:8192\n" \
    "ds_read_b64_tr_b16 a[14:15], v246 offset:12288\n" \
    "ds_read_b64_tr_b16 a[16:17], v245 offset:16384\n" \
    "ds_read_b64_tr_b16 a[18:19], v245 offset:20480\n" \
    "ds_read_b64_tr_b16 a[24:25], v246 offset:16384\n" \
    "ds_read_b64_tr_b16 a[26:27], v246 offset:20480\n" \
    "ds_read_b64_tr_b16 a[20:21], v245 offset:24576\n" \
    "ds_read_b64_tr_b16 a[22:23], v245 offset:28672\n" \
    "ds_read_b64_tr_b16 a[28:29], v246 offset:24576\n" \
    "ds_read_b64_tr_b16 a[30:31], v246 offset:28672\n" \
    "s_waitcnt vmcnt(8) lgkmcnt(0)\n" \
    "s_barrier\n" \
    "s_xor_b32 s97, %[hx], 1048576\n" \
    "buffer_load_dword v65, %[vh], %[dw], s97 offen\n" \
    "s_add_u32 m0, s64, 65536\n" \
    "s_add_u32 s97, s65, 3145728\n" \
    "buffer_load_dwordx4 %[vow], %[wd], s97 offen lds\n" \
    "s_add_u32 m0, s64, 66560\n" \
    "s_add_u32 s97, s65, 3211264\n" \
    "buffer_load_dwordx4 v244, %[wd], s97 offen lds\n" \
    "s_add_u32 m0, s64, 67584\n" \
    "s_add_u32 s97, s65, 3276800\n" \
    "buffer_load_dwordx4 %[vow], %[wd], s97 offen lds\n" \
    "s_add_u32 m0, s64, 68608\n" \
    "s_add_u32 s97, s65, 3342336\n" \
    "buffer_load_dwordx4 v244, %[wd], s97 offen lds\n" \
    "s_add_u32 m0, s64, 69632\n" \
    "s_add_u32 s97, s65, 3178496\n" \
    "buffer_load_dwordx4 %[vow], %[wd], s97 offen lds\n" \
    "s_add_u32 m0, s64, 70656\n" \
    "s_add_u32 s97, s65, 3244032\n" \
    "buffer_load_dwordx4 v244, %[wd], s97 offen lds\n" \
    "s_add_u32 m0, s64, 71680\n" \
    "s_add_u32 s97, s65, 3309568\n" \
    "buffer_load_dwordx4 %[vow], %[wd], s97 offen lds\n" \
    "s_add_u32 m0, s64, 72704\n" \
    "s_add_u32 s97, s65, 3375104\n" \
    "buffer_load_dwordx4 v244, %[wd], s97 offen lds\n" \
    "ds_read_b64_tr_b16 a[32:33], v245 offset:32768\n" \
    "ds_read_b64_tr_b16 a[34:35], v245 offset:36864\n" \
    "ds_read_b64_tr_b16 a[40:41], v246 offset:32768\n" \
    "ds_read_b64_tr_b16 a[42:43], v246 offset:36864\n" \
    "ds_read_b64_tr_b16 a[36:37], v245 offset:40960\n" \
    "ds_read_b64_tr_b16 a[38:39], v245 offset:45056\n" \
    "ds_read_b64_tr_b16 a[44:45], v246 offset:40960\n" \
    "ds_read_b64_tr_b16 a[46:47], v246 offset:45056\n" \
    "ds_read_b64_tr_b16 a[48:49], v245 offset:49152\n" \
    "ds_read_b64_tr_b16 a[50:51], v245 offset:53248\n" \
    "ds_read_b64_tr_b16 a[56:57], v246 offset:49152\n" \
    "ds_read_b64_tr_b16 a[58:59], v246 offset:53248\n" \
    "ds_read_b64_tr_b16 a[52:53], v245 offset:57344\n" \
    "ds_read_b64_tr_b16 a[54:55], v245 offset:61440\n" \
    "ds_read_b64_tr_b16 a[60:61], v246 offset:57344\n" \
    "ds_read_b64_tr_b16 a[62:63], v246 offset:61440\n" \
    "s_waitcnt vmcnt(8) lgkmcnt(0)\n" \
    "s_barrier\n" \
    "s_xor_b32 s97, %[hx], 1310720\n" \
    "buffer_load_dword v65, %[vh], %[dw], s97 offen\n" \
    "s_add_u32 m0, s64, 98304\n" \
    "s_add_u32 s97, s65, 4194304\n" \
    "buffer_load_dwordx4 %[vow], %[wd], s97 offen lds\n" \
    "s_add_u32 m0, s64, 99328\n" \
    "s_add_u32 s97, s65, 4259840\n" \
    "buffer_load_dwordx4 v244, %[wd], s97 offen lds\n" \
    "s_add_u32 m0, s64, 100352\n" \
    "s_add_u32 s97, s65, 4325376\n" \
    "buffer_load_dwordx4 %[vow], %[wd], s97 offen lds\n" \
    "s_add_u32 m0, s64, 101376\n" \
    "s_add_u32 s97, s65, 4390912\n" \
    "buffer_load_dwordx4 v244, %[wd], s97 offen lds\n" \
    "s_add_u32 m0, s64, 102400\n" \
    "s_add_u32 s97, s65, 4227072\n" \
    "buffer_load_dwordx4 %[vow], %[wd], s97 offen lds\n" \
    "s_add_u32 m0, s64, 103424\n" \
    "s_add_u32 s97, s65, 4292608\n" \
    "buffer_load_dwordx4 v244, %[wd], s97 offen lds\n" \
    "s_add_u32 m0, s64, 104448\n" \
    "s_add_u32 s97, s65, 4358144\n" \
    "buffer_load_dwordx4 %[vow], %[wd], s97 offen lds\n" \
    "s_add_u32 m0, s64, 105472\n" \
    "s_add_u32 s97, s65, 4423680\n" \
    "buffer_load_dwordx4 v244, %[wd], s97 offen lds\n" \
    "ds_read_b64_tr_b16 a[64:65], v247 offset:0\n" \
    "ds_read_b64_tr_b16 a[66:67], v247 offset:4096\n" \
    "ds_read_b64_tr_b16 a[72:73], v248 offset:0\n" \
    "ds_read_b64_tr_b16 a[74:75], v248 offset:4096\n" \
    "ds_read_b64_tr_b16 a[68:69], v247 offset:8192\n" \
    "ds_read_b64_tr_b16 a[70:71], v247 offset:12288\n" \
    "ds_read_b64_tr_b16 a[76:77], v248 offset:8192\n" \
    "ds_read_b64_tr_b16 a[78:79], v248 offset:12288\n" \
    "ds_read_b64_tr_b16 a[80:81], v247 offset:16384\n" \
    "ds_read_b64_tr_b16 a[82:83], v247 offset:20480\n" \
    "ds_read_b64_tr_b16 a[88:89], v248 offset:16384\n" \
    "ds_read_b64_tr_b16 a[90:91], v248 offset:20480\n" \
    "ds_read_b64_tr_b16 a[84:85], v247 offset:24576\n" \
    "ds_read_b64_tr_b16 a[86:87], v247 offset:28672\n" \
    "ds_read_b64_tr_b16 a[92:93], v248 offset:24576\n" \
    "ds_read_b64_tr_b16 a[94:95], v248 offset:28672\n" \
    "s_waitcnt vmcnt(8) lgkmcnt(0)\n" \
    "s_barrier\n" \
    "s_xor_b32 s97, %[hx], 1572864\n" \
    "buffer_load_dword v65, %[vh], %[dw], s97 offen\n" \
    "s_add_u32 m0, s64, 131072\n" \
    "s_add_u32 s97, s65, 5242880\n" \
    "buffer_load_dwordx4 %[vow], %[wd], s97 offen lds\n" \
    "s_add_u32 m0, s64, 132096\n" \
    "s_add_u32 s97, s65, 5308416\n" \
    "buffer_load_dwordx4 v244, %[wd], s97 offen lds\n" \
    "s_add_u32 m0, s64, 133120\n" \
    "s_add_u32 s97, s65, 5373952\n" \
    "buffer_load_dwordx4 %[vow], %[wd], s97 offen lds\n" \
    "s_add_u32 m0, s64, 134144\n" \
    "s_add_u32 s97, s65, 5439488\n" \
    "buffer_load_dwordx4 v244, %[wd], s97 offen lds\n" \
    "s_add_u32 m0, s64, 135168\n" \
    "s_add_u32 s97, s65, 5275648\n" \
    "buffer_load_dwordx4 %[vow], %[wd], s97 offen lds\n" \
    "s_add_u32 m0, s64, 136192\n" \
    "s_add_u32 s97, s65, 5341184\n" \
    "buffer_load_dwordx4 v244, %[wd], s97 offen lds\n" \
    "s_add_u32 m0, s64, 137216\n" \
    "s_add_u32 s97, s65, 5406720\n" \
    "buffer_load_dwordx4 %[vow], %[wd], s97 offen lds\n" \
    "s_add_u32 m0, s64, 138240\n" \
    "s_add_u32 s97, s65, 5472256\n" \
    "buffer_load_dwordx4 v244, %[wd], s97 offen lds\n" \
    "ds_read_b64_tr_b16 a[96:97], v245 offset:0\n" \
    "ds_read_b64_tr_b16 a[98:99], v245 offset:4096\n" \
    "ds_read_b64_tr_b16 a[104:105], v246 offset:0\n" \
    "ds_read_b64_tr_b16 a[106:107], v246 offset:4096\n" \
    "ds_read_b64_tr_b16 a[100:101], v245 offset:8192\n" \
    "ds_read_b64_tr_b16 a[102:103], v245 offset:12288\n" \
    "ds_read_b64_tr_b16 a[108:109], v246 offset:8192\n" \
    "ds_read_b64_tr_b16 a[110:111], v246 offset:12288\n" \
    "ds_read_b64_tr_b16 a[112:113], v245 offset:16384\n" \
    "ds_read_b64_tr_b16 a[114:115], v245 offset:20480\n" \
    "ds_read_b64_tr_b16 a[120:121], v246 offset:16384\n" \
    "ds_read_b64_tr_b16 a[122:123], v246 offset:20480\n" \
    "ds_read_b64_tr_b16 a[116:117], v245 offset:24576\n" \
    "ds_read_b64_tr_b16 a[118:119], v245 offset:28672\n" \
    "ds_read_b64_tr_b16 a[124:125], v246 offset:24576\n" \
    "ds_read_b64_tr_b16 a[126:127], v246 offset:28672\n" \
    "s_waitcnt vmcnt(8) lgkmcnt(0)\n" \
    "s_barrier\n" \
    "s_xor_b32 s97, %[hx], 1835008\n" \
    "buffer_load_dword v65, %[vh], %[dw], s97 offen\n" \
    "s_add_u32 m0, s64, 65536\n" \
    "s_add_u32 s97, s65, 6291456\n" \
    "buffer_load_dwordx4 %[vow], %[wd], s97 offen lds\n" \
    "s_add_u32 m0, s64, 66560\n" \
    "s_add_u32 s97, s65, 6356992\n" \
    "buffer_load_dwordx4 v244, %[wd], s97 offen lds\n" \
    "s_add_u32 m0, s64, 67584\n" \
    "s_add_u32 s97, s65, 6422528\n" \
    "buffer_load_dwordx4 %[vow], %[wd], s97 offen lds\n" \
    "s_add_u32 m0, s64, 68608\n" \
    "s_add_u32 s97, s65, 6488064\n" \
    "buffer_load_dwordx4 v244, %[wd], s97 offen lds\n" \
    "s_add_u32 m0, s64, 69632\n" \
    "s_add_u32 s97, s65, 6324224\n" \
    "buffer_load_dwordx4 %[vow], %[wd], s97 offen lds\n" \
    "s_add_u32 m0, s64, 70656\n" \
    "s_add_u32 s97, s65, 6389760\n" \
    "buffer_load_dwordx4 v244, %[wd], s97 offen lds\n" \
    "s_add_u32 m0, s64, 71680\n" \
    "s_add_u32 s97, s65, 6455296\n" \
    "buffer_load_dwordx4 %[vow], %[wd], s97 offen lds\n" \
    "s_add_u32 m0, s64, 72704\n" \
    "s_add_u32 s97, s65, 6520832\n" \
    "buffer_load_dwordx4 v244, %[wd], s97 offen lds\n" \
    "ds_read_b64_tr_b16 a[128:129], v245 offset:32768\n" \
    "ds_read_b64_tr_b16 a[130:131], v245 offset:36864\n" \
    "ds_read_b64_tr_b16 a[136:137], v246 offset:32768\n" \
    "ds_read_b64_tr_b16 a[138:139], v246 offset:36864\n" \
    "ds_read_b64_tr_b16 a[132:133], v245 offset:40960\n" \
    "ds_read_b64_tr_b16 a[134:135], v245 offset:45056\n" \
    "ds_read_b64_tr_b16 a[140:141], v246 offset:40960\n" \
    "ds_read_b64_tr_b16 a[142:143], v246 offset:45056\n" \
    "ds_read_b64_tr_b16 a[144:145], v245 offset:49152\n" \
    "ds_read_b64_tr_b16 a[146:147], v245 offset:53248\n" \
    "ds_read_b64_tr_b16 a[152:153], v246 offset:49152\n" \
    "ds_read_b64_tr_b16 a[154:155], v246 offset:53248\n" \
    "ds_read_b64_tr_b16 a[148:149], v245 offset:57344\n" \
    "ds_read_b64_tr_b16 a[150:151], v245 offset:61440\n" \
    "ds_read_b64_tr_b16 a[156:157], v246 offset:57344\n" \
    "ds_read_b64_tr_b16 a[158:159], v246 offset:61440\n" \
    "s_waitcnt vmcnt(8) lgkmcnt(0)\n" \
    "s_barrier\n" \
    "s_add_u32 m0, s64, 98304\n" \
    "s_add_u32 s97, s65, 7340032\n" \
    "buffer_load_dwordx4 %[vow], %[wd], s97 offen lds\n" \
    "s_add_u32 m0, s64, 99328\n" \
    "s_add_u32 s97, s65, 7405568\n" \
    "buffer_load_dwordx4 v244, %[wd], s97 offen lds\n" \
    "s_add_u32 m0, s64, 100352\n" \
    "s_add_u32 s97, s65, 7471104\n" \
    "buffer_load_dwordx4 %[vow], %[wd], s97 offen lds\n" \
    "s_add_u32 m0, s64, 101376\n" \
    "s_add_u32 s97, s65, 7536640\n" \
    "buffer_load_dwordx4 v244, %[wd], s97 offen lds\n" \
    "s_add_u32 m0, s64, 102400\n" \
    "s_add_u32 s97, s65, 7372800\n" \
    "buffer_load_dwordx4 %[vow], %[wd], s97 offen lds\n" \
    "s_add_u32 m0, s64, 103424\n" \
    "s_add_u32 s97, s65, 7438336\n" \
    "buffer_load_dwordx4 v244, %[wd], s97 offen lds\n" \
    "s_add_u32 m0, s64, 104448\n" \
    "s_add_u32 s97, s65, 7503872\n" \
    "buffer_load_dwordx4 %[vow], %[wd], s97 offen lds\n" \
    "s_add_u32 m0, s64, 105472\n" \
    "s_add_u32 s97, s65, 7569408\n" \
    "buffer_load_dwordx4 v244, %[wd], s97 offen lds\n" \
    "ds_read_b64_tr_b16 a[160:161], v247 offset:0\n" \
    "ds_read_b64_tr_b16 a[162:163], v247 offset:4096\n" \
    "ds_read_b64_tr_b16 a[168:169], v248 offset:0\n" \
    "ds_read_b64_tr_b16 a[170:171], v248 offset:4096\n" \
    "ds_read_b64_tr_b16 a[164:165], v247 offset:8192\n" \
    "ds_read_b64_tr_b16 a[166:167], v247 offset:12288\n" \
    "ds_read_b64_tr_b16 a[172:173], v248 offset:8192\n" \
    "ds_read_b64_tr_b16 a[174:175], v248 offset:12288\n" \
    "ds_read_b64_tr_b16 a[176:177], v247 offset:16384\n" \
    "ds_read_b64_tr_b16 a[178:179], v247 offset:20480\n" \
    "ds_read_b64_tr_b16 a[184:185], v248 offset:16384\n" \
    "ds_read_b64_tr_b16 a[186:187], v248 offset:20480\n" \
    "ds_read_b64_tr_b16 a[180:181], v247 offset:24576\n" \
    "ds_read_b64_tr_b16 a[182:183], v247 offset:28672\n" \
    "ds_read_b64_tr_b16 a[188:189], v248 offset:24576\n" \
    "ds_read_b64_tr_b16 a[190:191], v248 offset:28672\n" \
    "s_waitcnt vmcnt(8) lgkmcnt(0)\n" \
    "s_barrier\n" \
    "s_add_u32 s97, %[hsa], 32768\n" \
    "buffer_load_dword v31, off, %[ws], s97 sc1\n" \
    "ds_read_b64_tr_b16 a[192:193], v245 offset:0\n" \
    "ds_read_b64_tr_b16 a[194:195], v245 offset:4096\n" \
    "ds_read_b64_tr_b16 a[200:201], v246 offset:0\n" \
    "ds_read_b64_tr_b16 a[202:203], v246 offset:4096\n" \
    "ds_read_b64_tr_b16 a[196:197], v245 offset:8192\n" \
    "ds_read_b64_tr_b16 a[198:199], v245 offset:12288\n" \
    "ds_read_b64_tr_b16 a[204:205], v246 offset:8192\n" \
    "ds_read_b64_tr_b16 a[206:207], v246 offset:12288\n" \
    "ds_read_b64_tr_b16 a[208:209], v245 offset:16384\n" \
    "ds_read_b64_tr_b16 a[210:211], v245 offset:20480\n" \
    "ds_read_b64_tr_b16 a[216:217], v246 offset:16384\n" \
    "ds_read_b64_tr_b16 a[218:219], v246 offset:20480\n" \
    "ds_read_b64_tr_b16 a[212:213], v245 offset:24576\n" \
    "ds_read_b64_tr_b16 a[214:215], v245 offset:28672\n" \
    "ds_read_b64_tr_b16 a[220:221], v246 offset:24576\n" \
    "ds_read_b64_tr_b16 a[222:223], v246 offset:28672\n" \
    "s_waitcnt vmcnt(0) lgkmcnt(0)\n" \
    "s_barrier\n" \
    "ds_read_b64_tr_b16 a[224:225], v245 offset:32768\n" \
    "ds_read_b64_tr_b16 a[226:227], v245 offset:36864\n" \
    "ds_read_b64_tr_b16 a[232:233], v246 offset:32768\n" \
    "ds_read_b64_tr_b16 a[234:235], v246 offset:36864\n" \
    "ds_read_b64_tr_b16 a[228:229], v245 offset:40960\n" \
    "ds_read_b64_tr_b16 a[230:231], v245 offset:45056\n" \
    "ds_read_b64_tr_b16 a[236:237], v246 offset:40960\n" \
    "ds_read_b64_tr_b16 a[238:239], v246 offset:45056\n" \
    "ds_read_b64_tr_b16 a[240:241], v245 offset:49152\n" \
    "ds_read_b64_tr_b16 a[242:243], v245 offset:53248\n" \
    "ds_read_b64_tr_b16 a[248:249], v246 offset:49152\n" \
    "ds_read_b64_tr_b16 a[250:251], v246 offset:53248\n" \
    "ds_read_b64_tr_b16 a[244:245], v245 offset:57344\n" \
    "ds_read_b64_tr_b16 a[246:247], v245 offset:61440\n" \
    "ds_read_b64_tr_b16 a[252:253], v246 offset:57344\n" \
    "ds_read_b64_tr_b16 a[254:255], v246 offset:61440\n" \
    "s_waitcnt lgkmcnt(0)\n" \
    "s_branch LA_hsl_%=\n" \
    "LA_hs_%=:\n" \
    "s_add_u32 s97, %[hsa], 32768\n" \
    "buffer_load_dword v31, off, %[ws], s97 sc1\n" \
    "s_waitcnt vmcnt(0)\n" \
    "LA_hsl_%=:\n" \
    "v_readfirstlane_b32 s97, v31\n" \
    "s_nop 1\n" \
    "s_xor_b32 s97, s97, %[cfg]\n" \
    "s_cmp_gt_u32 s97, 255\n" \
    "s_cbranch_scc1 LA_hsr_%=\n" \
    "s_and_b32 s96, s97, 3\n" \
    "s_cmp_lg_u32 s96, %[est]\n" \
    "s_cbranch_scc1 LA_redo_%=\n" \
    "s_bitcmp0_b32 s97, 3\n" \
    "s_cbranch_scc1 LA_hsr_%=\n" \
    "s_mov_b32 %[cfg], s97\n" \
    "LA_hsok_%=:\n" \
    "s_and_b32 %[pol], %[cfg], 4\n" \
    "s_lshl_b32 %[pol], %[pol], 11\n" \
    "s_xor_b32 %[npol], %[pol], 8192\n" \
    "s_waitcnt vmcnt(0)\n" \
    "v_mov_b32 v31, 0\n" \
    "s_add_u32 s97, %[hsa], 69632\n" \
    "buffer_store_dword v31, off, %[ws], s97 sc1\n" \
    "s_mov_b32 s94, %[pol]\n" \
    "s_add_u32 s99, s94, 524288\n" \
    "s_mov_b32 s84, s94\n" \
    "v_mov_b32 v232, -1\n" \
    "v_mov_b32 v212, 0x3f803f80\n" \
    "v_mov_b32 v233, -1\n" \
    "v_mov_b32 v213, 0x3f803f80\n" \
    "v_mov_b32 v234, -1\n" \
    "v_mov_b32 v214, 0x3f803f80\n" \
    "v_mov_b32 v235, -1\n" \
    "v_mov_b32 v215, 0x3f803f80\n" \
    "v_mov_b32 v237, 0\n" \
    "v_mov_b32 v238, 0\n" \
    "v_mov_b32 v243, 0xff800000\n" \
    "v_xor_b32 v249, 512, %[vva]\n" \
    "v_mov_b32 v250, %[vox2]\n" \
    "v_xor_b32 v251, 2048, %[xva]\n" \
    "v_cmp_le_i32 s[40:41], 0, %[lim]\n" \
    "v_cmp_le_i32 s[42:43], 1, %[lim]\n" \
    "v_cmp_le_i32 s[44:45], 2, %[lim]\n" \
    "v_cmp_le_i32 s[46:47], 3, %[lim]\n" \
    "v_cmp_le_i32 s[48:49], 16, %[lim]\n" \
    "v_cmp_le_i32 s[50:51], 17, %[lim]\n" \
    "v_cmp_le_i32 s[52:53], 18, %[lim]\n" \
    "v_cmp_le_i32 s[54:55], 19, %[lim]\n" \
    "s_mov_b32 s56, 0x00ff00ff\n" \
    "s_mov_b32 s57, 0x00ff00ff\n" \
    "s_lshl_b32 s65, %[w], 12\n" \
    "s_lshl_b32 s66, %[w], 11\n" \
    "s_add_u32 s66, s66, 20480\n" \
    "s_lshl_b32 s61, %[w], 10\n" \
    "s_add_u32 s61, s61, 45056\n" \
    "v_mov_b32 v37, %[kr]\n" \
    "v_and_b32 v36, 32, %[lr]\n" \
    "v_lshl_add_u32 v36, v36, 4, %[qr]\n" \
    "buffer_store_dwordx4 v[232:235], %[vo], %[ws], s94 offen sc1\n" \
    "buffer_store_dwordx4 v[232:235], %[vo], %[ws], s99 offen sc1\n" \
    "s_mov_b32 s64, 0\n" \
    "v_mov_b32 v38, -1\n" \
    "s_xor_b32 s63, %[npol], 8192\n" \
    "v_cmp_gt_i32 vcc, s64, v38\n" \
    "s_mul_i32 s62, s64, 0x202000\n" \
    "s_and_b32 s62, s62, 0xffe02000\n" \
    "s_xor_b32 s62, s62, %[npol]\n" \
    "s_mov_b32 m0, s66\n" \
    "s_cbranch_vccnz LA_f0r_%=\n" \
    "LA_f0_%=:\n" \
    "buffer_load_dwordx4 %[vx], %[ws], s62 offen sc1 lds\n" \
    "buffer_load_dwordx4 %[vx], %[ws], s62 offen offset:1024 sc1 lds\n" \
    "s_add_u32 m0, s66, 8192\n" \
    "s_add_u32 s62, s62, 16384\n" \
    "buffer_load_dwordx4 %[vx], %[ws], s62 offen sc1 lds\n" \
    "buffer_load_dwordx4 %[vx], %[ws], s62 offen offset:1024 sc1 lds\n" \
    "s_add_u32 m0, s66, 16384\n" \
    "s_add_u32 s62, s62, 16384\n" \
    "buffer_load_dwordx4 %[vx], %[ws], s62 offen sc1 lds\n" \
    "buffer_load_dwordx4 %[vx], %[ws], s62 offen offset:1024 sc1 lds\n" \
    "s_mov_b32 m0, s65\n" \
    "v_mov_b64 v[120:121], 0\n" \
    "v_mov_b64 v[122:123], 0\n" \
    "v_mov_b64 v[124:125], 0\n" \
    "v_mov_b64 v[126:127], 0\n" \
    "s_waitcnt vmcnt(0)\n" \
    "s_barrier\n" \
    "s_add_u32 s97, %[rdy], %[npol]\n" \
    "v_mov_b32 v31, 0\n" \
    "s_cmp_lg_u32 %[w], 0\n" \
    "s_cbranch_scc1 LA_en_%=\n" \
    "buffer_store_dword v31, off, %[ws], s97 sc1\n" \
    "s_branch LA_rs_%=\n" \
    "LA_en_%=:\n" \
    "s_branch LA_tmx_%=\n" \
    "LA_top_%=:\n" \
    "s_cmp_lg_u32 %[w], 0\n" \
    "s_cbranch_scc1 LA_tmx_%=\n" \
    "LA_tm_%=:\n" \
    "s_memrealtime s[72:73]\n" \
    "s_nop 1\n" \
    "s_memrealtime s[74:75]\n" \
    "s_nop 1\n" \
    "s_memrealtime s[76:77]\n" \
    "s_nop 1\n" \
    "s_memrealtime s[78:79]\n" \
    "s_nop 1\n" \
    "s_memrealtime s[80:81]\n" \
    "s_nop 1\n" \
    "s_memrealtime s[82:83]\n" \
    "s_nop 1\n" \
    "s_memrealtime s[92:93]\n" \
    "s_nop 1\n" \
    "s_memrealtime s[70:71]\n" \
    "s_getpc_b64 s[68:69]\n" \
    "LA_tmp_%=:\n" \
    "s_add_u32 s68, s68, LA_tmx_%= - LA_tmp_%=\n" \
    "s_addc_u32 s69, s69, 0\n" \
    "s_waitcnt lgkmcnt(0)\n" \
    "s_sub_u32 s97, s87, s72\n" \
    "s_sub_u32 s97, s97, s74\n" \
    "s_sub_u32 s97, s97, s76\n" \
    "s_sub_u32 s97, s97, s78\n" \
    "s_sub_u32 s97, s97, s80\n" \
    "s_sub_u32 s97, s97, s82\n" \
    "s_sub_u32 s97, s97, s92\n" \
    "s_sub_u32 s97, s97, s70\n" \
    "s_cmp_gt_u32 s97, 160\n" \
    "s_cbranch_scc1 LA_tmc_%=\n" \
    "s_mul_i32 s97, s97, 47\n" \
    "s_lshr_b32 s97, s97, 4\n" \
    "s_and_b32 s97, s97, -4\n" \
    "s_sub_u32 s68, s68, s97\n" \
    "s_subb_u32 s69, s69, 0\n" \
    "s_setpc_b64 s[68:69]\n" \
    ".rept 118\n" \
    "s_nop 0\n" \
    ".endr\n" \
    "LA_tmx_%=:\n" \
    "s_barrier\n" \
    "buffer_load_dwordx4 v32, %[ws], s94 offen sc1 lds\n" \
    "s_xor_b32 s99, s94, 1048576\n" \
    "s_nop 15\n" \
    "s_nop 5\n" \
    "buffer_load_dwordx4 v33, %[ws], s94 offen offset:1024 sc1 lds\n" \
    "s_waitcnt vmcnt(2)\n" \
    "s_barrier\n" \
    "ds_read_b128 v[144:147], %[lr] offset:28672\n" \
    "ds_read_b128 v[148:151], %[lr] offset:29696\n" \
    "ds_read_b128 v[152:155], %[lr] offset:30720\n" \
    "ds_read_b128 v[156:159], %[lr] offset:31744\n" \
    "ds_read_b128 v[160:163], %[lr] offset:32768\n" \
    "ds_read_b128 v[164:167], %[lr] offset:33792\n" \
    "ds_read_b128 v[168:171], %[lr] offset:34816\n" \
    "ds_read_b128 v[172:175], %[lr] offset:35840\n" \
    "buffer_load_dwordx4 v34, %[ws], s94 offen offset:2048 sc1 lds\n" \
    "ds_read_b128 v[128:131], %[xq] offset:20480\n" \
    "ds_read_b128 v[132:135], %[xq] offset:21504\n" \
    "ds_read_b128 v[136:139], %[xq] offset:22528\n" \
    "ds_read_b128 v[140:143], %[xq] offset:23552\n" \
    "ds_read_b128 v[176:179], %[xva] offset:36864\n" \
    "ds_read_b128 v[184:187], v251 offset:36864\n" \
    "ds_read_b128 v[180:183], %[xva] offset:37888\n" \
    "ds_read_b128 v[188:191], v251 offset:37888\n" \
    "buffer_load_dwordx4 v35, %[ws], s94 offen offset:3072 sc1 lds\n" \
    "s_waitcnt vmcnt(3)\n" \
    "s_barrier\n" \
    "ds_read_b128 v[56:59], %[lr] offset:0\n" \
    "ds_read_b128 v[60:63], %[lr] offset:4096\n" \
    "ds_read_b128 v[64:67], %[lr] offset:8192\n" \
    "ds_read_b128 v[68:71], %[lr] offset:12288\n" \
    "s_waitcnt lgkmcnt(3)\n" \
    "v_smfmac_f32_16x16x64_bf16 v[120:123], v[56:59], a[0:7], %[idx]\n" \
    "v_smfmac_f32_16x16x64_bf16 v[124:127], v[56:59], a[8:15], %[idx]\n" \
    "s_waitcnt lgkmcnt(2)\n" \
    "v_smfmac_f32_16x16x64_bf16 v[120:123], v[60:63], a[64:71], %[idx]\n" \
    "s_waitcnt vmcnt(2)\n" \
    "s_barrier\n" \
    "v_smfmac_f32_16x16x64_bf16 v[124:127], v[60:63], a[72:79], %[idx]\n" \
    "ds_read_b128 v[72:75], %[lr] offset:1024\n" \
    "ds_read_b128 v[76:79], %[lr] offset:5120\n" \
    "s_waitcnt lgkmcnt(3)\n" \
    "v_smfmac_f32_16x16x64_bf16 v[120:123], v[64:67], a[128:135], %[idx]\n" \
    "ds_read_b128 v[80:83], %[lr] offset:9216\n" \
    "ds_read_b128 v[84:87], %[lr] offset:13312\n" \
    "v_smfmac_f32_16x16x64_bf16 v[124:127], v[64:67], a[136:143], %[idx]\n" \
    "s_waitcnt lgkmcnt(4)\n" \
    "v_smfmac_f32_16x16x64_bf16 v[120:123], v[68:71], a[192:199], %[idx]\n" \
    "v_smfmac_f32_16x16x64_bf16 v[124:127], v[68:71], a[200:207], %[idx]\n" \
    "s_waitcnt lgkmcnt(3)\n" \
    "v_smfmac_f32_16x16x64_bf16 v[120:123], v[72:75], a[16:23], %[idx]\n" \
    "v_smfmac_f32_16x16x64_bf16 v[124:127], v[72:75], a[24:31], %[idx]\n" \
    "s_waitcnt lgkmcnt(2)\n" \
    "v_smfmac_f32_16x16x64_bf16 v[120:123], v[76:79], a[80:87], %[idx]\n" \
    "s_waitcnt vmcnt(1)\n" \
    "s_barrier\n" \
    "v_smfmac_f32_16x16x64_bf16 v[124:127], v[76:79], a[88:95], %[idx]\n" \
    "ds_read_b128 v[88:91], %[lr] offset:2048\n" \
    "ds_read_b128 v[92:95], %[lr] offset:6144\n" \
    "s_waitcnt lgkmcnt(3)\n" \
    "v_smfmac_f32_16x16x64_bf16 v[120:123], v[80:83], a[144:151], %[idx]\n" \
    "ds_read_b128 v[96:99], %[lr] offset:10240\n" \
    "ds_read_b128 v[100:103], %[lr] offset:14336\n" \
    "v_smfmac_f32_16x16x64_bf16 v[124:127], v[80:83], a[152:159], %[idx]\n" \
    "s_waitcnt lgkmcnt(4)\n" \
    "v_smfmac_f32_16x16x64_bf16 v[120:123], v[84:87], a[208:215], %[idx]\n" \
    "v_smfmac_f32_16x16x64_bf16 v[124:127], v[84:87], a[216:223], %[idx]\n" \
    "s_waitcnt lgkmcnt(3)\n" \
    "v_smfmac_f32_16x16x64_bf16 v[120:123], v[88:91], a[32:39], %[idx]\n" \
    "v_smfmac_f32_16x16x64_bf16 v[124:127], v[88:91], a[40:47], %[idx]\n" \
    "s_waitcnt lgkmcnt(2)\n" \
    "v_smfmac_f32_16x16x64_bf16 v[120:123], v[92:95], a[96:103], %[idx]\n" \
    "s_waitcnt vmcnt(0)\n" \
    "s_barrier\n" \
    "v_smfmac_f32_16x16x64_bf16 v[124:127], v[92:95], a[104:111], %[idx]\n" \
    "ds_read_b128 v[104:107], %[lr] offset:3072\n" \
    "ds_read_b128 v[108:111], %[lr] offset:7168\n" \
    "s_waitcnt lgkmcnt(3)\n" \
    "v_smfmac_f32_16x16x64_bf16 v[120:123], v[96:99], a[160:167], %[idx]\n" \
    "ds_read_b128 v[112:115], %[lr] offset:11264\n" \
    "ds_read_b128 v[116:119], %[lr] offset:15360\n" \
    "v_smfmac_f32_16x16x64_bf16 v[124:127], v[96:99], a[168:175], %[idx]\n" \
    "s_waitcnt lgkmcnt(4)\n" \
    "v_smfmac_f32_16x16x64_bf16 v[120:123], v[100:103], a[224:231], %[idx]\n" \
    "v_smfmac_f32_16x16x64_bf16 v[124:127], v[100:103], a[232:239], %[idx]\n" \
    "s_waitcnt lgkmcnt(3)\n" \
    "v_smfmac_f32_16x16x64_bf16 v[120:123], v[104:107], a[48:55], %[idx]\n" \
    "v_smfmac_f32_16x16x64_bf16 v[124:127], v[104:107], a[56:63], %[idx]\n" \
    "s_waitcnt lgkmcnt(2)\n" \
    "v_smfmac_f32_16x16x64_bf16 v[120:123], v[108:111], a[112:119], %[idx]\n" \
    "v_smfmac_f32_16x16x64_bf16 v[124:127], v[108:111], a[120:127], %[idx]\n" \
    "s_waitcnt lgkmcnt(1)\n" \
    "v_smfmac_f32_16x16x64_bf16 v[120:123], v[112:115], a[176:183], %[idx]\n" \
    "v_smfmac_f32_16x16x64_bf16 v[124:127], v[112:115], a[184:191], %[idx]\n" \
    "s_mov_b32 m0, s61\n" \
    "s_waitcnt lgkmcnt(0)\n" \
    "v_smfmac_f32_16x16x64_bf16 v[120:123], v[116:119], a[240:247], %[idx]\n" \
    "buffer_load_dwordx4 %[vk], %[ws], s84 offen sc1 lds\n" \
    "s_mov_b32 m0, s65\n" \
    "v_smfmac_f32_16x16x64_bf16 v[124:127], v[116:119], a[248:255], %[idx]\n" \
    "s_nop 4\n" \
    "v_add_f32 v244, v120, v122\n" \
    "v_add_f32 v245, v121, v123\n" \
    "v_cmp_u_f32 vcc, v244, v245\n" \
    "v_cvt_pk_bf16_f32 v244, v244, v245\n" \
    "ds_write_b32 %[qw], v244 offset:16384\n" \
    "v_add_f32 v246, v124, v126\n" \
    "v_add_f32 v247, v125, v127\n" \
    "v_cvt_pk_bf16_f32 v246, v246, v247\n" \
    "ds_write_b32 %[qw], v246 offset:16640\n" \
    "s_cbranch_vccnz LA_memr_%=\n" \
    "LA_memok_%=:\n" \
    "s_waitcnt lgkmcnt(0)\n" \
    "s_barrier\n" \
    "ds_read_b64_tr_b16 v[216:217], %[qr] offset:16384\n" \
    "ds_read_b64_tr_b16 v[218:219], %[qr] offset:16640\n" \
    "ds_read_b64_tr_b16 v[220:221], %[qr] offset:16896\n" \
    "ds_read_b64_tr_b16 v[222:223], %[qr] offset:17152\n" \
    "ds_read_b64_tr_b16 v[224:225], %[qr] offset:17408\n" \
    "ds_read_b64_tr_b16 v[226:227], %[qr] offset:17664\n" \
    "ds_read_b64_tr_b16 v[228:229], %[qr] offset:17920\n" \
    "ds_read_b64_tr_b16 v[230:231], %[qr] offset:18176\n" \
    "ds_read_b64_tr_b16 v[56:57], v36 offset:16384\n" \
    "ds_read_b64_tr_b16 v[58:59], v36 offset:16640\n" \
    "ds_read_b64_tr_b16 v[60:61], v36 offset:17408\n" \
    "ds_read_b64_tr_b16 v[62:63], v36 offset:17664\n" \
    "s_waitcnt lgkmcnt(10)\n" \
    "v_mfma_f32_16x16x32_bf16 v[192:195], v[144:147], v[216:219], 0\n" \
    "v_mfma_f32_16x16x32_bf16 v[196:199], v[160:163], v[216:219], 0\n" \
    "s_waitcnt lgkmcnt(8)\n" \
    "v_mfma_f32_16x16x32_bf16 v[192:195], v[148:151], v[220:223], v[192:195]\n" \
    "v_mfma_f32_16x16x32_bf16 v[196:199], v[164:167], v[220:223], v[196:199]\n" \
    "s_waitcnt lgkmcnt(6)\n" \
    "v_mfma_f32_16x16x32_bf16 v[192:195], v[152:155], v[224:227], v[192:195]\n" \
    "v_mfma_f32_16x16x32_bf16 v[196:199], v[168:171], v[224:227], v[196:199]\n" \
    "s_waitcnt lgkmcnt(4)\n" \
    "v_mfma_f32_16x16x32_bf16 v[192:195], v[156:159], v[228:231], v[192:195]\n" \
    "buffer_load_dwordx2 v[208:209], %[vva], %[ws], s94 offen sc1\n" \
    "v_mfma_f32_16x16x32_bf16 v[196:199], v[172:175], v[228:231], v[196:199]\n" \
    "buffer_load_dwordx2 v[210:211], v249, %[ws], s94 offen sc1\n" \
    "s_waitcnt vmcnt(2)\n" \
    "s_barrier\n" \
    "ds_read_b64_tr_b16 v[40:41], v37 offset:45056\n" \
    "ds_read_b64_tr_b16 v[42:43], v37 offset:45568\n" \
    "ds_read_b64_tr_b16 v[44:45], v37 offset:46080\n" \
    "ds_read_b64_tr_b16 v[46:47], v37 offset:46592\n" \
    "ds_read_b64_tr_b16 v[48:49], v37 offset:47104\n" \
    "ds_read_b64_tr_b16 v[50:51], v37 offset:47616\n" \
    "ds_read_b64_tr_b16 v[52:53], v37 offset:48128\n" \
    "ds_read_b64_tr_b16 v[54:55], v37 offset:48640\n" \
    "LA_k_%=:\n" \
    "s_waitcnt lgkmcnt(6)\n" \
    "v_mfma_f32_16x16x32_bf16 v[200:203], v[40:43], v[56:59], 0\n" \
    "s_waitcnt lgkmcnt(4)\n" \
    "v_mfma_f32_16x16x32_bf16 v[200:203], v[44:47], v[60:63], v[200:203]\n" \
    "s_waitcnt lgkmcnt(2)\n" \
    "v_mfma_f32_16x16x32_bf16 v[200:203], v[48:51], v[56:59], v[200:203]\n" \
    "s_waitcnt lgkmcnt(0)\n" \
    "v_mfma_f32_16x16x32_bf16 v[200:203], v[52:55], v[60:63], v[200:203]\n" \
    "v_max3_f32 v104, v192, v193, v194\n" \
    "v_max3_f32 v104, v104, v195, v196\n" \
    "v_max3_f32 v104, v104, v197, v198\n" \
    "v_max_f32 v104, v104, v199\n" \
    "v_cndmask_b32 v92, v196, v192, s[56:57]\n" \
    "v_cndmask_b32 v93, v197, v193, s[56:57]\n" \
    "v_cndmask_b32 v94, v198, v194, s[56:57]\n" \
    "v_cndmask_b32 v95, v199, v195, s[56:57]\n" \
    "s_nop 0\n" \
    "v_cndmask_b32 v204, v203, v200, s[56:57]\n" \
    "v_cndmask_b32 v205, v202, v201, s[56:57]\n" \
    "s_nop 1\n" \
    "v_add_f32_dpp v206, v204, v205 row_ror:8 row_mask:0xf bank_mask:0xf\n" \
    "v_add_f32_dpp v96, v205, v204 row_ror:8 row_mask:0xf bank_mask:0xf\n" \
    "v_max3_f32 v105, v104, v96, v206\n" \
    "v_max3_f32 v104, v104, v96, v206\n" \
    "v_cmp_u_f32 vcc, v96, v206\n" \
    "s_nop 0\n" \
    "v_permlane32_swap_b32 v104, v105\n" \
    "v_max_f32 v106, v104, v105\n" \
    "v_max_f32 v107, v104, v105\n" \
    "s_nop 1\n" \
    "v_permlane16_swap_b32 v106, v107\n" \
    "v_max_f32 v104, v106, v107\n" \
    "v_mul_f32 v104, 0xbe0293ee, v104\n" \
    "v_fmamk_f32 v101, v96, 0x3e0293ee, v104\n" \
    "v_exp_f32 v101, v101\n" \
    "s_cbranch_vccnz LA_kr_%=\n" \
    "LA_kok_%=:\n" \
    "v_fmamk_f32 v97, v92, 0x3e0293ee, v104\n" \
    "v_exp_f32 v97, v97\n" \
    "v_mov_b32_dpp v105, v101 row_ror:8 row_mask:0xf bank_mask:0xf\n" \
    "v_fmamk_f32 v98, v93, 0x3e0293ee, v104\n" \
    "v_exp_f32 v98, v98\n" \
    "v_cvt_pk_bf16_f32 v236, v101, v105\n" \
    "v_cvt_pk_bf16_f32 v239, v101, v105\n" \
    "LA_v_%=:\n" \
    "v_fmamk_f32 v99, v94, 0x3e0293ee, v104\n" \
    "v_exp_f32 v99, v99\n" \
    "v_mfma_f32_16x16x16_bf16 v[116:119], v[212:213], v[236:237], 0\n" \
    "v_fmamk_f32 v100, v95, 0x3e0293ee, v104\n" \
    "v_exp_f32 v100, v100\n" \
    "s_waitcnt vmcnt(0)\n" \
    "v_mfma_f32_16x16x16_bf16 v[72:75], v[208:209], v[236:237], 0\n" \
    "v_cvt_pk_bf16_f32 v88, v97, v98\n" \
    "v_cvt_pk_bf16_f32 v89, v99, v100\n" \
    "v_mfma_f32_16x16x16_bf16 v[76:79], v[208:209], v[238:239], 0\n" \
    "v_mov_b32_dpp v90, v88 row_ror:8 row_mask:0xf bank_mask:0xf\n" \
    "v_mov_b32_dpp v91, v89 row_ror:8 row_mask:0xf bank_mask:0xf\n" \
    "v_max3_u32 v240, v208, v209, v210\n" \
    "v_max_u32 v240, v240, v211\n" \
    "v_mfma_f32_16x16x32_bf16 v[116:119], v[212:215], v[88:91], v[116:119]\n" \
    "v_cmp_eq_u32 vcc, -1, v240\n" \
    "v_mfma_f32_16x16x32_bf16 v[72:75], v[176:179], v[88:91], v[72:75]\n" \
    "v_mfma_f32_16x16x32_bf16 v[76:79], v[180:183], v[88:91], v[76:79]\n" \
    "s_cbranch_vccnz LA_vr_%=\n" \
    "v_rcp_f32 v106, v116\n" \
    "s_nop 2\n" \
    "v_pk_mul_f32 v[72:73], v[72:73], v[106:107] op_sel_hi:[1,0]\n" \
    "v_pk_mul_f32 v[74:75], v[74:75], v[106:107] op_sel_hi:[1,0]\n" \
    "v_cvt_pk_bf16_f32 v108, v72, v73\n" \
    "v_cvt_pk_bf16_f32 v109, v74, v75\n" \
    "v_pk_mul_f32 v[76:77], v[76:77], v[106:107] op_sel_hi:[1,0]\n" \
    "v_pk_mul_f32 v[78:79], v[78:79], v[106:107] op_sel_hi:[1,0]\n" \
    "v_cvt_pk_bf16_f32 v110, v76, v77\n" \
    "v_cvt_pk_bf16_f32 v111, v78, v79\n" \
    "buffer_store_dwordx4 v[108:111], %[vo], %[ws], s94 offen sc1\n" \
    "v_mfma_f32_16x16x32_bf16 v[192:195], v[144:147], v[128:131], 0\n" \
    "s_memrealtime s[72:73]\n" \
    "s_sub_u32 s96, s95, 1\n" \
    "v_mfma_f32_16x16x32_bf16 v[196:199], v[160:163], v[128:131], 0\n" \
    "s_memrealtime s[74:75]\n" \
    "v_mov_b32 v244, s96\n" \
    "v_mfma_f32_16x16x32_bf16 v[192:195], v[148:151], v[132:135], v[192:195]\n" \
    "s_memrealtime s[76:77]\n" \
    "s_lshr_b32 s85, s94, 19\n" \
    "v_mfma_f32_16x16x32_bf16 v[196:199], v[164:167], v[132:135], v[196:199]\n" \
    "s_memrealtime s[78:79]\n" \
    "s_lshl_b32 s85, s85, 8\n" \
    "v_mfma_f32_16x16x32_bf16 v[192:195], v[152:155], v[136:139], v[192:195]\n" \
    "s_memrealtime s[80:81]\n" \
    "s_add_u32 s85, s85, 2080768\n" \
    "v_mfma_f32_16x16x32_bf16 v[196:199], v[168:171], v[136:139], v[196:199]\n" \
    "s_memrealtime s[82:83]\n" \
    "s_add_u32 s85, s85, %[npol]\n" \
    "v_mfma_f32_16x16x32_bf16 v[192:195], v[156:159], v[140:143], v[192:195]\n" \
    "buffer_load_dword v38, %[vfp], %[ws], %[npol] offen sc1\n" \
    "v_mov_b64 v[120:121], 0\n" \
    "v_mfma_f32_16x16x32_bf16 v[196:199], v[172:175], v[140:143], v[196:199]\n" \
    "v_mov_b64 v[122:123], 0\n" \
    "v_mov_b64 v[124:125], 0\n" \
    "v_mfma_f32_16x16x32_bf16 v[200:203], v[40:43], v[128:131], 0\n" \
    "v_mov_b64 v[126:127], 0\n" \
    "s_add_u32 s94, s94, 524288\n" \
    "v_mfma_f32_16x16x32_bf16 v[204:207], v[40:43], v[132:135], 0\n" \
    "s_and_b32 s94, s94, 2097151\n" \
    "s_add_u32 s64, s95, 1\n" \
    "v_mfma_f32_16x16x32_bf16 v[200:203], v[44:47], v[136:139], v[200:203]\n" \
    "s_min_u32 s64, s64, 63\n" \
    "s_xor_b32 s84, s84, 1048576\n" \
    "v_mfma_f32_16x16x32_bf16 v[204:207], v[44:47], v[140:143], v[204:207]\n" \
    "buffer_load_dwordx3 v[252:254], off, %[ws], s85 sc1\n" \
    "v_mfma_f32_16x16x32_bf16 v[200:203], v[48:51], v[128:131], v[200:203]\n" \
    "v_mfma_f32_16x16x32_bf16 v[204:207], v[48:51], v[132:135], v[204:207]\n" \
    "v_mfma_f32_16x16x32_bf16 v[200:203], v[52:55], v[136:139], v[200:203]\n" \
    "v_mfma_f32_16x16x32_bf16 v[204:207], v[52:55], v[140:143], v[204:207]\n" \
    "s_waitcnt lgkmcnt(0)\n" \
    "buffer_store_dwordx4 v[232:235], %[vo], %[ws], s99 offen sc1\n" \
    "buffer_store_dword v244, %[vfo], %[ws], %[pol] offen sc1\n" \
    "s_add_u32 s95, s95, 1\n" \
    "v_cndmask_b32 v92, v243, v192, s[40:41]\n" \
    "v_cndmask_b32 v93, v243, v193, s[42:43]\n" \
    "v_cndmask_b32 v94, v243, v194, s[44:45]\n" \
    "v_cndmask_b32 v95, v243, v195, s[46:47]\n" \
    "v_cndmask_b32 v96, v243, v196, s[48:49]\n" \
    "v_cndmask_b32 v97, v243, v197, s[50:51]\n" \
    "v_cndmask_b32 v98, v243, v198, s[52:53]\n" \
    "v_cndmask_b32 v99, v243, v199, s[54:55]\n" \
    "v_max3_f32 v104, v92, v93, v94\n" \
    "v_add_f32 v100, v200, v206\n" \
    "v_add_f32 v101, v201, v207\n" \
    "v_max3_f32 v104, v104, v95, v96\n" \
    "v_max3_f32 v104, v104, v97, v98\n" \
    "v_max3_f32 v104, v104, v99, v100\n" \
    "v_max_f32 v105, v104, v101\n" \
    "v_max_f32 v104, v104, v101\n" \
    "s_nop 1\n" \
    "v_permlane32_swap_b32 v104, v105\n" \
    "v_max_f32 v106, v104, v105\n" \
    "v_max_f32 v107, v104, v105\n" \
    "s_nop 1\n" \
    "v_permlane16_swap_b32 v106, v107\n" \
    "v_max_f32 v104, v106, v107\n" \
    "v_mul_f32 v104, 0xbe0293ee, v104\n" \
    "v_fmamk_f32 v100, v100, 0x3e0293ee, v104\n" \
    "v_exp_f32 v100, v100\n" \
    "v_fmamk_f32 v101, v101, 0x3e0293ee, v104\n" \
    "v_exp_f32 v101, v101\n" \
    "v_fmamk_f32 v92, v92, 0x3e0293ee, v104\n" \
    "v_exp_f32 v92, v92\n" \
    "v_cvt_pk_bf16_f32 v236, v100, v101\n" \
    "v_cvt_pk_bf16_f32 v239, v100, v101\n" \
    "v_fmamk_f32 v93, v93, 0x3e0293ee, v104\n" \
    "v_exp_f32 v93, v93\n" \
    "v_mfma_f32_16x16x16_bf16 v[116:119], v[212:213], v[236:237], 0\n" \
    "v_fmamk_f32 v94, v94, 0x3e0293ee, v104\n" \
    "v_exp_f32 v94, v94\n" \
    "v_cvt_pk_bf16_f32 v88, v92, v93\n" \
    "v_mfma_f32_16x16x16_bf16 v[72:75], v[208:209], v[236:237], 0\n" \
    "v_fmamk_f32 v95, v95, 0x3e0293ee, v104\n" \
    "v_exp_f32 v95, v95\n" \
    "v_mfma_f32_16x16x16_bf16 v[76:79], v[208:209], v[238:239], 0\n" \
    "v_fmamk_f32 v96, v96, 0x3e0293ee, v104\n" \
    "v_exp_f32 v96, v96\n" \
    "v_cvt_pk_bf16_f32 v89, v94, v95\n" \
    "v_mfma_f32_16x16x16_bf16 v[80:83], v[210:211], v[236:237], 0\n" \
    "v_fmamk_f32 v97, v97, 0x3e0293ee, v104\n" \
    "s_waitcnt vmcnt(3)\n" \
    "v_cmp_gt_i32 vcc, s64, v38\n" \
    "s_mul_i32 s62, s64, 0x202000\n" \
    "s_and_b32 s62, s62, 0xffe02000\n" \
    "s_xor_b32 s62, s62, %[npol]\n" \
    "s_mov_b32 m0, s66\n" \
    "s_cbranch_vccnz LA_f1r_%=\n" \
    "LA_f1_%=:\n" \
    "buffer_load_dwordx4 %[vx], %[ws], s62 offen sc1 lds\n" \
    "v_exp_f32 v97, v97\n" \
    "v_mfma_f32_16x16x16_bf16 v[84:87], v[210:211], v[238:239], 0\n" \
    "v_fmamk_f32 v98, v98, 0x3e0293ee, v104\n" \
    "v_exp_f32 v98, v98\n" \
    "v_cvt_pk_bf16_f32 v90, v96, v97\n" \
    "v_fmamk_f32 v99, v99, 0x3e0293ee, v104\n" \
    "v_exp_f32 v99, v99\n" \
    "s_nop 0\n" \
    "buffer_load_dwordx4 %[vx], %[ws], s62 offen offset:1024 sc1 lds\n" \
    "s_add_u32 m0, s66, 8192\n" \
    "s_add_u32 s62, s62, 16384\n" \
    "v_cvt_pk_bf16_f32 v91, v98, v99\n" \
    "s_nop 1\n" \
    "v_mfma_f32_16x16x32_bf16 v[116:119], v[212:215], v[88:91], v[116:119]\n" \
    "s_add_u32 s72, s72, s74\n" \
    "s_add_u32 s72, s72, s76\n" \
    "s_add_u32 s72, s72, s78\n" \
    "v_mfma_f32_16x16x32_bf16 v[72:75], v[176:179], v[88:91], v[72:75]\n" \
    "s_add_u32 s72, s72, s80\n" \
    "buffer_load_dwordx4 %[vx], %[ws], s62 offen sc1 lds\n" \
    "s_add_u32 s72, s72, s82\n" \
    "s_lshl_b32 s72, s72, 2\n" \
    "v_mfma_f32_16x16x32_bf16 v[76:79], v[180:183], v[88:91], v[76:79]\n" \
    "s_sub_u32 s87, s87, " STR(OFF_A) "\n" \
    "s_mul_i32 s73, s87, 3\n" \
    "s_sub_u32 s72, s72, s73\n" \
    "v_mfma_f32_16x16x32_bf16 v[80:83], v[184:187], v[88:91], v[80:83]\n" \
    "v_rcp_f32 v106, v116\n" \
    "buffer_load_dwordx4 %[vx], %[ws], s62 offen offset:1024 sc1 lds\n" \
    "s_add_u32 m0, s66, 16384\n" \
    "s_add_u32 s62, s62, 16384\n" \
    "v_mfma_f32_16x16x32_bf16 v[84:87], v[188:191], v[88:91], v[84:87]\n" \
    "v_mov_b32 v245, s72\n" \
    "buffer_store_dword v245, %[vrp], %[ws], %[npol] offen sc1\n" \
    "v_pk_mul_f32 v[72:73], v[72:73], v[106:107] op_sel_hi:[1,0]\n" \
    "v_pk_mul_f32 v[74:75], v[74:75], v[106:107] op_sel_hi:[1,0]\n" \
    "v_pk_mul_f32 v[76:77], v[76:77], v[106:107] op_sel_hi:[1,0]\n" \
    "v_pk_mul_f32 v[78:79], v[78:79], v[106:107] op_sel_hi:[1,0]\n" \
    "v_cvt_pk_bf16_f32 v108, v72, v73\n" \
    "buffer_load_dwordx4 %[vx], %[ws], s62 offen sc1 lds\n" \
    "v_cvt_pk_bf16_f32 v109, v74, v75\n" \
    "v_cvt_pk_bf16_f32 v110, v76, v77\n" \
    "v_cvt_pk_bf16_f32 v111, v78, v79\n" \
    "buffer_store_dwordx4 v[108:111], %[vox], %[ws], s63 offen sc1\n" \
    "v_pk_mul_f32 v[80:81], v[80:81], v[106:107] op_sel_hi:[1,0]\n" \
    "v_pk_mul_f32 v[82:83], v[82:83], v[106:107] op_sel_hi:[1,0]\n" \
    "v_pk_mul_f32 v[84:85], v[84:85], v[106:107] op_sel_hi:[1,0]\n" \
    "v_pk_mul_f32 v[86:87], v[86:87], v[106:107] op_sel_hi:[1,0]\n" \
    "buffer_load_dwordx4 %[vx], %[ws], s62 offen offset:1024 sc1 lds\n" \
    "s_mov_b32 m0, s65\n" \
    "v_cvt_pk_bf16_f32 v112, v80, v81\n" \
    "v_cvt_pk_bf16_f32 v113, v82, v83\n" \
    "v_cvt_pk_bf16_f32 v114, v84, v85\n" \
    "v_cvt_pk_bf16_f32 v115, v86, v87\n" \
    "buffer_store_dwordx4 v[112:115], v250, %[ws], s63 offen sc1\n" \
    "s_cmp_eq_u32 s95, 64\n" \
    "s_cbranch_scc1 LA_done_%=\n" \
    "s_add_u32 s63, s63, 0x200000\n" \
    "s_xor_b32 s63, s63, 8192\n" \
    "s_waitcnt vmcnt(11)\n" \
    "LA_msg_%=:\n" \
    "v_readfirstlane_b32 s86, v252\n" \
    "v_readfirstlane_b32 s87, v253\n" \
    "v_readfirstlane_b32 s96, v254\n" \
    "s_nop 1\n" \
    "s_max_u32 s97, s86, s87\n" \
    "s_max_u32 s97, s97, s96\n" \
    "s_cmp_eq_u32 s97, -1\n" \
    "s_cbranch_scc1 LA_msgr_%=\n" \
    "s_add_u32 s87, s87, " STR(OFF_A) "\n" \
    "s_branch LA_top_%=\n" \
    "LA_done_%=:\n" \
    "s_waitcnt vmcnt(0)\n" \
    "s_barrier\n" \
    "v_mov_b32 v244, 63\n" \
    "buffer_store_dword v244, %[vfo], %[ws], %[pol] offen sc1\n" \
    "s_branch LA_end_%=\n" \
    "LA_hsr_%=:\n" \
    "s_sleep 2\n" \
    "s_add_u32 s98, s98, 1\n" \
    "s_cmp_gt_u32 s98, 131072\n" \
    "s_cbranch_scc1 LA_fail_%=\n" \
    "s_branch LA_hs_%=\n" \
    "LA_redo_%=:\n" \
    "s_waitcnt vmcnt(0) lgkmcnt(0)\n" \
    "s_barrier\n" \
    "s_or_b32 %[cfg], s97, 256\n" \
    "s_branch LA_end_%=\n" \
    "LA_f0r_%=:\n" \
    "buffer_load_dword v38, %[vfp], %[ws], %[npol] offen sc1\n" \
    "s_waitcnt vmcnt(0)\n" \
    "v_cmp_gt_i32 vcc, s64, v38\n" \
    "s_cbranch_vccz LA_f0_%=\n" \
    "s_sleep 1\n" \
    "s_add_u32 s98, s98, 1\n" \
    "s_cmp_gt_u32 s98, 131072\n" \
    "s_cbranch_scc1 LA_fail_%=\n" \
    "s_branch LA_f0r_%=\n" \
    "LA_tmc_%=:\n" \
    "s_cmp_lt_i32 s97, " STR(T_LEADA) "\n" \
    "s_cbranch_scc1 LA_rs_%=\n" \
    "s_cmp_gt_u32 s97, 4000\n" \
    "s_cbranch_scc1 LA_tmx_%=\n" \
    "LA_tmd_%=:\n" \
    "s_memrealtime s[92:93]\n" \
    "s_waitcnt lgkmcnt(0)\n" \
    "s_lshl_b32 s97, s92, 3\n" \
    "s_sub_u32 s97, s87, s97\n" \
    "s_cmp_gt_i32 s97, 96\n" \
    "s_cbranch_scc1 LA_tmd_%=\n" \
    "s_branch LA_tm_%=\n" \
    "LA_t2_%=:\n" \
    "s_memrealtime s[72:73]\n" \
    "s_nop 1\n" \
    "s_memrealtime s[74:75]\n" \
    "s_nop 1\n" \
    "s_memrealtime s[76:77]\n" \
    "s_nop 1\n" \
    "s_memrealtime s[78:79]\n" \
    "s_nop 1\n" \
    "s_memrealtime s[80:81]\n" \
    "s_nop 1\n" \
    "s_memrealtime s[82:83]\n" \
    "s_nop 1\n" \
    "s_memrealtime s[92:93]\n" \
    "s_nop 1\n" \
    "s_memrealtime s[70:71]\n" \
    "s_getpc_b64 s[68:69]\n" \
    "LA_t2p_%=:\n" \
    "s_add_u32 s68, s68, LA_t2x_%= - LA_t2p_%=\n" \
    "s_addc_u32 s69, s69, 0\n" \
    "s_waitcnt lgkmcnt(0)\n" \
    "s_sub_u32 s97, s87, s72\n" \
    "s_sub_u32 s97, s97, s74\n" \
    "s_sub_u32 s97, s97, s76\n" \
    "s_sub_u32 s97, s97, s78\n" \
    "s_sub_u32 s97, s97, s80\n" \
    "s_sub_u32 s97, s97, s82\n" \
    "s_sub_u32 s97, s97, s92\n" \
    "s_sub_u32 s97, s97, s70\n" \
    "s_cmp_gt_u32 s97, 160\n" \
    "s_cbranch_scc1 LA_t2c_%=\n" \
    "s_mul_i32 s97, s97, 47\n" \
    "s_lshr_b32 s97, s97, 4\n" \
    "s_and_b32 s97, s97, -4\n" \
    "s_sub_u32 s68, s68, s97\n" \
    "s_subb_u32 s69, s69, 0\n" \
    "s_setpc_b64 s[68:69]\n" \
    ".rept 118\n" \
    "s_nop 0\n" \
    ".endr\n" \
    "LA_t2x_%=:\n" \
    "s_branch LA_tmx_%=\n" \
    "LA_t2c_%=:\n" \
    "s_cmp_gt_u32 s97, 4000\n" \
    "s_cbranch_scc1 LA_t2x_%=\n" \
    "LA_t2d_%=:\n" \
    "s_memrealtime s[92:93]\n" \
    "s_waitcnt lgkmcnt(0)\n" \
    "s_lshl_b32 s97, s92, 3\n" \
    "s_sub_u32 s97, s87, s97\n" \
    "s_cmp_gt_i32 s97, 96\n" \
    "s_cbranch_scc1 LA_t2d_%=\n" \
    "s_branch LA_t2_%=\n" \
    "LA_memr_%=:\n" \
    "v_max3_u32 v240, v56, v57, v57\n" \
    "v_max3_u32 v240, v240, v58, v59\n" \
    "v_max3_u32 v240, v240, v60, v61\n" \
    "v_max3_u32 v240, v240, v62, v63\n" \
    "v_max3_u32 v240, v240, v64, v65\n" \
    "v_max3_u32 v240, v240, v66, v67\n" \
    "v_max3_u32 v240, v240, v68, v69\n" \
    "v_max3_u32 v240, v240, v70, v71\n" \
    "v_max3_u32 v240, v240, v72, v73\n" \
    "v_max3_u32 v240, v240, v74, v75\n" \
    "v_max3_u32 v240, v240, v76, v77\n" \
    "v_max3_u32 v240, v240, v78, v79\n" \
    "v_max3_u32 v240, v240, v80, v81\n" \
    "v_max3_u32 v240, v240, v82, v83\n" \
    "v_max3_u32 v240, v240, v84, v85\n" \
    "v_max3_u32 v240, v240, v86, v87\n" \
    "v_max3_u32 v240, v240, v88, v89\n" \
    "v_max3_u32 v240, v240, v90, v91\n" \
    "v_max3_u32 v240, v240, v92, v93\n" \
    "v_max3_u32 v240, v240, v94, v95\n" \
    "v_max3_u32 v240, v240, v96, v97\n" \
    "v_max3_u32 v240, v240, v98, v99\n" \
    "v_max3_u32 v240, v240, v100, v101\n" \
    "v_max3_u32 v240, v240, v102, v103\n" \
    "v_max3_u32 v240, v240, v104, v105\n" \
    "v_max3_u32 v240, v240, v106, v107\n" \
    "v_max3_u32 v240, v240, v108, v109\n" \
    "v_max3_u32 v240, v240, v110, v111\n" \
    "v_max3_u32 v240, v240, v112, v113\n" \
    "v_max3_u32 v240, v240, v114, v115\n" \
    "v_max3_u32 v240, v240, v116, v117\n" \
    "v_max3_u32 v240, v240, v118, v119\n" \
    "v_cmp_eq_u32 vcc, -1, v240\n" \
    "s_cbranch_vccz LA_memok_%=\n" \
    "s_waitcnt vmcnt(0) lgkmcnt(0)\n" \
    "s_mov_b32 m0, s65\n" \
    "v_mov_b64 v[120:121], 0\n" \
    "v_mov_b64 v[122:123], 0\n" \
    "v_mov_b64 v[124:125], 0\n" \
    "v_mov_b64 v[126:127], 0\n" \
    "s_add_u32 s98, s98, 1\n" \
    "s_cmp_gt_u32 s98, 131072\n" \
    "s_cbranch_scc1 LA_fail_%=\n" \
    "s_cmp_lg_u32 %[w], 0\n" \
    "s_cbranch_scc1 LA_tmx_%=\n" \
    "LA_rs_%=:\n" \
    "s_add_u32 s98, s98, 1\n" \
    "s_cmp_gt_u32 s98, 131072\n" \
    "s_cbranch_scc1 LA_fail_%=\n" \
    "s_lshr_b32 s85, s94, 19\n" \
    "s_lshl_b32 s85, s85, 8\n" \
    "s_add_u32 s85, s85, 2080768\n" \
    "s_add_u32 s85, s85, %[npol]\n" \
    "buffer_load_dwordx3 v[240:242], off, %[ws], s85 sc1\n" \
    "s_waitcnt vmcnt(0)\n" \
    "v_readfirstlane_b32 s86, v240\n" \
    "v_readfirstlane_b32 s87, v241\n" \
    "v_readfirstlane_b32 s96, v242\n" \
    "s_nop 1\n" \
    "s_max_u32 s97, s86, s87\n" \
    "s_max_u32 s97, s97, s96\n" \
    "s_cmp_eq_u32 s97, -1\n" \
    "s_cbranch_scc1 LA_rs_%=\n" \
    "s_add_u32 s87, s96, " STR(OFF_A) "\n" \
    "s_branch LA_t2_%=\n" \
    "LA_kr_%=:\n" \
    "ds_read_b128 v[72:75], %[lr] offset:45056\n" \
    "ds_read_b128 v[76:79], %[lr] offset:46080\n" \
    "ds_read_b128 v[80:83], %[lr] offset:47104\n" \
    "ds_read_b128 v[84:87], %[lr] offset:48128\n" \
    "s_waitcnt lgkmcnt(0)\n" \
    "v_max3_u32 v240, v72, v73, v73\n" \
    "v_max3_u32 v240, v240, v74, v75\n" \
    "v_max3_u32 v240, v240, v76, v77\n" \
    "v_max3_u32 v240, v240, v78, v79\n" \
    "v_max3_u32 v240, v240, v80, v81\n" \
    "v_max3_u32 v240, v240, v82, v83\n" \
    "v_max3_u32 v240, v240, v84, v85\n" \
    "v_max3_u32 v240, v240, v86, v87\n" \
    "v_cmp_eq_u32 vcc, -1, v240\n" \
    "s_cbranch_vccz LA_kok_%=\n" \
    "s_barrier\n" \
    "s_mov_b32 m0, s61\n" \
    "s_nop 0\n" \
    "buffer_load_dwordx4 %[vk], %[ws], s84 offen sc1 lds\n" \
    "s_mov_b32 m0, s65\n" \
    "s_waitcnt vmcnt(0)\n" \
    "s_barrier\n" \
    "ds_read_b64_tr_b16 v[40:41], v37 offset:45056\n" \
    "ds_read_b64_tr_b16 v[42:43], v37 offset:45568\n" \
    "ds_read_b64_tr_b16 v[44:45], v37 offset:46080\n" \
    "ds_read_b64_tr_b16 v[46:47], v37 offset:46592\n" \
    "ds_read_b64_tr_b16 v[48:49], v37 offset:47104\n" \
    "ds_read_b64_tr_b16 v[50:51], v37 offset:47616\n" \
    "ds_read_b64_tr_b16 v[52:53], v37 offset:48128\n" \
    "ds_read_b64_tr_b16 v[54:55], v37 offset:48640\n" \
    "s_add_u32 s98, s98, 1\n" \
    "s_cmp_gt_u32 s98, 131072\n" \
    "s_cbranch_scc1 LA_fail_%=\n" \
    "s_branch LA_k_%=\n" \
    "LA_vr_%=:\n" \
    "s_nop 7\n" \
    "buffer_load_dwordx2 v[208:209], %[vva], %[ws], s94 offen sc1\n" \
    "buffer_load_dwordx2 v[210:211], v249, %[ws], s94 offen sc1\n" \
    "s_waitcnt vmcnt(0)\n" \
    "s_add_u32 s98, s98, 1\n" \
    "s_cmp_gt_u32 s98, 131072\n" \
    "s_cbranch_scc1 LA_fail_%=\n" \
    "s_branch LA_v_%=\n" \
    "LA_f1r_%=:\n" \
    "buffer_load_dword v38, %[vfp], %[ws], %[npol] offen sc1\n" \
    "s_waitcnt vmcnt(0)\n" \
    "v_cmp_gt_i32 vcc, s64, v38\n" \
    "s_cbranch_vccz LA_f1_%=\n" \
    "s_sleep 1\n" \
    "s_add_u32 s98, s98, 1\n" \
    "s_cmp_gt_u32 s98, 131072\n" \
    "s_cbranch_scc1 LA_fail_%=\n" \
    "s_branch LA_f1r_%=\n" \
    "LA_msgr_%=:\n" \
    "buffer_load_dwordx3 v[252:254], off, %[ws], s85 sc1\n" \
    "s_waitcnt vmcnt(0)\n" \
    "s_add_u32 s98, s98, 1\n" \
    "s_cmp_gt_u32 s98, 131072\n" \
    "s_cbranch_scc1 LA_fail_%=\n" \
    "s_branch LA_msg_%=\n" \
    "LA_fail_%=:\n" \
    "v_mov_b32 v244, 2043904\n" \
    "v_mov_b32 v245, 1\n" \
    "buffer_store_dword v245, v244, %[ws], 0 offen sc1\n" \
    "s_endpgm\n" \
    "LA_end_%=:\n"
#define ASM_ROLE_PQ \
    "s_nop 4\n" \
    "s_mov_b32 s98, 0\n" \
    "s_mov_b32 s95, 0\n" \
    "s_add_u32 s97, %[hsa], 32768\n" \
    "buffer_load_dword v31, off, %[ws], s97 sc1\n" \
    "v_mov_b32 v32, %[fo]\n" \
    "v_xor_b32 v33, 64, %[fo]\n" \
    "v_add_u32 v34, 0x10000, v32\n" \
    "v_add_u32 v35, 0x10000, v33\n" \
    "s_lshr_b32 s61, %[w], 1\n" \
    "s_lshl_b32 s61, s61, 5\n" \
    "s_and_b32 s97, %[w], 1\n" \
    "s_add_u32 s61, s61, s97\n" \
    "s_lshl_b32 s61, s61, 10\n" \
    "s_lshl_b32 s97, %[w], 13\n" \
    "s_add_u32 s61, s61, s97\n" \
    "s_lshl_b32 s60, %[w], 9\n" \
    "s_xor_b32 m0, s61, 0\n" \
    "s_xor_b32 s97, s60, 0\n" \
    "buffer_load_dwordx4 %[vox], %[dx], s97 offen lds\n" \
    "s_xor_b32 m0, s61, 2048\n" \
    "s_xor_b32 s97, s60, 128\n" \
    "buffer_load_dwordx4 %[vox], %[dx], s97 offen lds\n" \
    "s_xor_b32 m0, s61, 4096\n" \
    "s_xor_b32 s97, s60, 256\n" \
    "buffer_load_dwordx4 %[vox], %[dx], s97 offen lds\n" \
    "s_xor_b32 m0, s61, 6144\n" \
    "s_xor_b32 s97, s60, 384\n" \
    "buffer_load_dwordx4 %[vox], %[dx], s97 offen lds\n" \
    "s_xor_b32 m0, s61, 8192\n" \
    "s_xor_b32 s97, s60, 512\n" \
    "buffer_load_dwordx4 %[vox], %[dx], s97 offen lds\n" \
    "s_xor_b32 m0, s61, 10240\n" \
    "s_xor_b32 s97, s60, 640\n" \
    "buffer_load_dwordx4 %[vox], %[dx], s97 offen lds\n" \
    "s_xor_b32 m0, s61, 12288\n" \
    "s_xor_b32 s97, s60, 768\n" \
    "buffer_load_dwordx4 %[vox], %[dx], s97 offen lds\n" \
    "s_xor_b32 m0, s61, 14336\n" \
    "s_xor_b32 s97, s60, 896\n" \
    "buffer_load_dwordx4 %[vox], %[dx], s97 offen lds\n" \
    "s_xor_b32 m0, s61, 16384\n" \
    "s_xor_b32 s97, s60, 1024\n" \
    "buffer_load_dwordx4 %[vox], %[dx], s97 offen lds\n" \
    "s_xor_b32 m0, s61, 18432\n" \
    "s_xor_b32 s97, s60, 1152\n" \
    "buffer_load_dwordx4 %[vox], %[dx], s97 offen lds\n" \
    "s_xor_b32 m0, s61, 20480\n" \
    "s_xor_b32 s97, s60, 1280\n" \
    "buffer_load_dwordx4 %[vox], %[dx], s97 offen lds\n" \
    "s_xor_b32 m0, s61, 22528\n" \
    "s_xor_b32 s97, s60, 1408\n" \
    "buffer_load_dwordx4 %[vox], %[dx], s97 offen lds\n" \
    "s_xor_b32 m0, s61, 24576\n" \
    "s_xor_b32 s97, s60, 1536\n" \
    "buffer_load_dwordx4 %[vox], %[dx], s97 offen lds\n" \
    "s_xor_b32 m0, s61, 26624\n" \
    "s_xor_b32 s97, s60, 1664\n" \
    "buffer_load_dwordx4 %[vox], %[dx], s97 offen lds\n" \
    "s_xor_b32 m0, s61, 28672\n" \
    "s_xor_b32 s97, s60, 1792\n" \
    "buffer_load_dwordx4 %[vox], %[dx], s97 offen lds\n" \
    "s_xor_b32 m0, s61, 30720\n" \
    "s_xor_b32 s97, s60, 1920\n" \
    "buffer_load_dwordx4 %[vox], %[dx], s97 offen lds\n" \
    "s_add_u32 s60, s60, 65536\n" \
    "v_xor_b32 v88, 128, %[vow]\n" \
    "v_mov_b32 v89, %[ra]\n" \
    "v_xor_b32 v90, 32, %[ra]\n" \
    "v_add_u32 v91, 0x10000, v89\n" \
    "v_add_u32 v92, 0x10000, v90\n" \
    "s_lshl_b32 s64, %[w], 13\n" \
    "s_lshl_b32 s65, %[w], 18\n" \
    "s_add_u32 m0, s64, 65536\n" \
    "s_add_u32 s97, s65, 0\n" \
    "buffer_load_dwordx4 %[vow], %[wd], s97 offen lds\n" \
    "s_add_u32 m0, s64, 66560\n" \
    "s_add_u32 s97, s65, 65536\n" \
    "buffer_load_dwordx4 v88, %[wd], s97 offen lds\n" \
    "s_add_u32 m0, s64, 67584\n" \
    "s_add_u32 s97, s65, 131072\n" \
    "buffer_load_dwordx4 %[vow], %[wd], s97 offen lds\n" \
    "s_add_u32 m0, s64, 68608\n" \
    "s_add_u32 s97, s65, 196608\n" \
    "buffer_load_dwordx4 v88, %[wd], s97 offen lds\n" \
    "s_add_u32 m0, s64, 69632\n" \
    "s_add_u32 s97, s65, 32768\n" \
    "buffer_load_dwordx4 %[vow], %[wd], s97 offen lds\n" \
    "s_add_u32 m0, s64, 70656\n" \
    "s_add_u32 s97, s65, 98304\n" \
    "buffer_load_dwordx4 v88, %[wd], s97 offen lds\n" \
    "s_add_u32 m0, s64, 71680\n" \
    "s_add_u32 s97, s65, 163840\n" \
    "buffer_load_dwordx4 %[vow], %[wd], s97 offen lds\n" \
    "s_add_u32 m0, s64, 72704\n" \
    "s_add_u32 s97, s65, 229376\n" \
    "buffer_load_dwordx4 v88, %[wd], s97 offen lds\n" \
    "s_add_u32 m0, s64, 98304\n" \
    "s_add_u32 s97, s65, 1048576\n" \
    "buffer_load_dwordx4 %[vow], %[wd], s97 offen lds\n" \
    "s_add_u32 m0, s64, 99328\n" \
    "s_add_u32 s97, s65, 1114112\n" \
    "buffer_load_dwordx4 v88, %[wd], s97 offen lds\n" \
    "s_add_u32 m0, s64, 100352\n" \
    "s_add_u32 s97, s65, 1179648\n" \
    "buffer_load_dwordx4 %[vow], %[wd], s97 offen lds\n" \
    "s_add_u32 m0, s64, 101376\n" \
    "s_add_u32 s97, s65, 1245184\n" \
    "buffer_load_dwordx4 v88, %[wd], s97 offen lds\n" \
    "s_add_u32 m0, s64, 102400\n" \
    "s_add_u32 s97, s65, 1081344\n" \
    "buffer_load_dwordx4 %[vow], %[wd], s97 offen lds\n" \
    "s_add_u32 m0, s64, 103424\n" \
    "s_add_u32 s97, s65, 1146880\n" \
    "buffer_load_dwordx4 v88, %[wd], s97 offen lds\n" \
    "s_add_u32 m0, s64, 104448\n" \
    "s_add_u32 s97, s65, 1212416\n" \
    "buffer_load_dwordx4 %[vow], %[wd], s97 offen lds\n" \
    "s_add_u32 m0, s64, 105472\n" \
    "s_add_u32 s97, s65, 1277952\n" \
    "buffer_load_dwordx4 v88, %[wd], s97 offen lds\n" \
    "s_waitcnt vmcnt(8) lgkmcnt(0)\n" \
    "s_barrier\n" \
    "v_readfirstlane_b32 %[cfg], v31\n" \
    "s_nop 1\n" \
    "s_add_u32 %[cfg], %[cfg], 256\n" \
    "s_and_b32 %[cfg], %[cfg], 0xffffff00\n" \
    "v_mov_b32 v31, %[cfg]\n" \
    "buffer_store_dword v31, off, %[ws], %[hsa] sc1\n" \
    "s_add_u32 m0, s64, 131072\n" \
    "s_add_u32 s97, s65, 2097152\n" \
    "buffer_load_dwordx4 %[vow], %[wd], s97 offen lds\n" \
    "s_add_u32 m0, s64, 132096\n" \
    "s_add_u32 s97, s65, 2162688\n" \
    "buffer_load_dwordx4 v88, %[wd], s97 offen lds\n" \
    "s_add_u32 m0, s64, 133120\n" \
    "s_add_u32 s97, s65, 2228224\n" \
    "buffer_load_dwordx4 %[vow], %[wd], s97 offen lds\n" \
    "s_add_u32 m0, s64, 134144\n" \
    "s_add_u32 s97, s65, 2293760\n" \
    "buffer_load_dwordx4 v88, %[wd], s97 offen lds\n" \
    "s_add_u32 m0, s64, 135168\n" \
    "s_add_u32 s97, s65, 2129920\n" \
    "buffer_load_dwordx4 %[vow], %[wd], s97 offen lds\n" \
    "s_add_u32 m0, s64, 136192\n" \
    "s_add_u32 s97, s65, 2195456\n" \
    "buffer_load_dwordx4 v88, %[wd], s97 offen lds\n" \
    "s_add_u32 m0, s64, 137216\n" \
    "s_add_u32 s97, s65, 2260992\n" \
    "buffer_load_dwordx4 %[vow], %[wd], s97 offen lds\n" \
    "s_add_u32 m0, s64, 138240\n" \
    "s_add_u32 s97, s65, 2326528\n" \
    "buffer_load_dwordx4 v88, %[wd], s97 offen lds\n" \
    "ds_read_b64_tr_b16 a[0:1], v89 offset:0\n" \
    "ds_read_b64_tr_b16 a[2:3], v89 offset:4096\n" \
    "ds_read_b64_tr_b16 a[4:5], v90 offset:0\n" \
    "ds_read_b64_tr_b16 a[6:7], v90 offset:4096\n" \
    "ds_read_b64_tr_b16 a[8:9], v89 offset:8192\n" \
    "ds_read_b64_tr_b16 a[10:11], v89 offset:12288\n" \
    "ds_read_b64_tr_b16 a[12:13], v90 offset:8192\n" \
    "ds_read_b64_tr_b16 a[14:15], v90 offset:12288\n" \
    "ds_read_b64_tr_b16 a[16:17], v89 offset:16384\n" \
    "ds_read_b64_tr_b16 a[18:19], v89 offset:20480\n" \
    "ds_read_b64_tr_b16 a[20:21], v90 offset:16384\n" \
    "ds_read_b64_tr_b16 a[22:23], v90 offset:20480\n" \
    "ds_read_b64_tr_b16 a[24:25], v89 offset:24576\n" \
    "ds_read_b64_tr_b16 a[26:27], v89 offset:28672\n" \
    "ds_read_b64_tr_b16 a[28:29], v90 offset:24576\n" \
    "ds_read_b64_tr_b16 a[30:31], v90 offset:28672\n" \
    "s_waitcnt vmcnt(8) lgkmcnt(0)\n" \
    "s_barrier\n" \
    "s_add_u32 m0, s64, 65536\n" \
    "s_add_u32 s97, s65, 3145728\n" \
    "buffer_load_dwordx4 %[vow], %[wd], s97 offen lds\n" \
    "s_add_u32 m0, s64, 66560\n" \
    "s_add_u32 s97, s65, 3211264\n" \
    "buffer_load_dwordx4 v88, %[wd], s97 offen lds\n" \
    "s_add_u32 m0, s64, 67584\n" \
    "s_add_u32 s97, s65, 3276800\n" \
    "buffer_load_dwordx4 %[vow], %[wd], s97 offen lds\n" \
    "s_add_u32 m0, s64, 68608\n" \
    "s_add_u32 s97, s65, 3342336\n" \
    "buffer_load_dwordx4 v88, %[wd], s97 offen lds\n" \
    "s_add_u32 m0, s64, 69632\n" \
    "s_add_u32 s97, s65, 3178496\n" \
    "buffer_load_dwordx4 %[vow], %[wd], s97 offen lds\n" \
    "s_add_u32 m0, s64, 70656\n" \
    "s_add_u32 s97, s65, 3244032\n" \
    "buffer_load_dwordx4 v88, %[wd], s97 offen lds\n" \
    "s_add_u32 m0, s64, 71680\n" \
    "s_add_u32 s97, s65, 3309568\n" \
    "buffer_load_dwordx4 %[vow], %[wd], s97 offen lds\n" \
    "s_add_u32 m0, s64, 72704\n" \
    "s_add_u32 s97, s65, 3375104\n" \
    "buffer_load_dwordx4 v88, %[wd], s97 offen lds\n" \
    "ds_read_b64_tr_b16 a[32:33], v89 offset:32768\n" \
    "ds_read_b64_tr_b16 a[34:35], v89 offset:36864\n" \
    "ds_read_b64_tr_b16 a[36:37], v90 offset:32768\n" \
    "ds_read_b64_tr_b16 a[38:39], v90 offset:36864\n" \
    "ds_read_b64_tr_b16 a[40:41], v89 offset:40960\n" \
    "ds_read_b64_tr_b16 a[42:43], v89 offset:45056\n" \
    "ds_read_b64_tr_b16 a[44:45], v90 offset:40960\n" \
    "ds_read_b64_tr_b16 a[46:47], v90 offset:45056\n" \
    "ds_read_b64_tr_b16 a[48:49], v89 offset:49152\n" \
    "ds_read_b64_tr_b16 a[50:51], v89 offset:53248\n" \
    "ds_read_b64_tr_b16 a[52:53], v90 offset:49152\n" \
    "ds_read_b64_tr_b16 a[54:55], v90 offset:53248\n" \
    "ds_read_b64_tr_b16 a[56:57], v89 offset:57344\n" \
    "ds_read_b64_tr_b16 a[58:59], v89 offset:61440\n" \
    "ds_read_b64_tr_b16 a[60:61], v90 offset:57344\n" \
    "ds_read_b64_tr_b16 a[62:63], v90 offset:61440\n" \
    "s_waitcnt vmcnt(8) lgkmcnt(0)\n" \
    "s_barrier\n" \
    "s_add_u32 m0, s64, 98304\n" \
    "s_add_u32 s97, s65, 4194304\n" \
    "buffer_load_dwordx4 %[vow], %[wd], s97 offen lds\n" \
    "s_add_u32 m0, s64, 99328\n" \
    "s_add_u32 s97, s65, 4259840\n" \
    "buffer_load_dwordx4 v88, %[wd], s97 offen lds\n" \
    "s_add_u32 m0, s64, 100352\n" \
    "s_add_u32 s97, s65, 4325376\n" \
    "buffer_load_dwordx4 %[vow], %[wd], s97 offen lds\n" \
    "s_add_u32 m0, s64, 101376\n" \
    "s_add_u32 s97, s65, 4390912\n" \
    "buffer_load_dwordx4 v88, %[wd], s97 offen lds\n" \
    "s_add_u32 m0, s64, 102400\n" \
    "s_add_u32 s97, s65, 4227072\n" \
    "buffer_load_dwordx4 %[vow], %[wd], s97 offen lds\n" \
    "s_add_u32 m0, s64, 103424\n" \
    "s_add_u32 s97, s65, 4292608\n" \
    "buffer_load_dwordx4 v88, %[wd], s97 offen lds\n" \
    "s_add_u32 m0, s64, 104448\n" \
    "s_add_u32 s97, s65, 4358144\n" \
    "buffer_load_dwordx4 %[vow], %[wd], s97 offen lds\n" \
    "s_add_u32 m0, s64, 105472\n" \
    "s_add_u32 s97, s65, 4423680\n" \
    "buffer_load_dwordx4 v88, %[wd], s97 offen lds\n" \
    "ds_read_b64_tr_b16 a[64:65], v91 offset:0\n" \
    "ds_read_b64_tr_b16 a[66:67], v91 offset:4096\n" \
    "ds_read_b64_tr_b16 a[68:69], v92 offset:0\n" \
    "ds_read_b64_tr_b16 a[70:71], v92 offset:4096\n" \
    "ds_read_b64_tr_b16 a[72:73], v91 offset:8192\n" \
    "ds_read_b64_tr_b16 a[74:75], v91 offset:12288\n" \
    "ds_read_b64_tr_b16 a[76:77], v92 offset:8192\n" \
    "ds_read_b64_tr_b16 a[78:79], v92 offset:12288\n" \
    "ds_read_b64_tr_b16 a[80:81], v91 offset:16384\n" \
    "ds_read_b64_tr_b16 a[82:83], v91 offset:20480\n" \
    "ds_read_b64_tr_b16 a[84:85], v92 offset:16384\n" \
    "ds_read_b64_tr_b16 a[86:87], v92 offset:20480\n" \
    "ds_read_b64_tr_b16 a[88:89], v91 offset:24576\n" \
    "ds_read_b64_tr_b16 a[90:91], v91 offset:28672\n" \
    "ds_read_b64_tr_b16 a[92:93], v92 offset:24576\n" \
    "ds_read_b64_tr_b16 a[94:95], v92 offset:28672\n" \
    "s_waitcnt vmcnt(8) lgkmcnt(0)\n" \
    "s_barrier\n" \
    "s_add_u32 m0, s64, 131072\n" \
    "s_add_u32 s97, s65, 5242880\n" \
    "buffer_load_dwordx4 %[vow], %[wd], s97 offen lds\n" \
    "s_add_u32 m0, s64, 132096\n" \
    "s_add_u32 s97, s65, 5308416\n" \
    "buffer_load_dwordx4 v88, %[wd], s97 offen lds\n" \
    "s_add_u32 m0, s64, 133120\n" \
    "s_add_u32 s97, s65, 5373952\n" \
    "buffer_load_dwordx4 %[vow], %[wd], s97 offen lds\n" \
    "s_add_u32 m0, s64, 134144\n" \
    "s_add_u32 s97, s65, 5439488\n" \
    "buffer_load_dwordx4 v88, %[wd], s97 offen lds\n" \
    "s_add_u32 m0, s64, 135168\n" \
    "s_add_u32 s97, s65, 5275648\n" \
    "buffer_load_dwordx4 %[vow], %[wd], s97 offen lds\n" \
    "s_add_u32 m0, s64, 136192\n" \
    "s_add_u32 s97, s65, 5341184\n" \
    "buffer_load_dwordx4 v88, %[wd], s97 offen lds\n" \
    "s_add_u32 m0, s64, 137216\n" \
    "s_add_u32 s97, s65, 5406720\n" \
    "buffer_load_dwordx4 %[vow], %[wd], s97 offen lds\n" \
    "s_add_u32 m0, s64, 138240\n" \
    "s_add_u32 s97, s65, 5472256\n" \
    "buffer_load_dwordx4 v88, %[wd], s97 offen lds\n" \
    "ds_read_b64_tr_b16 a[96:97], v89 offset:0\n" \
    "ds_read_b64_tr_b16 a[98:99], v89 offset:4096\n" \
    "ds_read_b64_tr_b16 a[100:101], v90 offset:0\n" \
    "ds_read_b64_tr_b16 a[102:103], v90 offset:4096\n" \
    "ds_read_b64_tr_b16 a[104:105], v89 offset:8192\n" \
    "ds_read_b64_tr_b16 a[106:107], v89 offset:12288\n" \
    "ds_read_b64_tr_b16 a[108:109], v90 offset:8192\n" \
    "ds_read_b64_tr_b16 a[110:111], v90 offset:12288\n" \
    "ds_read_b64_tr_b16 a[112:113], v89 offset:16384\n" \
    "ds_read_b64_tr_b16 a[114:115], v89 offset:20480\n" \
    "ds_read_b64_tr_b16 a[116:117], v90 offset:16384\n" \
    "ds_read_b64_tr_b16 a[118:119], v90 offset:20480\n" \
    "ds_read_b64_tr_b16 a[120:121], v89 offset:24576\n" \
    "ds_read_b64_tr_b16 a[122:123], v89 offset:28672\n" \
    "ds_read_b64_tr_b16 a[124:125], v90 offset:24576\n" \
    "ds_read_b64_tr_b16 a[126:127], v90 offset:28672\n" \
    "s_waitcnt vmcnt(8) lgkmcnt(0)\n" \
    "s_barrier\n" \
    "s_add_u32 m0, s64, 65536\n" \
    "s_add_u32 s97, s65, 6291456\n" \
    "buffer_load_dwordx4 %[vow], %[wd], s97 offen lds\n" \
    "s_add_u32 m0, s64, 66560\n" \
    "s_add_u32 s97, s65, 6356992\n" \
    "buffer_load_dwordx4 v88, %[wd], s97 offen lds\n" \
    "s_add_u32 m0, s64, 67584\n" \
    "s_add_u32 s97, s65, 6422528\n" \
    "buffer_load_dwordx4 %[vow], %[wd], s97 offen lds\n" \
    "s_add_u32 m0, s64, 68608\n" \
    "s_add_u32 s97, s65, 6488064\n" \
    "buffer_load_dwordx4 v88, %[wd], s97 offen lds\n" \
    "s_add_u32 m0, s64, 69632\n" \
    "s_add_u32 s97, s65, 6324224\n" \
    "buffer_load_dwordx4 %[vow], %[wd], s97 offen lds\n" \
    "s_add_u32 m0, s64, 70656\n" \
    "s_add_u32 s97, s65, 6389760\n" \
    "buffer_load_dwordx4 v88, %[wd], s97 offen lds\n" \
    "s_add_u32 m0, s64, 71680\n" \
    "s_add_u32 s97, s65, 6455296\n" \
    "buffer_load_dwordx4 %[vow], %[wd], s97 offen lds\n" \
    "s_add_u32 m0, s64, 72704\n" \
    "s_add_u32 s97, s65, 6520832\n" \
    "buffer_load_dwordx4 v88, %[wd], s97 offen lds\n" \
    "ds_read_b64_tr_b16 a[128:129], v89 offset:32768\n" \
    "ds_read_b64_tr_b16 a[130:131], v89 offset:36864\n" \
    "ds_read_b64_tr_b16 a[132:133], v90 offset:32768\n" \
    "ds_read_b64_tr_b16 a[134:135], v90 offset:36864\n" \
    "ds_read_b64_tr_b16 a[136:137], v89 offset:40960\n" \
    "ds_read_b64_tr_b16 a[138:139], v89 offset:45056\n" \
    "ds_read_b64_tr_b16 a[140:141], v90 offset:40960\n" \
    "ds_read_b64_tr_b16 a[142:143], v90 offset:45056\n" \
    "ds_read_b64_tr_b16 a[144:145], v89 offset:49152\n" \
    "ds_read_b64_tr_b16 a[146:147], v89 offset:53248\n" \
    "ds_read_b64_tr_b16 a[148:149], v90 offset:49152\n" \
    "ds_read_b64_tr_b16 a[150:151], v90 offset:53248\n" \
    "ds_read_b64_tr_b16 a[152:153], v89 offset:57344\n" \
    "ds_read_b64_tr_b16 a[154:155], v89 offset:61440\n" \
    "ds_read_b64_tr_b16 a[156:157], v90 offset:57344\n" \
    "ds_read_b64_tr_b16 a[158:159], v90 offset:61440\n" \
    "s_waitcnt vmcnt(8) lgkmcnt(0)\n" \
    "s_barrier\n" \
    "s_add_u32 m0, s64, 98304\n" \
    "s_add_u32 s97, s65, 7340032\n" \
    "buffer_load_dwordx4 %[vow], %[wd], s97 offen lds\n" \
    "s_add_u32 m0, s64, 99328\n" \
    "s_add_u32 s97, s65, 7405568\n" \
    "buffer_load_dwordx4 v88, %[wd], s97 offen lds\n" \
    "s_add_u32 m0, s64, 100352\n" \
    "s_add_u32 s97, s65, 7471104\n" \
    "buffer_load_dwordx4 %[vow], %[wd], s97 offen lds\n" \
    "s_add_u32 m0, s64, 101376\n" \
    "s_add_u32 s97, s65, 7536640\n" \
    "buffer_load_dwordx4 v88, %[wd], s97 offen lds\n" \
    "s_add_u32 m0, s64, 102400\n" \
    "s_add_u32 s97, s65, 7372800\n" \
    "buffer_load_dwordx4 %[vow], %[wd], s97 offen lds\n" \
    "s_add_u32 m0, s64, 103424\n" \
    "s_add_u32 s97, s65, 7438336\n" \
    "buffer_load_dwordx4 v88, %[wd], s97 offen lds\n" \
    "s_add_u32 m0, s64, 104448\n" \
    "s_add_u32 s97, s65, 7503872\n" \
    "buffer_load_dwordx4 %[vow], %[wd], s97 offen lds\n" \
    "s_add_u32 m0, s64, 105472\n" \
    "s_add_u32 s97, s65, 7569408\n" \
    "buffer_load_dwordx4 v88, %[wd], s97 offen lds\n" \
    "ds_read_b64_tr_b16 a[160:161], v91 offset:0\n" \
    "ds_read_b64_tr_b16 a[162:163], v91 offset:4096\n" \
    "ds_read_b64_tr_b16 a[164:165], v92 offset:0\n" \
    "ds_read_b64_tr_b16 a[166:167], v92 offset:4096\n" \
    "ds_read_b64_tr_b16 a[168:169], v91 offset:8192\n" \
    "ds_read_b64_tr_b16 a[170:171], v91 offset:12288\n" \
    "ds_read_b64_tr_b16 a[172:173], v92 offset:8192\n" \
    "ds_read_b64_tr_b16 a[174:175], v92 offset:12288\n" \
    "ds_read_b64_tr_b16 a[176:177], v91 offset:16384\n" \
    "ds_read_b64_tr_b16 a[178:179], v91 offset:20480\n" \
    "ds_read_b64_tr_b16 a[180:181], v92 offset:16384\n" \
    "ds_read_b64_tr_b16 a[182:183], v92 offset:20480\n" \
    "ds_read_b64_tr_b16 a[184:185], v91 offset:24576\n" \
    "ds_read_b64_tr_b16 a[186:187], v91 offset:28672\n" \
    "ds_read_b64_tr_b16 a[188:189], v92 offset:24576\n" \
    "ds_read_b64_tr_b16 a[190:191], v92 offset:28672\n" \
    "s_waitcnt vmcnt(8) lgkmcnt(0)\n" \
    "s_barrier\n" \
    "s_add_u32 s97, %[hsa], 32768\n" \
    "buffer_load_dword v31, off, %[ws], s97 sc1\n" \
    "ds_read_b64_tr_b16 a[192:193], v89 offset:0\n" \
    "ds_read_b64_tr_b16 a[194:195], v89 offset:4096\n" \
    "ds_read_b64_tr_b16 a[196:197], v90 offset:0\n" \
    "ds_read_b64_tr_b16 a[198:199], v90 offset:4096\n" \
    "ds_read_b64_tr_b16 a[200:201], v89 offset:8192\n" \
    "ds_read_b64_tr_b16 a[202:203], v89 offset:12288\n" \
    "ds_read_b64_tr_b16 a[204:205], v90 offset:8192\n" \
    "ds_read_b64_tr_b16 a[206:207], v90 offset:12288\n" \
    "ds_read_b64_tr_b16 a[208:209], v89 offset:16384\n" \
    "ds_read_b64_tr_b16 a[210:211], v89 offset:20480\n" \
    "ds_read_b64_tr_b16 a[212:213], v90 offset:16384\n" \
    "ds_read_b64_tr_b16 a[214:215], v90 offset:20480\n" \
    "ds_read_b64_tr_b16 a[216:217], v89 offset:24576\n" \
    "ds_read_b64_tr_b16 a[218:219], v89 offset:28672\n" \
    "ds_read_b64_tr_b16 a[220:221], v90 offset:24576\n" \
    "ds_read_b64_tr_b16 a[222:223], v90 offset:28672\n" \
    "s_waitcnt vmcnt(0) lgkmcnt(0)\n" \
    "s_barrier\n" \
    "ds_read_b64_tr_b16 a[224:225], v89 offset:32768\n" \
    "ds_read_b64_tr_b16 a[226:227], v89 offset:36864\n" \
    "ds_read_b64_tr_b16 a[228:229], v90 offset:32768\n" \
    "ds_read_b64_tr_b16 a[230:231], v90 offset:36864\n" \
    "ds_read_b64_tr_b16 a[232:233], v89 offset:40960\n" \
    "ds_read_b64_tr_b16 a[234:235], v89 offset:45056\n" \
    "ds_read_b64_tr_b16 a[236:237], v90 offset:40960\n" \
    "ds_read_b64_tr_b16 a[238:239], v90 offset:45056\n" \
    "ds_read_b64_tr_b16 a[240:241], v89 offset:49152\n" \
    "ds_read_b64_tr_b16 a[242:243], v89 offset:53248\n" \
    "ds_read_b64_tr_b16 a[244:245], v90 offset:49152\n" \
    "ds_read_b64_tr_b16 a[246:247], v90 offset:53248\n" \
    "ds_read_b64_tr_b16 a[248:249], v89 offset:57344\n" \
    "ds_read_b64_tr_b16 a[250:251], v89 offset:61440\n" \
    "ds_read_b64_tr_b16 a[252:253], v90 offset:57344\n" \
    "ds_read_b64_tr_b16 a[254:255], v90 offset:61440\n" \
    "s_waitcnt lgkmcnt(0)\n" \
    "s_branch LPQ_hsl_%=\n" \
    "LPQ_hs_%=:\n" \
    "s_add_u32 s97, %[hsa], 32768\n" \
    "buffer_load_dword v31, off, %[ws], s97 sc1\n" \
    "s_waitcnt vmcnt(0)\n" \
    "LPQ_hsl_%=:\n" \
    "v_readfirstlane_b32 s97, v31\n" \
    "s_nop 1\n" \
    "s_xor_b32 s97, s97, %[cfg]\n" \
    "s_cmp_gt_u32 s97, 255\n" \
    "s_cbranch_scc1 LPQ_hsr_%=\n" \
    "s_and_b32 s96, s97, 3\n" \
    "s_cmp_lg_u32 s96, %[est]\n" \
    "s_cbranch_scc1 LPQ_redo_%=\n" \
    "s_bitcmp0_b32 s97, 3\n" \
    "s_cbranch_scc1 LPQ_hsr_%=\n" \
    "s_mov_b32 %[cfg], s97\n" \
    "LPQ_hsok_%=:\n" \
    "s_and_b32 %[pol], %[cfg], 4\n" \
    "s_lshl_b32 %[pol], %[pol], 11\n" \
    "s_xor_b32 %[npol], %[pol], 8192\n" \
    "s_waitcnt vmcnt(0)\n" \
    "v_mov_b32 v31, 0\n" \
    "s_add_u32 s97, %[hsa], 69632\n" \
    "buffer_store_dword v31, off, %[ws], s97 sc1\n" \
    "v_mov_b32 v36, 0\n" \
    "buffer_store_dword v36, %[vfl], %[ws], %[npol] offen offset:16 sc1\n" \
    "s_mul_i32 s62, 63, 0x202000\n" \
    "s_and_b32 s62, s62, 0xffe02000\n" \
    "s_xor_b32 s62, s62, %[npol]\n" \
    "ds_read_b128 v[88:91], v32 offset:0\n" \
    "ds_read_b128 v[92:95], v32 offset:32768\n" \
    "ds_read_b128 v[96:99], v33 offset:0\n" \
    "ds_read_b128 v[100:103], v33 offset:32768\n" \
    "ds_read_b128 v[104:107], v32 offset:2048\n" \
    "LPQ_seg_%=:\n" \
    "s_waitcnt lgkmcnt(4)\n" \
    "v_mfma_f32_16x16x32_bf16 v[40:43], a[0:3], v[88:91], 0\n" \
    "ds_read_b128 v[108:111], v32 offset:34816\n" \
    "v_mfma_f32_16x16x32_bf16 v[48:51], a[4:7], v[88:91], 0\n" \
    "s_waitcnt lgkmcnt(4)\n" \
    "v_mfma_f32_16x16x32_bf16 v[44:47], a[0:3], v[92:95], 0\n" \
    "ds_read_b128 v[112:115], v33 offset:2048\n" \
    "v_mfma_f32_16x16x32_bf16 v[52:55], a[4:7], v[92:95], 0\n" \
    "s_barrier\n" \
    "s_waitcnt lgkmcnt(4)\n" \
    "v_mfma_f32_16x16x32_bf16 v[40:43], a[8:11], v[96:99], v[40:43]\n" \
    "ds_read_b128 v[116:119], v33 offset:34816\n" \
    "s_add_u32 s63, s62, 4096\n" \
    "v_mfma_f32_16x16x32_bf16 v[48:51], a[12:15], v[96:99], v[48:51]\n" \
    "v_cvt_pk_bf16_f32 v120, v56, v57\n" \
    "s_waitcnt lgkmcnt(4)\n" \
    "v_mfma_f32_16x16x32_bf16 v[44:47], a[8:11], v[100:103], v[44:47]\n" \
    "ds_read_b128 v[88:91], v32 offset:4096\n" \
    "v_cvt_pk_bf16_f32 v121, v58, v59\n" \
    "v_cvt_pk_bf16_f32 v122, v64, v65\n" \
    "v_mfma_f32_16x16x32_bf16 v[52:55], a[12:15], v[100:103], v[52:55]\n" \
    "s_xor_b32 m0, s61, 65536\n" \
    "s_xor_b32 s97, s60, 0\n" \
    "buffer_load_dwordx4 %[vox], %[dx], s97 offen lds\n" \
    "s_waitcnt lgkmcnt(4)\n" \
    "v_mfma_f32_16x16x32_bf16 v[40:43], a[16:19], v[104:107], v[40:43]\n" \
    "ds_read_b128 v[92:95], v32 offset:36864\n" \
    "v_cvt_pk_bf16_f32 v123, v66, v67\n" \
    "v_mfma_f32_16x16x32_bf16 v[48:51], a[20:23], v[104:107], v[48:51]\n" \
    "buffer_store_dwordx4 v[120:123], %[voa], %[ws], s62 offen sc1\n" \
    "s_waitcnt lgkmcnt(4)\n" \
    "v_mfma_f32_16x16x32_bf16 v[44:47], a[16:19], v[108:111], v[44:47]\n" \
    "ds_read_b128 v[96:99], v33 offset:4096\n" \
    "v_cvt_pk_bf16_f32 v124, v60, v61\n" \
    "v_cvt_pk_bf16_f32 v125, v62, v63\n" \
    "v_mfma_f32_16x16x32_bf16 v[52:55], a[20:23], v[108:111], v[52:55]\n" \
    "s_xor_b32 m0, s61, 67584\n" \
    "s_xor_b32 s97, s60, 128\n" \
    "buffer_load_dwordx4 %[vox], %[dx], s97 offen lds\n" \
    "s_waitcnt lgkmcnt(4)\n" \
    "v_mfma_f32_16x16x32_bf16 v[40:43], a[24:27], v[112:115], v[40:43]\n" \
    "ds_read_b128 v[100:103], v33 offset:36864\n" \
    "v_cvt_pk_bf16_f32 v126, v68, v69\n" \
    "v_mfma_f32_16x16x32_bf16 v[48:51], a[28:31], v[112:115], v[48:51]\n" \
    "v_cvt_pk_bf16_f32 v127, v70, v71\n" \
    "s_waitcnt lgkmcnt(4)\n" \
    "v_mfma_f32_16x16x32_bf16 v[44:47], a[24:27], v[116:119], v[44:47]\n" \
    "ds_read_b128 v[104:107], v32 offset:6144\n" \
    "buffer_store_dwordx4 v[124:127], %[voa], %[ws], s63 offen sc1\n" \
    "s_mul_i32 s62, s95, 0x202000\n" \
    "v_mfma_f32_16x16x32_bf16 v[52:55], a[28:31], v[116:119], v[52:55]\n" \
    "s_xor_b32 m0, s61, 69632\n" \
    "s_xor_b32 s97, s60, 256\n" \
    "buffer_load_dwordx4 %[vox], %[dx], s97 offen lds\n" \
    "s_waitcnt lgkmcnt(4)\n" \
    "v_mfma_f32_16x16x32_bf16 v[40:43], a[32:35], v[88:91], v[40:43]\n" \
    "ds_read_b128 v[108:111], v32 offset:38912\n" \
    "s_and_b32 s62, s62, 0xffe02000\n" \
    "v_mfma_f32_16x16x32_bf16 v[48:51], a[36:39], v[88:91], v[48:51]\n" \
    "s_xor_b32 s62, s62, %[npol]\n" \
    "s_waitcnt lgkmcnt(4)\n" \
    "v_mfma_f32_16x16x32_bf16 v[44:47], a[32:35], v[92:95], v[44:47]\n" \
    "ds_read_b128 v[112:115], v33 offset:6144\n" \
    "s_xor_b32 m0, s61, 71680\n" \
    "v_mfma_f32_16x16x32_bf16 v[52:55], a[36:39], v[92:95], v[52:55]\n" \
    "s_xor_b32 s97, s60, 384\n" \
    "buffer_load_dwordx4 %[vox], %[dx], s97 offen lds\n" \
    "s_waitcnt lgkmcnt(4)\n" \
    "v_mfma_f32_16x16x32_bf16 v[40:43], a[40:43], v[96:99], v[40:43]\n" \
    "ds_read_b128 v[116:119], v33 offset:38912\n" \
    "v_mfma_f32_16x16x32_bf16 v[48:51], a[44:47], v[96:99], v[48:51]\n" \
    "s_waitcnt lgkmcnt(4)\n" \
    "v_mfma_f32_16x16x32_bf16 v[44:47], a[40:43], v[100:103], v[44:47]\n" \
    "ds_read_b128 v[88:91], v32 offset:8192\n" \
    "s_xor_b32 m0, s61, 73728\n" \
    "v_mfma_f32_16x16x32_bf16 v[52:55], a[44:47], v[100:103], v[52:55]\n" \
    "s_xor_b32 s97, s60, 512\n" \
    "buffer_load_dwordx4 %[vox], %[dx], s97 offen lds\n" \
    "s_waitcnt lgkmcnt(4)\n" \
    "v_mfma_f32_16x16x32_bf16 v[40:43], a[48:51], v[104:107], v[40:43]\n" \
    "ds_read_b128 v[92:95], v32 offset:40960\n" \
    "v_mfma_f32_16x16x32_bf16 v[48:51], a[52:55], v[104:107], v[48:51]\n" \
    "s_waitcnt lgkmcnt(4)\n" \
    "v_mfma_f32_16x16x32_bf16 v[44:47], a[48:51], v[108:111], v[44:47]\n" \
    "ds_read_b128 v[96:99], v33 offset:8192\n" \
    "s_xor_b32 m0, s61, 75776\n" \
    "v_mfma_f32_16x16x32_bf16 v[52:55], a[52:55], v[108:111], v[52:55]\n" \
    "s_xor_b32 s97, s60, 640\n" \
    "buffer_load_dwordx4 %[vox], %[dx], s97 offen lds\n" \
    "s_waitcnt lgkmcnt(4)\n" \
    "v_mfma_f32_16x16x32_bf16 v[40:43], a[56:59], v[112:115], v[40:43]\n" \
    "ds_read_b128 v[100:103], v33 offset:40960\n" \
    "v_mfma_f32_16x16x32_bf16 v[48:51], a[60:63], v[112:115], v[48:51]\n" \
    "s_waitcnt lgkmcnt(4)\n" \
    "v_mfma_f32_16x16x32_bf16 v[44:47], a[56:59], v[116:119], v[44:47]\n" \
    "ds_read_b128 v[104:107], v32 offset:10240\n" \
    "s_xor_b32 m0, s61, 77824\n" \
    "v_mfma_f32_16x16x32_bf16 v[52:55], a[60:63], v[116:119], v[52:55]\n" \
    "s_xor_b32 s97, s60, 768\n" \
    "buffer_load_dwordx4 %[vox], %[dx], s97 offen lds\n" \
    "s_waitcnt lgkmcnt(4)\n" \
    "v_mfma_f32_16x16x32_bf16 v[40:43], a[64:67], v[88:91], v[40:43]\n" \
    "ds_read_b128 v[108:111], v32 offset:43008\n" \
    "v_mfma_f32_16x16x32_bf16 v[48:51], a[68:71], v[88:91], v[48:51]\n" \
    "s_waitcnt lgkmcnt(4)\n" \
    "v_mfma_f32_16x16x32_bf16 v[44:47], a[64:67], v[92:95], v[44:47]\n" \
    "ds_read_b128 v[112:115], v33 offset:10240\n" \
    "s_xor_b32 m0, s61, 79872\n" \
    "v_mfma_f32_16x16x32_bf16 v[52:55], a[68:71], v[92:95], v[52:55]\n" \
    "s_xor_b32 s97, s60, 896\n" \
    "buffer_load_dwordx4 %[vox], %[dx], s97 offen lds\n" \
    "s_waitcnt lgkmcnt(4)\n" \
    "v_mfma_f32_16x16x32_bf16 v[40:43], a[72:75], v[96:99], v[40:43]\n" \
    "ds_read_b128 v[116:119], v33 offset:43008\n" \
    "v_mfma_f32_16x16x32_bf16 v[48:51], a[76:79], v[96:99], v[48:51]\n" \
    "s_waitcnt lgkmcnt(4)\n" \
    "v_mfma_f32_16x16x32_bf16 v[44:47], a[72:75], v[100:103], v[44:47]\n" \
    "ds_read_b128 v[88:91], v32 offset:12288\n" \
    "s_xor_b32 m0, s61, 81920\n" \
    "v_mfma_f32_16x16x32_bf16 v[52:55], a[76:79], v[100:103], v[52:55]\n" \
    "s_xor_b32 s97, s60, 1024\n" \
    "buffer_load_dwordx4 %[vox], %[dx], s97 offen lds\n" \
    "s_waitcnt lgkmcnt(4)\n" \
    "v_mfma_f32_16x16x32_bf16 v[40:43], a[80:83], v[104:107], v[40:43]\n" \
    "ds_read_b128 v[92:95], v32 offset:45056\n" \
    "v_mfma_f32_16x16x32_bf16 v[48:51], a[84:87], v[104:107], v[48:51]\n" \
    "s_waitcnt lgkmcnt(4)\n" \
    "v_mfma_f32_16x16x32_bf16 v[44:47], a[80:83], v[108:111], v[44:47]\n" \
    "ds_read_b128 v[96:99], v33 offset:12288\n" \
    "s_xor_b32 m0, s61, 83968\n" \
    "v_mfma_f32_16x16x32_bf16 v[52:55], a[84:87], v[108:111], v[52:55]\n" \
    "s_xor_b32 s97, s60, 1152\n" \
    "buffer_load_dwordx4 %[vox], %[dx], s97 offen lds\n" \
    "s_waitcnt lgkmcnt(4)\n" \
    "v_mfma_f32_16x16x32_bf16 v[40:43], a[88:91], v[112:115], v[40:43]\n" \
    "ds_read_b128 v[100:103], v33 offset:45056\n" \
    "v_mfma_f32_16x16x32_bf16 v[48:51], a[92:95], v[112:115], v[48:51]\n" \
    "s_waitcnt lgkmcnt(4)\n" \
    "v_mfma_f32_16x16x32_bf16 v[44:47], a[88:91], v[116:119], v[44:47]\n" \
    "ds_read_b128 v[104:107], v32 offset:14336\n" \
    "s_xor_b32 m0, s61, 86016\n" \
    "v_mfma_f32_16x16x32_bf16 v[52:55], a[92:95], v[116:119], v[52:55]\n" \
    "s_xor_b32 s97, s60, 1280\n" \
    "buffer_load_dwordx4 %[vox], %[dx], s97 offen lds\n" \
    "s_waitcnt lgkmcnt(4)\n" \
    "v_mfma_f32_16x16x32_bf16 v[40:43], a[96:99], v[88:91], v[40:43]\n" \
    "ds_read_b128 v[108:111], v32 offset:47104\n" \
    "v_mfma_f32_16x16x32_bf16 v[48:51], a[100:103], v[88:91], v[48:51]\n" \
    "s_waitcnt lgkmcnt(4)\n" \
    "v_mfma_f32_16x16x32_bf16 v[44:47], a[96:99], v[92:95], v[44:47]\n" \
    "ds_read_b128 v[112:115], v33 offset:14336\n" \
    "s_xor_b32 m0, s61, 88064\n" \
    "v_mfma_f32_16x16x32_bf16 v[52:55], a[100:103], v[92:95], v[52:55]\n" \
    "s_xor_b32 s97, s60, 1408\n" \
    "buffer_load_dwordx4 %[vox], %[dx], s97 offen lds\n" \
    "s_waitcnt lgkmcnt(4)\n" \
    "v_mfma_f32_16x16x32_bf16 v[40:43], a[104:107], v[96:99], v[40:43]\n" \
    "ds_read_b128 v[116:119], v33 offset:47104\n" \
    "v_mfma_f32_16x16x32_bf16 v[48:51], a[108:111], v[96:99], v[48:51]\n" \
    "s_waitcnt lgkmcnt(4)\n" \
    "v_mfma_f32_16x16x32_bf16 v[44:47], a[104:107], v[100:103], v[44:47]\n" \
    "ds_read_b128 v[88:91], v32 offset:16384\n" \
    "s_xor_b32 m0, s61, 90112\n" \
    "v_mfma_f32_16x16x32_bf16 v[52:55], a[108:111], v[100:103], v[52:55]\n" \
    "s_xor_b32 s97, s60, 1536\n" \
    "buffer_load_dwordx4 %[vox], %[dx], s97 offen lds\n" \
    "s_waitcnt lgkmcnt(4)\n" \
    "v_mfma_f32_16x16x32_bf16 v[40:43], a[112:115], v[104:107], v[40:43]\n" \
    "ds_read_b128 v[92:95], v32 offset:49152\n" \
    "v_mfma_f32_16x16x32_bf16 v[48:51], a[116:119], v[104:107], v[48:51]\n" \
    "s_waitcnt lgkmcnt(4)\n" \
    "v_mfma_f32_16x16x32_bf16 v[44:47], a[112:115], v[108:111], v[44:47]\n" \
    "ds_read_b128 v[96:99], v33 offset:16384\n" \
    "s_xor_b32 m0, s61, 92160\n" \
    "v_mfma_f32_16x16x32_bf16 v[52:55], a[116:119], v[108:111], v[52:55]\n" \
    "s_xor_b32 s97, s60, 1664\n" \
    "buffer_load_dwordx4 %[vox], %[dx], s97 offen lds\n" \
    "s_waitcnt lgkmcnt(4)\n" \
    "v_mfma_f32_16x16x32_bf16 v[40:43], a[120:123], v[112:115], v[40:43]\n" \
    "ds_read_b128 v[100:103], v33 offset:49152\n" \
    "v_mfma_f32_16x16x32_bf16 v[48:51], a[124:127], v[112:115], v[48:51]\n" \
    "s_waitcnt lgkmcnt(4)\n" \
    "v_mfma_f32_16x16x32_bf16 v[44:47], a[120:123], v[116:119], v[44:47]\n" \
    "ds_read_b128 v[104:107], v32 offset:18432\n" \
    "s_xor_b32 m0, s61, 94208\n" \
    "v_mfma_f32_16x16x32_bf16 v[52:55], a[124:127], v[116:119], v[52:55]\n" \
    "s_xor_b32 s97, s60, 1792\n" \
    "buffer_load_dwordx4 %[vox], %[dx], s97 offen lds\n" \
    "s_waitcnt lgkmcnt(4)\n" \
    "v_mfma_f32_16x16x32_bf16 v[40:43], a[128:131], v[88:91], v[40:43]\n" \
    "ds_read_b128 v[108:111], v32 offset:51200\n" \
    "v_mfma_f32_16x16x32_bf16 v[48:51], a[132:135], v[88:91], v[48:51]\n" \
    "s_waitcnt lgkmcnt(4)\n" \
    "v_mfma_f32_16x16x32_bf16 v[44:47], a[128:131], v[92:95], v[44:47]\n" \
    "ds_read_b128 v[112:115], v33 offset:18432\n" \
    "s_xor_b32 m0, s61, 96256\n" \
    "v_mfma_f32_16x16x32_bf16 v[52:55], a[132:135], v[92:95], v[52:55]\n" \
    "s_xor_b32 s97, s60, 1920\n" \
    "buffer_load_dwordx4 %[vox], %[dx], s97 offen lds\n" \
    "s_waitcnt lgkmcnt(4)\n" \
    "v_mfma_f32_16x16x32_bf16 v[40:43], a[136:139], v[96:99], v[40:43]\n" \
    "ds_read_b128 v[116:119], v33 offset:51200\n" \
    "v_mfma_f32_16x16x32_bf16 v[48:51], a[140:143], v[96:99], v[48:51]\n" \
    "s_waitcnt lgkmcnt(4)\n" \
    "v_mfma_f32_16x16x32_bf16 v[44:47], a[136:139], v[100:103], v[44:47]\n" \
    "ds_read_b128 v[88:91], v32 offset:20480\n" \
    "v_mfma_f32_16x16x32_bf16 v[52:55], a[140:143], v[100:103], v[52:55]\n" \
    "s_waitcnt lgkmcnt(4)\n" \
    "v_mfma_f32_16x16x32_bf16 v[40:43], a[144:147], v[104:107], v[40:43]\n" \
    "ds_read_b128 v[92:95], v32 offset:53248\n" \
    "v_mfma_f32_16x16x32_bf16 v[48:51], a[148:151], v[104:107], v[48:51]\n" \
    "s_waitcnt lgkmcnt(4)\n" \
    "v_mfma_f32_16x16x32_bf16 v[44:47], a[144:147], v[108:111], v[44:47]\n" \
    "ds_read_b128 v[96:99], v33 offset:20480\n" \
    "v_mfma_f32_16x16x32_bf16 v[52:55], a[148:151], v[108:111], v[52:55]\n" \
    "s_waitcnt lgkmcnt(4)\n" \
    "v_mfma_f32_16x16x32_bf16 v[40:43], a[152:155], v[112:115], v[40:43]\n" \
    "ds_read_b128 v[100:103], v33 offset:53248\n" \
    "v_mfma_f32_16x16x32_bf16 v[48:51], a[156:159], v[112:115], v[48:51]\n" \
    "s_waitcnt lgkmcnt(4)\n" \
    "v_mfma_f32_16x16x32_bf16 v[44:47], a[152:155], v[116:119], v[44:47]\n" \
    "ds_read_b128 v[104:107], v32 offset:22528\n" \
    "v_mfma_f32_16x16x32_bf16 v[52:55], a[156:159], v[116:119], v[52:55]\n" \
    "s_waitcnt lgkmcnt(4)\n" \
    "v_mfma_f32_16x16x32_bf16 v[40:43], a[160:163], v[88:91], v[40:43]\n" \
    "ds_read_b128 v[108:111], v32 offset:55296\n" \
    "buffer_load_dword v39, %[vbp], %[ws], %[pol] offen sc1\n" \
    "v_mfma_f32_16x16x32_bf16 v[48:51], a[164:167], v[88:91], v[48:51]\n" \
    "s_sub_u32 s66, s95, 8\n" \
    "s_waitcnt lgkmcnt(4)\n" \
    "v_mfma_f32_16x16x32_bf16 v[44:47], a[160:163], v[92:95], v[44:47]\n" \
    "ds_read_b128 v[112:115], v33 offset:22528\n" \
    "v_mfma_f32_16x16x32_bf16 v[52:55], a[164:167], v[92:95], v[52:55]\n" \
    "s_waitcnt lgkmcnt(4)\n" \
    "v_mfma_f32_16x16x32_bf16 v[40:43], a[168:171], v[96:99], v[40:43]\n" \
    "ds_read_b128 v[116:119], v33 offset:55296\n" \
    "v_mfma_f32_16x16x32_bf16 v[48:51], a[172:175], v[96:99], v[48:51]\n" \
    "s_waitcnt lgkmcnt(4)\n" \
    "v_mfma_f32_16x16x32_bf16 v[44:47], a[168:171], v[100:103], v[44:47]\n" \
    "ds_read_b128 v[88:91], v32 offset:24576\n" \
    "v_mfma_f32_16x16x32_bf16 v[52:55], a[172:175], v[100:103], v[52:55]\n" \
    "s_waitcnt lgkmcnt(4)\n" \
    "v_mfma_f32_16x16x32_bf16 v[40:43], a[176:179], v[104:107], v[40:43]\n" \
    "ds_read_b128 v[92:95], v32 offset:57344\n" \
    "v_mfma_f32_16x16x32_bf16 v[48:51], a[180:183], v[104:107], v[48:51]\n" \
    "s_waitcnt lgkmcnt(4)\n" \
    "v_mfma_f32_16x16x32_bf16 v[44:47], a[176:179], v[108:111], v[44:47]\n" \
    "ds_read_b128 v[96:99], v33 offset:24576\n" \
    "v_mfma_f32_16x16x32_bf16 v[52:55], a[180:183], v[108:111], v[52:55]\n" \
    "s_waitcnt lgkmcnt(4)\n" \
    "v_mfma_f32_16x16x32_bf16 v[40:43], a[184:187], v[112:115], v[40:43]\n" \
    "ds_read_b128 v[100:103], v33 offset:57344\n" \
    "v_mfma_f32_16x16x32_bf16 v[48:51], a[188:191], v[112:115], v[48:51]\n" \
    "s_waitcnt lgkmcnt(4)\n" \
    "v_mfma_f32_16x16x32_bf16 v[44:47], a[184:187], v[116:119], v[44:47]\n" \
    "ds_read_b128 v[104:107], v32 offset:26624\n" \
    "v_mfma_f32_16x16x32_bf16 v[52:55], a[188:191], v[116:119], v[52:55]\n" \
    "s_waitcnt lgkmcnt(4)\n" \
    "v_mfma_f32_16x16x32_bf16 v[40:43], a[192:195], v[88:91], v[40:43]\n" \
    "ds_read_b128 v[108:111], v32 offset:59392\n" \
    "v_mfma_f32_16x16x32_bf16 v[48:51], a[196:199], v[88:91], v[48:51]\n" \
    "s_waitcnt lgkmcnt(4)\n" \
    "v_mfma_f32_16x16x32_bf16 v[44:47], a[192:195], v[92:95], v[44:47]\n" \
    "ds_read_b128 v[112:115], v33 offset:26624\n" \
    "v_mfma_f32_16x16x32_bf16 v[52:55], a[196:199], v[92:95], v[52:55]\n" \
    "s_waitcnt lgkmcnt(4)\n" \
    "v_mfma_f32_16x16x32_bf16 v[40:43], a[200:203], v[96:99], v[40:43]\n" \
    "ds_read_b128 v[116:119], v33 offset:59392\n" \
    "v_mfma_f32_16x16x32_bf16 v[48:51], a[204:207], v[96:99], v[48:51]\n" \
    "s_waitcnt lgkmcnt(4)\n" \
    "v_mfma_f32_16x16x32_bf16 v[44:47], a[200:203], v[100:103], v[44:47]\n" \
    "ds_read_b128 v[88:91], v32 offset:28672\n" \
    "v_mfma_f32_16x16x32_bf16 v[52:55], a[204:207], v[100:103], v[52:55]\n" \
    "s_waitcnt lgkmcnt(4)\n" \
    "v_mfma_f32_16x16x32_bf16 v[40:43], a[208:211], v[104:107], v[40:43]\n" \
    "ds_read_b128 v[92:95], v32 offset:61440\n" \
    "v_mfma_f32_16x16x32_bf16 v[48:51], a[212:215], v[104:107], v[48:51]\n" \
    "s_waitcnt lgkmcnt(4)\n" \
    "v_mfma_f32_16x16x32_bf16 v[44:47], a[208:211], v[108:111], v[44:47]\n" \
    "ds_read_b128 v[96:99], v33 offset:28672\n" \
    "v_mfma_f32_16x16x32_bf16 v[52:55], a[212:215], v[108:111], v[52:55]\n" \
    "s_waitcnt lgkmcnt(4)\n" \
    "v_mfma_f32_16x16x32_bf16 v[40:43], a[216:219], v[112:115], v[40:43]\n" \
    "ds_read_b128 v[100:103], v33 offset:61440\n" \
    "v_mfma_f32_16x16x32_bf16 v[48:51], a[220:223], v[112:115], v[48:51]\n" \
    "s_waitcnt lgkmcnt(4)\n" \
    "v_mfma_f32_16x16x32_bf16 v[44:47], a[216:219], v[116:119], v[44:47]\n" \
    "ds_read_b128 v[104:107], v32 offset:30720\n" \
    "v_mfma_f32_16x16x32_bf16 v[52:55], a[220:223], v[116:119], v[52:55]\n" \
    "s_waitcnt lgkmcnt(4)\n" \
    "v_mfma_f32_16x16x32_bf16 v[40:43], a[224:227], v[88:91], v[40:43]\n" \
    "ds_read_b128 v[108:111], v32 offset:63488\n" \
    "v_mfma_f32_16x16x32_bf16 v[48:51], a[228:231], v[88:91], v[48:51]\n" \
    "s_waitcnt lgkmcnt(4)\n" \
    "v_mfma_f32_16x16x32_bf16 v[44:47], a[224:227], v[92:95], v[44:47]\n" \
    "ds_read_b128 v[112:115], v33 offset:30720\n" \
    "v_mfma_f32_16x16x32_bf16 v[52:55], a[228:231], v[92:95], v[52:55]\n" \
    "s_waitcnt lgkmcnt(4)\n" \
    "v_mfma_f32_16x16x32_bf16 v[40:43], a[232:235], v[96:99], v[40:43]\n" \
    "ds_read_b128 v[116:119], v33 offset:63488\n" \
    "s_waitcnt vmcnt(0)\n" \
    "v_mfma_f32_16x16x32_bf16 v[48:51], a[236:239], v[96:99], v[48:51]\n" \
    "s_barrier\n" \
    "s_waitcnt lgkmcnt(4)\n" \
    "v_mfma_f32_16x16x32_bf16 v[44:47], a[232:235], v[100:103], v[44:47]\n" \
    "ds_read_b128 v[88:91], v34 offset:0\n" \
    "s_sub_u32 s96, s95, 1\n" \
    "v_mfma_f32_16x16x32_bf16 v[52:55], a[236:239], v[100:103], v[52:55]\n" \
    "v_mov_b32 v36, s96\n" \
    "buffer_store_dword v36, %[vfl], %[ws], %[npol] offen sc1\n" \
    "s_waitcnt lgkmcnt(4)\n" \
    "v_mfma_f32_16x16x32_bf16 v[40:43], a[240:243], v[104:107], v[40:43]\n" \
    "ds_read_b128 v[92:95], v34 offset:32768\n" \
    "s_add_u32 s95, s95, 1\n" \
    "v_mfma_f32_16x16x32_bf16 v[48:51], a[244:247], v[104:107], v[48:51]\n" \
    "s_add_u32 s60, s60, 65536\n" \
    "s_and_b32 s60, s60, 0x3fffff\n" \
    "s_waitcnt lgkmcnt(4)\n" \
    "v_mfma_f32_16x16x32_bf16 v[44:47], a[240:243], v[108:111], v[44:47]\n" \
    "ds_read_b128 v[96:99], v35 offset:0\n" \
    "s_add_u32 s97, s60, 65536\n" \
    "v_mfma_f32_16x16x32_bf16 v[52:55], a[244:247], v[108:111], v[52:55]\n" \
    "s_and_b32 s97, s97, 0x3f0000\n" \
    "buffer_load_dword v38, %[vxt], %[dx], s97 offen\n" \
    "s_waitcnt lgkmcnt(4)\n" \
    "v_mfma_f32_16x16x32_bf16 v[40:43], a[248:251], v[112:115], v[40:43]\n" \
    "ds_read_b128 v[100:103], v35 offset:32768\n" \
    "v_mfma_f32_16x16x32_bf16 v[48:51], a[252:255], v[112:115], v[48:51]\n" \
    "v_cmp_gt_i32 vcc, s66, v39\n" \
    "s_waitcnt lgkmcnt(4)\n" \
    "v_mfma_f32_16x16x32_bf16 v[44:47], a[248:251], v[116:119], v[44:47]\n" \
    "ds_read_b128 v[104:107], v34 offset:2048\n" \
    "v_mfma_f32_16x16x32_bf16 v[52:55], a[252:255], v[116:119], v[52:55]\n" \
    "s_cbranch_vccnz LPQ_bp0r_%=\n" \
    "LPQ_bp0_%=:\n" \
    "s_waitcnt lgkmcnt(4)\n" \
    "v_mfma_f32_16x16x32_bf16 v[56:59], a[0:3], v[88:91], 0\n" \
    "ds_read_b128 v[108:111], v34 offset:34816\n" \
    "v_mfma_f32_16x16x32_bf16 v[64:67], a[4:7], v[88:91], 0\n" \
    "s_waitcnt lgkmcnt(4)\n" \
    "v_mfma_f32_16x16x32_bf16 v[60:63], a[0:3], v[92:95], 0\n" \
    "ds_read_b128 v[112:115], v35 offset:2048\n" \
    "v_mfma_f32_16x16x32_bf16 v[68:71], a[4:7], v[92:95], 0\n" \
    "s_barrier\n" \
    "s_waitcnt lgkmcnt(4)\n" \
    "v_mfma_f32_16x16x32_bf16 v[56:59], a[8:11], v[96:99], v[56:59]\n" \
    "ds_read_b128 v[116:119], v35 offset:34816\n" \
    "s_add_u32 s63, s62, 4096\n" \
    "v_mfma_f32_16x16x32_bf16 v[64:67], a[12:15], v[96:99], v[64:67]\n" \
    "v_cvt_pk_bf16_f32 v120, v40, v41\n" \
    "s_waitcnt lgkmcnt(4)\n" \
    "v_mfma_f32_16x16x32_bf16 v[60:63], a[8:11], v[100:103], v[60:63]\n" \
    "ds_read_b128 v[88:91], v34 offset:4096\n" \
    "v_cvt_pk_bf16_f32 v121, v42, v43\n" \
    "v_cvt_pk_bf16_f32 v122, v48, v49\n" \
    "v_mfma_f32_16x16x32_bf16 v[68:71], a[12:15], v[100:103], v[68:71]\n" \
    "s_xor_b32 m0, s61, 0\n" \
    "s_xor_b32 s97, s60, 0\n" \
    "buffer_load_dwordx4 %[vox], %[dx], s97 offen lds\n" \
    "s_waitcnt lgkmcnt(4)\n" \
    "v_mfma_f32_16x16x32_bf16 v[56:59], a[16:19], v[104:107], v[56:59]\n" \
    "ds_read_b128 v[92:95], v34 offset:36864\n" \
    "v_cvt_pk_bf16_f32 v123, v50, v51\n" \
    "v_mfma_f32_16x16x32_bf16 v[64:67], a[20:23], v[104:107], v[64:67]\n" \
    "buffer_store_dwordx4 v[120:123], %[voa], %[ws], s62 offen sc1\n" \
    "s_waitcnt lgkmcnt(4)\n" \
    "v_mfma_f32_16x16x32_bf16 v[60:63], a[16:19], v[108:111], v[60:63]\n" \
    "ds_read_b128 v[96:99], v35 offset:4096\n" \
    "v_cvt_pk_bf16_f32 v124, v44, v45\n" \
    "v_cvt_pk_bf16_f32 v125, v46, v47\n" \
    "v_mfma_f32_16x16x32_bf16 v[68:71], a[20:23], v[108:111], v[68:71]\n" \
    "s_xor_b32 m0, s61, 2048\n" \
    "s_xor_b32 s97, s60, 128\n" \
    "buffer_load_dwordx4 %[vox], %[dx], s97 offen lds\n" \
    "s_waitcnt lgkmcnt(4)\n" \
    "v_mfma_f32_16x16x32_bf16 v[56:59], a[24:27], v[112:115], v[56:59]\n" \
    "ds_read_b128 v[100:103], v35 offset:36864\n" \
    "v_cvt_pk_bf16_f32 v126, v52, v53\n" \
    "v_mfma_f32_16x16x32_bf16 v[64:67], a[28:31], v[112:115], v[64:67]\n" \
    "v_cvt_pk_bf16_f32 v127, v54, v55\n" \
    "s_waitcnt lgkmcnt(4)\n" \
    "v_mfma_f32_16x16x32_bf16 v[60:63], a[24:27], v[116:119], v[60:63]\n" \
    "ds_read_b128 v[104:107], v34 offset:6144\n" \
    "buffer_store_dwordx4 v[124:127], %[voa], %[ws], s63 offen sc1\n" \
    "s_mul_i32 s62, s95, 0x202000\n" \
    "v_mfma_f32_16x16x32_bf16 v[68:71], a[28:31], v[116:119], v[68:71]\n" \
    "s_xor_b32 m0, s61, 4096\n" \
    "s_xor_b32 s97, s60, 256\n" \
    "buffer_load_dwordx4 %[vox], %[dx], s97 offen lds\n" \
    "s_waitcnt lgkmcnt(4)\n" \
    "v_mfma_f32_16x16x32_bf16 v[56:59], a[32:35], v[88:91], v[56:59]\n" \
    "ds_read_b128 v[108:111], v34 offset:38912\n" \
    "s_and_b32 s62, s62, 0xffe02000\n" \
    "v_mfma_f32_16x16x32_bf16 v[64:67], a[36:39], v[88:91], v[64:67]\n" \
    "s_xor_b32 s62, s62, %[npol]\n" \
    "s_waitcnt lgkmcnt(4)\n" \
    "v_mfma_f32_16x16x32_bf16 v[60:63], a[32:35], v[92:95], v[60:63]\n" \
    "ds_read_b128 v[112:115], v35 offset:6144\n" \
    "s_cmp_eq_u32 s95, 1\n" \
    "s_cbranch_scc1 LPQ_e1_%=\n" \
    "LPQ_e2_%=:\n" \
    "s_xor_b32 m0, s61, 6144\n" \
    "v_mfma_f32_16x16x32_bf16 v[68:71], a[36:39], v[92:95], v[68:71]\n" \
    "s_xor_b32 s97, s60, 384\n" \
    "buffer_load_dwordx4 %[vox], %[dx], s97 offen lds\n" \
    "s_waitcnt lgkmcnt(4)\n" \
    "v_mfma_f32_16x16x32_bf16 v[56:59], a[40:43], v[96:99], v[56:59]\n" \
    "ds_read_b128 v[116:119], v35 offset:38912\n" \
    "v_mfma_f32_16x16x32_bf16 v[64:67], a[44:47], v[96:99], v[64:67]\n" \
    "s_waitcnt lgkmcnt(4)\n" \
    "v_mfma_f32_16x16x32_bf16 v[60:63], a[40:43], v[100:103], v[60:63]\n" \
    "ds_read_b128 v[88:91], v34 offset:8192\n" \
    "s_xor_b32 m0, s61, 8192\n" \
    "v_mfma_f32_16x16x32_bf16 v[68:71], a[44:47], v[100:103], v[68:71]\n" \
    "s_xor_b32 s97, s60, 512\n" \
    "buffer_load_dwordx4 %[vox], %[dx], s97 offen lds\n" \
    "s_waitcnt lgkmcnt(4)\n" \
    "v_mfma_f32_16x16x32_bf16 v[56:59], a[48:51], v[104:107], v[56:59]\n" \
    "ds_read_b128 v[92:95], v34 offset:40960\n" \
    "v_mfma_f32_16x16x32_bf16 v[64:67], a[52:55], v[104:107], v[64:67]\n" \
    "s_waitcnt lgkmcnt(4)\n" \
    "v_mfma_f32_16x16x32_bf16 v[60:63], a[48:51], v[108:111], v[60:63]\n" \
    "ds_read_b128 v[96:99], v35 offset:8192\n" \
    "s_xor_b32 m0, s61, 10240\n" \
    "v_mfma_f32_16x16x32_bf16 v[68:71], a[52:55], v[108:111], v[68:71]\n" \
    "s_xor_b32 s97, s60, 640\n" \
    "buffer_load_dwordx4 %[vox], %[dx], s97 offen lds\n" \
    "s_waitcnt lgkmcnt(4)\n" \
    "v_mfma_f32_16x16x32_bf16 v[56:59], a[56:59], v[112:115], v[56:59]\n" \
    "ds_read_b128 v[100:103], v35 offset:40960\n" \
    "v_mfma_f32_16x16x32_bf16 v[64:67], a[60:63], v[112:115], v[64:67]\n" \
    "s_waitcnt lgkmcnt(4)\n" \
    "v_mfma_f32_16x16x32_bf16 v[60:63], a[56:59], v[116:119], v[60:63]\n" \
    "ds_read_b128 v[104:107], v34 offset:10240\n" \
    "s_xor_b32 m0, s61, 12288\n" \
    "v_mfma_f32_16x16x32_bf16 v[68:71], a[60:63], v[116:119], v[68:71]\n" \
    "s_xor_b32 s97, s60, 768\n" \
    "buffer_load_dwordx4 %[vox], %[dx], s97 offen lds\n" \
    "s_waitcnt lgkmcnt(4)\n" \
    "v_mfma_f32_16x16x32_bf16 v[56:59], a[64:67], v[88:91], v[56:59]\n" \
    "ds_read_b128 v[108:111], v34 offset:43008\n" \
    "v_mfma_f32_16x16x32_bf16 v[64:67], a[68:71], v[88:91], v[64:67]\n" \
    "s_waitcnt lgkmcnt(4)\n" \
    "v_mfma_f32_16x16x32_bf16 v[60:63], a[64:67], v[92:95], v[60:63]\n" \
    "ds_read_b128 v[112:115], v35 offset:10240\n" \
    "s_xor_b32 m0, s61, 14336\n" \
    "v_mfma_f32_16x16x32_bf16 v[68:71], a[68:71], v[92:95], v[68:71]\n" \
    "s_xor_b32 s97, s60, 896\n" \
    "buffer_load_dwordx4 %[vox], %[dx], s97 offen lds\n" \
    "s_waitcnt lgkmcnt(4)\n" \
    "v_mfma_f32_16x16x32_bf16 v[56:59], a[72:75], v[96:99], v[56:59]\n" \
    "ds_read_b128 v[116:119], v35 offset:43008\n" \
    "v_mfma_f32_16x16x32_bf16 v[64:67], a[76:79], v[96:99], v[64:67]\n" \
    "s_waitcnt lgkmcnt(4)\n" \
    "v_mfma_f32_16x16x32_bf16 v[60:63], a[72:75], v[100:103], v[60:63]\n" \
    "ds_read_b128 v[88:91], v34 offset:12288\n" \
    "s_xor_b32 m0, s61, 16384\n" \
    "v_mfma_f32_16x16x32_bf16 v[68:71], a[76:79], v[100:103], v[68:71]\n" \
    "s_xor_b32 s97, s60, 1024\n" \
    "buffer_load_dwordx4 %[vox], %[dx], s97 offen lds\n" \
    "s_waitcnt lgkmcnt(4)\n" \
    "v_mfma_f32_16x16x32_bf16 v[56:59], a[80:83], v[104:107], v[56:59]\n" \
    "ds_read_b128 v[92:95], v34 offset:45056\n" \
    "v_mfma_f32_16x16x32_bf16 v[64:67], a[84:87], v[104:107], v[64:67]\n" \
    "s_waitcnt lgkmcnt(4)\n" \
    "v_mfma_f32_16x16x32_bf16 v[60:63], a[80:83], v[108:111], v[60:63]\n" \
    "ds_read_b128 v[96:99], v35 offset:12288\n" \
    "s_xor_b32 m0, s61, 18432\n" \
    "v_mfma_f32_16x16x32_bf16 v[68:71], a[84:87], v[108:111], v[68:71]\n" \
    "s_xor_b32 s97, s60, 1152\n" \
    "buffer_load_dwordx4 %[vox], %[dx], s97 offen lds\n" \
    "s_waitcnt lgkmcnt(4)\n" \
    "v_mfma_f32_16x16x32_bf16 v[56:59], a[88:91], v[112:115], v[56:59]\n" \
    "ds_read_b128 v[100:103], v35 offset:45056\n" \
    "v_mfma_f32_16x16x32_bf16 v[64:67], a[92:95], v[112:115], v[64:67]\n" \
    "s_waitcnt lgkmcnt(4)\n" \
    "v_mfma_f32_16x16x32_bf16 v[60:63], a[88:91], v[116:119], v[60:63]\n" \
    "ds_read_b128 v[104:107], v34 offset:14336\n" \
    "s_xor_b32 m0, s61, 20480\n" \
    "v_mfma_f32_16x16x32_bf16 v[68:71], a[92:95], v[116:119], v[68:71]\n" \
    "s_xor_b32 s97, s60, 1280\n" \
    "buffer_load_dwordx4 %[vox], %[dx], s97 offen lds\n" \
    "s_waitcnt lgkmcnt(4)\n" \
    "v_mfma_f32_16x16x32_bf16 v[56:59], a[96:99], v[88:91], v[56:59]\n" \
    "ds_read_b128 v[108:111], v34 offset:47104\n" \
    "v_mfma_f32_16x16x32_bf16 v[64:67], a[100:103], v[88:91], v[64:67]\n" \
    "s_waitcnt lgkmcnt(4)\n" \
    "v_mfma_f32_16x16x32_bf16 v[60:63], a[96:99], v[92:95], v[60:63]\n" \
    "ds_read_b128 v[112:115], v35 offset:14336\n" \
    "s_xor_b32 m0, s61, 22528\n" \
    "v_mfma_f32_16x16x32_bf16 v[68:71], a[100:103], v[92:95], v[68:71]\n" \
    "s_xor_b32 s97, s60, 1408\n" \
    "buffer_load_dwordx4 %[vox], %[dx], s97 offen lds\n" \
    "s_waitcnt lgkmcnt(4)\n" \
    "v_mfma_f32_16x16x32_bf16 v[56:59], a[104:107], v[96:99], v[56:59]\n" \
    "ds_read_b128 v[116:119], v35 offset:47104\n" \
    "v_mfma_f32_16x16x32_bf16 v[64:67], a[108:111], v[96:99], v[64:67]\n" \
    "s_waitcnt lgkmcnt(4)\n" \
    "v_mfma_f32_16x16x32_bf16 v[60:63], a[104:107], v[100:103], v[60:63]\n" \
    "ds_read_b128 v[88:91], v34 offset:16384\n" \
    "s_xor_b32 m0, s61, 24576\n" \
    "v_mfma_f32_16x16x32_bf16 v[68:71], a[108:111], v[100:103], v[68:71]\n" \
    "s_xor_b32 s97, s60, 1536\n" \
    "buffer_load_dwordx4 %[vox], %[dx], s97 offen lds\n" \
    "s_waitcnt lgkmcnt(4)\n" \
    "v_mfma_f32_16x16x32_bf16 v[56:59], a[112:115], v[104:107], v[56:59]\n" \
    "ds_read_b128 v[92:95], v34 offset:49152\n" \
    "v_mfma_f32_16x16x32_bf16 v[64:67], a[116:119], v[104:107], v[64:67]\n" \
    "s_waitcnt lgkmcnt(4)\n" \
    "v_mfma_f32_16x16x32_bf16 v[60:63], a[112:115], v[108:111], v[60:63]\n" \
    "ds_read_b128 v[96:99], v35 offset:16384\n" \
    "s_xor_b32 m0, s61, 26624\n" \
    "v_mfma_f32_16x16x32_bf16 v[68:71], a[116:119], v[108:111], v[68:71]\n" \
    "s_xor_b32 s97, s60, 1664\n" \
    "buffer_load_dwordx4 %[vox], %[dx], s97 offen lds\n" \
    "s_waitcnt lgkmcnt(4)\n" \
    "v_mfma_f32_16x16x32_bf16 v[56:59], a[120:123], v[112:115], v[56:59]\n" \
    "ds_read_b128 v[100:103], v35 offset:49152\n" \
    "v_mfma_f32_16x16x32_bf16 v[64:67], a[124:127], v[112:115], v[64:67]\n" \
    "s_waitcnt lgkmcnt(4)\n" \
    "v_mfma_f32_16x16x32_bf16 v[60:63], a[120:123], v[116:119], v[60:63]\n" \
    "ds_read_b128 v[104:107], v34 offset:18432\n" \
    "s_xor_b32 m0, s61, 28672\n" \
    "v_mfma_f32_16x16x32_bf16 v[68:71], a[124:127], v[116:119], v[68:71]\n" \
    "s_xor_b32 s97, s60, 1792\n" \
    "buffer_load_dwordx4 %[vox], %[dx], s97 offen lds\n" \
    "s_waitcnt lgkmcnt(4)\n" \
    "v_mfma_f32_16x16x32_bf16 v[56:59], a[128:131], v[88:91], v[56:59]\n" \
    "ds_read_b128 v[108:111], v34 offset:51200\n" \
    "v_mfma_f32_16x16x32_bf16 v[64:67], a[132:135], v[88:91], v[64:67]\n" \
    "s_waitcnt lgkmcnt(4)\n" \
    "v_mfma_f32_16x16x32_bf16 v[60:63], a[128:131], v[92:95], v[60:63]\n" \
    "ds_read_b128 v[112:115], v35 offset:18432\n" \
    "s_xor_b32 m0, s61, 30720\n" \
    "v_mfma_f32_16x16x32_bf16 v[68:71], a[132:135], v[92:95], v[68:71]\n" \
    "s_xor_b32 s97, s60, 1920\n" \
    "buffer_load_dwordx4 %[vox], %[dx], s97 offen lds\n" \
    "s_waitcnt lgkmcnt(4)\n" \
    "v_mfma_f32_16x16x32_bf16 v[56:59], a[136:139], v[96:99], v[56:59]\n" \
    "ds_read_b128 v[116:119], v35 offset:51200\n" \
    "v_mfma_f32_16x16x32_bf16 v[64:67], a[140:143], v[96:99], v[64:67]\n" \
    "s_waitcnt lgkmcnt(4)\n" \
    "v_mfma_f32_16x16x32_bf16 v[60:63], a[136:139], v[100:103], v[60:63]\n" \
    "ds_read_b128 v[88:91], v34 offset:20480\n" \
    "v_mfma_f32_16x16x32_bf16 v[68:71], a[140:143], v[100:103], v[68:71]\n" \
    "s_waitcnt lgkmcnt(4)\n" \
    "v_mfma_f32_16x16x32_bf16 v[56:59], a[144:147], v[104:107], v[56:59]\n" \
    "ds_read_b128 v[92:95], v34 offset:53248\n" \
    "v_mfma_f32_16x16x32_bf16 v[64:67], a[148:151], v[104:107], v[64:67]\n" \
    "s_waitcnt lgkmcnt(4)\n" \
    "v_mfma_f32_16x16x32_bf16 v[60:63], a[144:147], v[108:111], v[60:63]\n" \
    "ds_read_b128 v[96:99], v35 offset:20480\n" \
    "v_mfma_f32_16x16x32_bf16 v[68:71], a[148:151], v[108:111], v[68:71]\n" \
    "s_waitcnt lgkmcnt(4)\n" \
    "v_mfma_f32_16x16x32_bf16 v[56:59], a[152:155], v[112:115], v[56:59]\n" \
    "ds_read_b128 v[100:103], v35 offset:53248\n" \
    "v_mfma_f32_16x16x32_bf16 v[64:67], a[156:159], v[112:115], v[64:67]\n" \
    "s_waitcnt lgkmcnt(4)\n" \
    "v_mfma_f32_16x16x32_bf16 v[60:63], a[152:155], v[116:119], v[60:63]\n" \
    "ds_read_b128 v[104:107], v34 offset:22528\n" \
    "v_mfma_f32_16x16x32_bf16 v[68:71], a[156:159], v[116:119], v[68:71]\n" \
    "s_waitcnt lgkmcnt(4)\n" \
    "v_mfma_f32_16x16x32_bf16 v[56:59], a[160:163], v[88:91], v[56:59]\n" \
    "ds_read_b128 v[108:111], v34 offset:55296\n" \
    "buffer_load_dword v39, %[vbp], %[ws], %[pol] offen sc1\n" \
    "v_mfma_f32_16x16x32_bf16 v[64:67], a[164:167], v[88:91], v[64:67]\n" \
    "s_sub_u32 s66, s95, 8\n" \
    "s_waitcnt lgkmcnt(4)\n" \
    "v_mfma_f32_16x16x32_bf16 v[60:63], a[160:163], v[92:95], v[60:63]\n" \
    "ds_read_b128 v[112:115], v35 offset:22528\n" \
    "v_mfma_f32_16x16x32_bf16 v[68:71], a[164:167], v[92:95], v[68:71]\n" \
    "s_waitcnt lgkmcnt(4)\n" \
    "v_mfma_f32_16x16x32_bf16 v[56:59], a[168:171], v[96:99], v[56:59]\n" \
    "ds_read_b128 v[116:119], v35 offset:55296\n" \
    "v_mfma_f32_16x16x32_bf16 v[64:67], a[172:175], v[96:99], v[64:67]\n" \
    "s_waitcnt lgkmcnt(4)\n" \
    "v_mfma_f32_16x16x32_bf16 v[60:63], a[168:171], v[100:103], v[60:63]\n" \
    "ds_read_b128 v[88:91], v34 offset:24576\n" \
    "v_mfma_f32_16x16x32_bf16 v[68:71], a[172:175], v[100:103], v[68:71]\n" \
    "s_waitcnt lgkmcnt(4)\n" \
    "v_mfma_f32_16x16x32_bf16 v[56:59], a[176:179], v[104:107], v[56:59]\n" \
    "ds_read_b128 v[92:95], v34 offset:57344\n" \
    "v_mfma_f32_16x16x32_bf16 v[64:67], a[180:183], v[104:107], v[64:67]\n" \
    "s_waitcnt lgkmcnt(4)\n" \
    "v_mfma_f32_16x16x32_bf16 v[60:63], a[176:179], v[108:111], v[60:63]\n" \
    "ds_read_b128 v[96:99], v35 offset:24576\n" \
    "v_mfma_f32_16x16x32_bf16 v[68:71], a[180:183], v[108:111], v[68:71]\n" \
    "s_waitcnt lgkmcnt(4)\n" \
    "v_mfma_f32_16x16x32_bf16 v[56:59], a[184:187], v[112:115], v[56:59]\n" \
    "ds_read_b128 v[100:103], v35 offset:57344\n" \
    "v_mfma_f32_16x16x32_bf16 v[64:67], a[188:191], v[112:115], v[64:67]\n" \
    "s_waitcnt lgkmcnt(4)\n" \
    "v_mfma_f32_16x16x32_bf16 v[60:63], a[184:187], v[116:119], v[60:63]\n" \
    "ds_read_b128 v[104:107], v34 offset:26624\n" \
    "v_mfma_f32_16x16x32_bf16 v[68:71], a[188:191], v[116:119], v[68:71]\n" \
    "s_waitcnt lgkmcnt(4)\n" \
    "v_mfma_f32_16x16x32_bf16 v[56:59], a[192:195], v[88:91], v[56:59]\n" \
    "ds_read_b128 v[108:111], v34 offset:59392\n" \
    "v_mfma_f32_16x16x32_bf16 v[64:67], a[196:199], v[88:91], v[64:67]\n" \
    "s_waitcnt lgkmcnt(4)\n" \
    "v_mfma_f32_16x16x32_bf16 v[60:63], a[192:195], v[92:95], v[60:63]\n" \
    "ds_read_b128 v[112:115], v35 offset:26624\n" \
    "v_mfma_f32_16x16x32_bf16 v[68:71], a[196:199], v[92:95], v[68:71]\n" \
    "s_waitcnt lgkmcnt(4)\n" \
    "v_mfma_f32_16x16x32_bf16 v[56:59], a[200:203], v[96:99], v[56:59]\n" \
    "ds_read_b128 v[116:119], v35 offset:59392\n" \
    "v_mfma_f32_16x16x32_bf16 v[64:67], a[204:207], v[96:99], v[64:67]\n" \
    "s_waitcnt lgkmcnt(4)\n" \
    "v_mfma_f32_16x16x32_bf16 v[60:63], a[200:203], v[100:103], v[60:63]\n" \
    "ds_read_b128 v[88:91], v34 offset:28672\n" \
    "v_mfma_f32_16x16x32_bf16 v[68:71], a[204:207], v[100:103], v[68:71]\n" \
    "s_waitcnt lgkmcnt(4)\n" \
    "v_mfma_f32_16x16x32_bf16 v[56:59], a[208:211], v[104:107], v[56:59]\n" \
    "ds_read_b128 v[92:95], v34 offset:61440\n" \
    "v_mfma_f32_16x16x32_bf16 v[64:67], a[212:215], v[104:107], v[64:67]\n" \
    "s_waitcnt lgkmcnt(4)\n" \
    "v_mfma_f32_16x16x32_bf16 v[60:63], a[208:211], v[108:111], v[60:63]\n" \
    "ds_read_b128 v[96:99], v35 offset:28672\n" \
    "v_mfma_f32_16x16x32_bf16 v[68:71], a[212:215], v[108:111], v[68:71]\n" \
    "s_waitcnt lgkmcnt(4)\n" \
    "v_mfma_f32_16x16x32_bf16 v[56:59], a[216:219], v[112:115], v[56:59]\n" \
    "ds_read_b128 v[100:103], v35 offset:61440\n" \
    "v_mfma_f32_16x16x32_bf16 v[64:67], a[220:223], v[112:115], v[64:67]\n" \
    "s_waitcnt lgkmcnt(4)\n" \
    "v_mfma_f32_16x16x32_bf16 v[60:63], a[216:219], v[116:119], v[60:63]\n" \
    "ds_read_b128 v[104:107], v34 offset:30720\n" \
    "v_mfma_f32_16x16x32_bf16 v[68:71], a[220:223], v[116:119], v[68:71]\n" \
    "s_waitcnt lgkmcnt(4)\n" \
    "v_mfma_f32_16x16x32_bf16 v[56:59], a[224:227], v[88:91], v[56:59]\n" \
    "ds_read_b128 v[108:111], v34 offset:63488\n" \
    "v_mfma_f32_16x16x32_bf16 v[64:67], a[228:231], v[88:91], v[64:67]\n" \
    "s_waitcnt lgkmcnt(4)\n" \
    "v_mfma_f32_16x16x32_bf16 v[60:63], a[224:227], v[92:95], v[60:63]\n" \
    "ds_read_b128 v[112:115], v35 offset:30720\n" \
    "v_mfma_f32_16x16x32_bf16 v[68:71], a[228:231], v[92:95], v[68:71]\n" \
    "s_waitcnt lgkmcnt(4)\n" \
    "v_mfma_f32_16x16x32_bf16 v[56:59], a[232:235], v[96:99], v[56:59]\n" \
    "ds_read_b128 v[116:119], v35 offset:63488\n" \
    "s_waitcnt vmcnt(0)\n" \
    "v_mfma_f32_16x16x32_bf16 v[64:67], a[236:239], v[96:99], v[64:67]\n" \
    "s_barrier\n" \
    "s_waitcnt lgkmcnt(4)\n" \
    "v_mfma_f32_16x16x32_bf16 v[60:63], a[232:235], v[100:103], v[60:63]\n" \
    "ds_read_b128 v[88:91], v32 offset:0\n" \
    "s_sub_u32 s96, s95, 1\n" \
    "v_mfma_f32_16x16x32_bf16 v[68:71], a[236:239], v[100:103], v[68:71]\n" \
    "v_mov_b32 v36, s96\n" \
    "buffer_store_dword v36, %[vfl], %[ws], %[npol] offen sc1\n" \
    "s_waitcnt lgkmcnt(4)\n" \
    "v_mfma_f32_16x16x32_bf16 v[56:59], a[240:243], v[104:107], v[56:59]\n" \
    "ds_read_b128 v[92:95], v32 offset:32768\n" \
    "s_add_u32 s95, s95, 1\n" \
    "v_mfma_f32_16x16x32_bf16 v[64:67], a[244:247], v[104:107], v[64:67]\n" \
    "s_add_u32 s60, s60, 65536\n" \
    "s_and_b32 s60, s60, 0x3fffff\n" \
    "s_waitcnt lgkmcnt(4)\n" \
    "v_mfma_f32_16x16x32_bf16 v[60:63], a[240:243], v[108:111], v[60:63]\n" \
    "ds_read_b128 v[96:99], v33 offset:0\n" \
    "s_add_u32 s97, s60, 65536\n" \
    "v_mfma_f32_16x16x32_bf16 v[68:71], a[244:247], v[108:111], v[68:71]\n" \
    "s_and_b32 s97, s97, 0x3f0000\n" \
    "buffer_load_dword v38, %[vxt], %[dx], s97 offen\n" \
    "s_waitcnt lgkmcnt(4)\n" \
    "v_mfma_f32_16x16x32_bf16 v[56:59], a[248:251], v[112:115], v[56:59]\n" \
    "ds_read_b128 v[100:103], v33 offset:32768\n" \
    "v_mfma_f32_16x16x32_bf16 v[64:67], a[252:255], v[112:115], v[64:67]\n" \
    "v_cmp_gt_i32 vcc, s66, v39\n" \
    "s_waitcnt lgkmcnt(4)\n" \
    "v_mfma_f32_16x16x32_bf16 v[60:63], a[248:251], v[116:119], v[60:63]\n" \
    "ds_read_b128 v[104:107], v32 offset:2048\n" \
    "v_mfma_f32_16x16x32_bf16 v[68:71], a[252:255], v[116:119], v[68:71]\n" \
    "s_cbranch_vccnz LPQ_bp1r_%=\n" \
    "LPQ_bp1_%=:\n" \
    "s_cmp_lt_u32 s95, 64\n" \
    "s_cbranch_scc1 LPQ_seg_%=\n" \
    "s_nop 7\n" \
    "s_nop 1\n" \
    "s_add_u32 s63, s62, 4096\n" \
    "v_cvt_pk_bf16_f32 v120, v56, v57\n" \
    "v_cvt_pk_bf16_f32 v121, v58, v59\n" \
    "v_cvt_pk_bf16_f32 v122, v64, v65\n" \
    "v_cvt_pk_bf16_f32 v123, v66, v67\n" \
    "buffer_store_dwordx4 v[120:123], %[voa], %[ws], s62 offen sc1\n" \
    "v_cvt_pk_bf16_f32 v124, v60, v61\n" \
    "v_cvt_pk_bf16_f32 v125, v62, v63\n" \
    "v_cvt_pk_bf16_f32 v126, v68, v69\n" \
    "v_cvt_pk_bf16_f32 v127, v70, v71\n" \
    "buffer_store_dwordx4 v[124:127], %[voa], %[ws], s63 offen sc1\n" \
    "s_mul_i32 s62, s95, 0x202000\n" \
    "s_and_b32 s62, s62, 0xffe02000\n" \
    "s_xor_b32 s62, s62, %[npol]\n" \
    "s_waitcnt vmcnt(0) lgkmcnt(0)\n" \
    "s_barrier\n" \
    "v_mov_b32 v36, 63\n" \
    "buffer_store_dword v36, %[vfl], %[ws], %[npol] offen sc1\n" \
    "s_branch LPQ_end_%=\n" \
    "LPQ_hsr_%=:\n" \
    "s_sleep 2\n" \
    "s_add_u32 s98, s98, 1\n" \
    "s_cmp_gt_u32 s98, 131072\n" \
    "s_cbranch_scc1 LPQ_fail_%=\n" \
    "s_branch LPQ_hs_%=\n" \
    "LPQ_redo_%=:\n" \
    "s_waitcnt vmcnt(0) lgkmcnt(0)\n" \
    "s_barrier\n" \
    "s_or_b32 %[cfg], s97, 256\n" \
    "s_branch LPQ_end_%=\n" \
    "LPQ_bp0r_%=:\n" \
    "s_sleep 8\n" \
    "buffer_load_dword v39, %[vbp], %[ws], %[pol] offen sc1\n" \
    "s_waitcnt vmcnt(0)\n" \
    "v_cmp_gt_i32 vcc, s66, v39\n" \
    "s_cbranch_vccz LPQ_bp0_%=\n" \
    "s_add_u32 s98, s98, 1\n" \
    "s_cmp_gt_u32 s98, 131072\n" \
    "s_cbranch_scc1 LPQ_fail_%=\n" \
    "s_branch LPQ_bp0r_%=\n" \
    "LPQ_e1_%=:\n" \
    "s_waitcnt vmcnt(0)\n" \
    "s_barrier\n" \
    "s_sub_u32 s96, s95, 1\n" \
    "v_mov_b32 v36, s96\n" \
    "buffer_store_dword v36, %[vfl], %[ws], %[npol] offen sc1\n" \
    "s_branch LPQ_e2_%=\n" \
    "LPQ_bp1r_%=:\n" \
    "s_sleep 8\n" \
    "buffer_load_dword v39, %[vbp], %[ws], %[pol] offen sc1\n" \
    "s_waitcnt vmcnt(0)\n" \
    "v_cmp_gt_i32 vcc, s66, v39\n" \
    "s_cbranch_vccz LPQ_bp1_%=\n" \
    "s_add_u32 s98, s98, 1\n" \
    "s_cmp_gt_u32 s98, 131072\n" \
    "s_cbranch_scc1 LPQ_fail_%=\n" \
    "s_branch LPQ_bp1r_%=\n" \
    "LPQ_fail_%=:\n" \
    "v_mov_b32 v36, 2043904\n" \
    "v_mov_b32 v37, 1\n" \
    "buffer_store_dword v37, v36, %[ws], 0 offen sc1\n" \
    "s_endpgm\n" \
    "LPQ_end_%=:\n"
#define ASM_ROLE_S1 \
    "s_nop 4\n" \
    "s_mov_b32 s98, 0\n" \
    "s_mov_b32 s95, 0\n" \
    "s_add_u32 s97, %[hsa], 32768\n" \
    "buffer_load_dword v31, off, %[ws], s97 sc1\n" \
    "ds_read_b128 v[128:131], %[kc] offset:0\n" \
    "ds_read_b128 v[132:135], %[kc] offset:4096\n" \
    "ds_read_b128 v[136:139], %[kc] offset:8192\n" \
    "ds_read_b128 v[140:143], %[kc] offset:12288\n" \
    "ds_read_b128 v[144:147], %[kc] offset:16384\n" \
    "ds_read_b128 v[148:151], %[kc] offset:20480\n" \
    "ds_read_b128 v[152:155], %[kc] offset:24576\n" \
    "ds_read_b128 v[156:159], %[kc] offset:28672\n" \
    "ds_read_b128 v[160:163], %[kc] offset:32768\n" \
    "ds_read_b128 v[164:167], %[kc] offset:36864\n" \
    "ds_read_b128 v[168:171], %[kc] offset:40960\n" \
    "ds_read_b128 v[172:175], %[kc] offset:45056\n" \
    "ds_read_b128 v[176:179], %[kc] offset:49152\n" \
    "ds_read_b128 v[180:183], %[kc] offset:53248\n" \
    "v_mov_b64 v[40:41], 0\n" \
    "v_mov_b64 v[42:43], 0\n" \
    "v_mov_b64 v[44:45], 0\n" \
    "v_mov_b64 v[46:47], 0\n" \
    "v_mov_b64 v[48:49], 0\n" \
    "v_mov_b64 v[50:51], 0\n" \
    "v_mov_b64 v[52:53], 0\n" \
    "v_mov_b64 v[54:55], 0\n" \
    "v_mov_b64 v[56:57], 0\n" \
    "v_mov_b64 v[58:59], 0\n" \
    "v_mov_b64 v[60:61], 0\n" \
    "v_mov_b64 v[62:63], 0\n" \
    "v_mov_b64 v[64:65], 0\n" \
    "v_mov_b64 v[66:67], 0\n" \
    "v_mov_b64 v[68:69], 0\n" \
    "v_mov_b64 v[70:71], 0\n" \
    "v_mov_b64 v[200:201], 0\n" \
    "v_mov_b64 v[202:203], 0\n" \
    "v_mov_b64 v[204:205], 0\n" \
    "v_mov_b64 v[206:207], 0\n" \
    "s_waitcnt lgkmcnt(0)\n" \
    "s_barrier\n" \
    "v_add_u32 v88, 32768, %[vow]\n" \
    "v_xor_b32 v88, 64, v88\n" \
    "s_lshl_b32 s64, %[w], 15\n" \
    "v_add_u32 v89, s64, %[fo]\n" \
    "v_xor_b32 v90, 64, v89\n" \
    "s_add_u32 m0, s64, 0\n" \
    "s_mov_b32 s97, 0\n" \
    "buffer_load_dwordx4 %[vow], %[dr], s97 offen nt lds\n" \
    "s_add_u32 m0, s64, 1024\n" \
    "s_mov_b32 s97, 0\n" \
    "buffer_load_dwordx4 v88, %[dr], s97 offen nt lds\n" \
    "s_add_u32 m0, s64, 2048\n" \
    "s_mov_b32 s97, 128\n" \
    "buffer_load_dwordx4 %[vow], %[dr], s97 offen nt lds\n" \
    "s_add_u32 m0, s64, 3072\n" \
    "s_mov_b32 s97, 128\n" \
    "buffer_load_dwordx4 v88, %[dr], s97 offen nt lds\n" \
    "s_add_u32 m0, s64, 4096\n" \
    "s_mov_b32 s97, 256\n" \
    "buffer_load_dwordx4 %[vow], %[dr], s97 offen nt lds\n" \
    "s_add_u32 m0, s64, 5120\n" \
    "s_mov_b32 s97, 256\n" \
    "buffer_load_dwordx4 v88, %[dr], s97 offen nt lds\n" \
    "s_add_u32 m0, s64, 6144\n" \
    "s_mov_b32 s97, 384\n" \
    "buffer_load_dwordx4 %[vow], %[dr], s97 offen nt lds\n" \
    "s_add_u32 m0, s64, 7168\n" \
    "s_mov_b32 s97, 384\n" \
    "buffer_load_dwordx4 v88, %[dr], s97 offen nt lds\n" \
    "s_add_u32 m0, s64, 8192\n" \
    "s_mov_b32 s97, 512\n" \
    "buffer_load_dwordx4 %[vow], %[dr], s97 offen nt lds\n" \
    "s_add_u32 m0, s64, 9216\n" \
    "s_mov_b32 s97, 512\n" \
    "buffer_load_dwordx4 v88, %[dr], s97 offen nt lds\n" \
    "s_add_u32 m0, s64, 10240\n" \
    "s_mov_b32 s97, 640\n" \
    "buffer_load_dwordx4 %[vow], %[dr], s97 offen nt lds\n" \
    "s_add_u32 m0, s64, 11264\n" \
    "s_mov_b32 s97, 640\n" \
    "buffer_load_dwordx4 v88, %[dr], s97 offen nt lds\n" \
    "s_add_u32 m0, s64, 12288\n" \
    "s_mov_b32 s97, 768\n" \
    "buffer_load_dwordx4 %[vow], %[dr], s97 offen nt lds\n" \
    "s_add_u32 m0, s64, 13312\n" \
    "s_mov_b32 s97, 768\n" \
    "buffer_load_dwordx4 v88, %[dr], s97 offen nt lds\n" \
    "s_add_u32 m0, s64, 14336\n" \
    "s_mov_b32 s97, 896\n" \
    "buffer_load_dwordx4 %[vow], %[dr], s97 offen nt lds\n" \
    "s_add_u32 m0, s64, 15360\n" \
    "s_mov_b32 s97, 896\n" \
    "buffer_load_dwordx4 v88, %[dr], s97 offen nt lds\n" \
    "s_add_u32 m0, s64, 16384\n" \
    "s_mov_b32 s97, 1024\n" \
    "buffer_load_dwordx4 %[vow], %[dr], s97 offen nt lds\n" \
    "s_add_u32 m0, s64, 17408\n" \
    "s_mov_b32 s97, 1024\n" \
    "buffer_load_dwordx4 v88, %[dr], s97 offen nt lds\n" \
    "s_add_u32 m0, s64, 18432\n" \
    "s_mov_b32 s97, 1152\n" \
    "buffer_load_dwordx4 %[vow], %[dr], s97 offen nt lds\n" \
    "s_add_u32 m0, s64, 19456\n" \
    "s_mov_b32 s97, 1152\n" \
    "buffer_load_dwordx4 v88, %[dr], s97 offen nt lds\n" \
    "s_add_u32 m0, s64, 20480\n" \
    "s_mov_b32 s97, 1280\n" \
    "buffer_load_dwordx4 %[vow], %[dr], s97 offen nt lds\n" \
    "s_add_u32 m0, s64, 21504\n" \
    "s_mov_b32 s97, 1280\n" \
    "buffer_load_dwordx4 v88, %[dr], s97 offen nt lds\n" \
    "s_add_u32 m0, s64, 22528\n" \
    "s_mov_b32 s97, 1408\n" \
    "buffer_load_dwordx4 %[vow], %[dr], s97 offen nt lds\n" \
    "s_add_u32 m0, s64, 23552\n" \
    "s_mov_b32 s97, 1408\n" \
    "buffer_load_dwordx4 v88, %[dr], s97 offen nt lds\n" \
    "s_add_u32 m0, s64, 24576\n" \
    "s_mov_b32 s97, 1536\n" \
    "buffer_load_dwordx4 %[vow], %[dr], s97 offen nt lds\n" \
    "s_add_u32 m0, s64, 25600\n" \
    "s_mov_b32 s97, 1536\n" \
    "buffer_load_dwordx4 v88, %[dr], s97 offen nt lds\n" \
    "s_add_u32 m0, s64, 26624\n" \
    "s_mov_b32 s97, 1664\n" \
    "buffer_load_dwordx4 %[vow], %[dr], s97 offen nt lds\n" \
    "s_add_u32 m0, s64, 27648\n" \
    "s_mov_b32 s97, 1664\n" \
    "buffer_load_dwordx4 v88, %[dr], s97 offen nt lds\n" \
    "s_add_u32 m0, s64, 28672\n" \
    "s_mov_b32 s97, 1792\n" \
    "buffer_load_dwordx4 %[vow], %[dr], s97 offen nt lds\n" \
    "s_add_u32 m0, s64, 29696\n" \
    "s_mov_b32 s97, 1792\n" \
    "buffer_load_dwordx4 v88, %[dr], s97 offen nt lds\n" \
    "s_add_u32 m0, s64, 30720\n" \
    "s_mov_b32 s97, 1920\n" \
    "buffer_load_dwordx4 %[vow], %[dr], s97 offen nt lds\n" \
    "s_add_u32 m0, s64, 31744\n" \
    "s_mov_b32 s97, 1920\n" \
    "buffer_load_dwordx4 v88, %[dr], s97 offen nt lds\n" \
    "s_waitcnt vmcnt(24)\n" \
    "v_readfirstlane_b32 %[cfg], v31\n" \
    "s_nop 1\n" \
    "s_add_u32 %[cfg], %[cfg], 256\n" \
    "s_and_b32 %[cfg], %[cfg], 0xffffff00\n" \
    "v_mov_b32 v31, %[cfg]\n" \
    "buffer_store_dword v31, off, %[ws], %[hsa] sc1\n" \
    "ds_read_b128 a[0:3], v89 offset:0\n" \
    "ds_read_b128 a[8:11], v90 offset:0\n" \
    "ds_read_b128 a[16:19], v89 offset:2048\n" \
    "ds_read_b128 a[24:27], v90 offset:2048\n" \
    "ds_read_b128 a[32:35], v89 offset:4096\n" \
    "ds_read_b128 a[40:43], v90 offset:4096\n" \
    "ds_read_b128 a[48:51], v89 offset:6144\n" \
    "ds_read_b128 a[56:59], v90 offset:6144\n" \
    "s_waitcnt lgkmcnt(0)\n" \
    "s_add_u32 m0, s64, 0\n" \
    "s_mov_b32 s97, 0\n" \
    "buffer_load_dwordx4 %[vow], %[di], s97 offen nt lds\n" \
    "s_add_u32 m0, s64, 1024\n" \
    "s_mov_b32 s97, 0\n" \
    "buffer_load_dwordx4 v88, %[di], s97 offen nt lds\n" \
    "s_add_u32 m0, s64, 2048\n" \
    "s_mov_b32 s97, 128\n" \
    "buffer_load_dwordx4 %[vow], %[di], s97 offen nt lds\n" \
    "s_add_u32 m0, s64, 3072\n" \
    "s_mov_b32 s97, 128\n" \
    "buffer_load_dwordx4 v88, %[di], s97 offen nt lds\n" \
    "s_add_u32 m0, s64, 4096\n" \
    "s_mov_b32 s97, 256\n" \
    "buffer_load_dwordx4 %[vow], %[di], s97 offen nt lds\n" \
    "s_add_u32 m0, s64, 5120\n" \
    "s_mov_b32 s97, 256\n" \
    "buffer_load_dwordx4 v88, %[di], s97 offen nt lds\n" \
    "s_add_u32 m0, s64, 6144\n" \
    "s_mov_b32 s97, 384\n" \
    "buffer_load_dwordx4 %[vow], %[di], s97 offen nt lds\n" \
    "s_add_u32 m0, s64, 7168\n" \
    "s_mov_b32 s97, 384\n" \
    "buffer_load_dwordx4 v88, %[di], s97 offen nt lds\n" \
    "s_waitcnt vmcnt(24)\n" \
    "ds_read_b128 a[64:67], v89 offset:8192\n" \
    "ds_read_b128 a[72:75], v90 offset:8192\n" \
    "ds_read_b128 a[80:83], v89 offset:10240\n" \
    "ds_read_b128 a[88:91], v90 offset:10240\n" \
    "ds_read_b128 a[96:99], v89 offset:12288\n" \
    "ds_read_b128 a[104:107], v90 offset:12288\n" \
    "ds_read_b128 a[112:115], v89 offset:14336\n" \
    "ds_read_b128 a[120:123], v90 offset:14336\n" \
    "s_waitcnt lgkmcnt(0)\n" \
    "s_add_u32 m0, s64, 8192\n" \
    "s_mov_b32 s97, 512\n" \
    "buffer_load_dwordx4 %[vow], %[di], s97 offen nt lds\n" \
    "s_add_u32 m0, s64, 9216\n" \
    "s_mov_b32 s97, 512\n" \
    "buffer_load_dwordx4 v88, %[di], s97 offen nt lds\n" \
    "s_add_u32 m0, s64, 10240\n" \
    "s_mov_b32 s97, 640\n" \
    "buffer_load_dwordx4 %[vow], %[di], s97 offen nt lds\n" \
    "s_add_u32 m0, s64, 11264\n" \
    "s_mov_b32 s97, 640\n" \
    "buffer_load_dwordx4 v88, %[di], s97 offen nt lds\n" \
    "s_add_u32 m0, s64, 12288\n" \
    "s_mov_b32 s97, 768\n" \
    "buffer_load_dwordx4 %[vow], %[di], s97 offen nt lds\n" \
    "s_add_u32 m0, s64, 13312\n" \
    "s_mov_b32 s97, 768\n" \
    "buffer_load_dwordx4 v88, %[di], s97 offen nt lds\n" \
    "s_add_u32 m0, s64, 14336\n" \
    "s_mov_b32 s97, 896\n" \
    "buffer_load_dwordx4 %[vow], %[di], s97 offen nt lds\n" \
    "s_add_u32 m0, s64, 15360\n" \
    "s_mov_b32 s97, 896\n" \
    "buffer_load_dwordx4 v88, %[di], s97 offen nt lds\n" \
    "s_waitcnt vmcnt(24)\n" \
    "ds_read_b128 a[128:131], v89 offset:16384\n" \
    "ds_read_b128 a[136:139], v90 offset:16384\n" \
    "ds_read_b128 a[144:147], v89 offset:18432\n" \
    "ds_read_b128 a[152:155], v90 offset:18432\n" \
    "ds_read_b128 a[160:163], v89 offset:20480\n" \
    "ds_read_b128 a[168:171], v90 offset:20480\n" \
    "ds_read_b128 a[176:179], v89 offset:22528\n" \
    "ds_read_b128 a[184:187], v90 offset:22528\n" \
    "s_waitcnt lgkmcnt(0)\n" \
    "s_add_u32 m0, s64, 16384\n" \
    "s_mov_b32 s97, 1024\n" \
    "buffer_load_dwordx4 %[vow], %[di], s97 offen nt lds\n" \
    "s_add_u32 m0, s64, 17408\n" \
    "s_mov_b32 s97, 1024\n" \
    "buffer_load_dwordx4 v88, %[di], s97 offen nt lds\n" \
    "s_add_u32 m0, s64, 18432\n" \
    "s_mov_b32 s97, 1152\n" \
    "buffer_load_dwordx4 %[vow], %[di], s97 offen nt lds\n" \
    "s_add_u32 m0, s64, 19456\n" \
    "s_mov_b32 s97, 1152\n" \
    "buffer_load_dwordx4 v88, %[di], s97 offen nt lds\n" \
    "s_add_u32 m0, s64, 20480\n" \
    "s_mov_b32 s97, 1280\n" \
    "buffer_load_dwordx4 %[vow], %[di], s97 offen nt lds\n" \
    "s_add_u32 m0, s64, 21504\n" \
    "s_mov_b32 s97, 1280\n" \
    "buffer_load_dwordx4 v88, %[di], s97 offen nt lds\n" \
    "s_add_u32 m0, s64, 22528\n" \
    "s_mov_b32 s97, 1408\n" \
    "buffer_load_dwordx4 %[vow], %[di], s97 offen nt lds\n" \
    "s_add_u32 m0, s64, 23552\n" \
    "s_mov_b32 s97, 1408\n" \
    "buffer_load_dwordx4 v88, %[di], s97 offen nt lds\n" \
    "s_waitcnt vmcnt(24)\n" \
    "ds_read_b128 a[192:195], v89 offset:24576\n" \
    "ds_read_b128 a[200:203], v90 offset:24576\n" \
    "ds_read_b128 a[208:211], v89 offset:26624\n" \
    "ds_read_b128 a[216:219], v90 offset:26624\n" \
    "ds_read_b128 a[224:227], v89 offset:28672\n" \
    "ds_read_b128 a[232:235], v90 offset:28672\n" \
    "ds_read_b128 a[240:243], v89 offset:30720\n" \
    "ds_read_b128 a[248:251], v90 offset:30720\n" \
    "s_waitcnt lgkmcnt(0)\n" \
    "s_add_u32 m0, s64, 24576\n" \
    "s_mov_b32 s97, 1536\n" \
    "buffer_load_dwordx4 %[vow], %[di], s97 offen nt lds\n" \
    "s_add_u32 m0, s64, 25600\n" \
    "s_mov_b32 s97, 1536\n" \
    "buffer_load_dwordx4 v88, %[di], s97 offen nt lds\n" \
    "s_add_u32 m0, s64, 26624\n" \
    "s_mov_b32 s97, 1664\n" \
    "buffer_load_dwordx4 %[vow], %[di], s97 offen nt lds\n" \
    "s_add_u32 m0, s64, 27648\n" \
    "s_mov_b32 s97, 1664\n" \
    "buffer_load_dwordx4 v88, %[di], s97 offen nt lds\n" \
    "s_add_u32 m0, s64, 28672\n" \
    "s_mov_b32 s97, 1792\n" \
    "buffer_load_dwordx4 %[vow], %[di], s97 offen nt lds\n" \
    "s_add_u32 m0, s64, 29696\n" \
    "s_mov_b32 s97, 1792\n" \
    "buffer_load_dwordx4 v88, %[di], s97 offen nt lds\n" \
    "s_add_u32 m0, s64, 30720\n" \
    "s_mov_b32 s97, 1920\n" \
    "buffer_load_dwordx4 %[vow], %[di], s97 offen nt lds\n" \
    "s_add_u32 m0, s64, 31744\n" \
    "s_mov_b32 s97, 1920\n" \
    "buffer_load_dwordx4 v88, %[di], s97 offen nt lds\n" \
    "s_waitcnt vmcnt(24)\n" \
    "ds_read_b128 a[4:7], v89 offset:0\n" \
    "ds_read_b128 a[12:15], v90 offset:0\n" \
    "ds_read_b128 a[20:23], v89 offset:2048\n" \
    "ds_read_b128 a[28:31], v90 offset:2048\n" \
    "ds_read_b128 a[36:39], v89 offset:4096\n" \
    "ds_read_b128 a[44:47], v90 offset:4096\n" \
    "ds_read_b128 a[52:55], v89 offset:6144\n" \
    "ds_read_b128 a[60:63], v90 offset:6144\n" \
    "s_waitcnt lgkmcnt(0)\n" \
    "s_waitcnt vmcnt(16)\n" \
    "ds_read_b128 a[68:71], v89 offset:8192\n" \
    "ds_read_b128 a[76:79], v90 offset:8192\n" \
    "ds_read_b128 a[84:87], v89 offset:10240\n" \
    "ds_read_b128 a[92:95], v90 offset:10240\n" \
    "ds_read_b128 a[100:103], v89 offset:12288\n" \
    "ds_read_b128 a[108:111], v90 offset:12288\n" \
    "ds_read_b128 a[116:119], v89 offset:14336\n" \
    "ds_read_b128 a[124:127], v90 offset:14336\n" \
    "s_waitcnt lgkmcnt(0)\n" \
    "s_waitcnt vmcnt(8)\n" \
    "ds_read_b128 a[132:135], v89 offset:16384\n" \
    "ds_read_b128 a[140:143], v90 offset:16384\n" \
    "ds_read_b128 a[148:151], v89 offset:18432\n" \
    "ds_read_b128 a[156:159], v90 offset:18432\n" \
    "ds_read_b128 a[164:167], v89 offset:20480\n" \
    "ds_read_b128 a[172:175], v90 offset:20480\n" \
    "ds_read_b128 a[180:183], v89 offset:22528\n" \
    "ds_read_b128 a[188:191], v90 offset:22528\n" \
    "s_waitcnt lgkmcnt(0)\n" \
    "s_waitcnt vmcnt(0)\n" \
    "LS1_hs_%=:\n" \
    "s_add_u32 s97, %[hsa], 32768\n" \
    "buffer_load_dword v31, off, %[ws], s97 sc1\n" \
    "s_waitcnt vmcnt(0)\n" \
    "v_readfirstlane_b32 s97, v31\n" \
    "s_nop 1\n" \
    "s_xor_b32 s97, s97, %[cfg]\n" \
    "s_cmp_gt_u32 s97, 255\n" \
    "s_cbranch_scc1 LS1_hsr_%=\n" \
    "s_and_b32 s96, s97, 3\n" \
    "s_cmp_lg_u32 s96, %[est]\n" \
    "s_cbranch_scc1 LS1_redo_%=\n" \
    "s_bitcmp0_b32 s97, 3\n" \
    "s_cbranch_scc1 LS1_hsr_%=\n" \
    "s_mov_b32 %[cfg], s97\n" \
    "LS1_hsok_%=:\n" \
    "s_and_b32 %[pol], %[cfg], 4\n" \
    "s_lshl_b32 %[pol], %[pol], 11\n" \
    "s_xor_b32 %[npol], %[pol], 8192\n" \
    "s_waitcnt vmcnt(0)\n" \
    "v_mov_b32 v31, 0\n" \
    "s_add_u32 s97, %[hsa], 69632\n" \
    "buffer_store_dword v31, off, %[ws], s97 sc1\n" \
    "ds_read_b128 a[196:199], v89 offset:24576\n" \
    "ds_read_b128 a[204:207], v90 offset:24576\n" \
    "ds_read_b128 a[212:215], v89 offset:26624\n" \
    "ds_read_b128 a[220:223], v90 offset:26624\n" \
    "ds_read_b128 a[228:231], v89 offset:28672\n" \
    "ds_read_b128 a[236:239], v90 offset:28672\n" \
    "ds_read_b128 a[244:247], v89 offset:30720\n" \
    "ds_read_b128 a[252:255], v90 offset:30720\n" \
    "s_waitcnt lgkmcnt(0)\n" \
    "s_barrier\n" \
    "v_mov_b32 v32, %[fo]\n" \
    "v_xor_b32 v33, 64, %[fo]\n" \
    "v_add_u32 v34, 0x10000, v32\n" \
    "v_add_u32 v35, 0x10000, v33\n" \
    "s_lshr_b32 s61, %[w], 1\n" \
    "s_lshl_b32 s61, s61, 5\n" \
    "s_and_b32 s97, %[w], 1\n" \
    "s_add_u32 s61, s61, s97\n" \
    "s_lshl_b32 s61, s61, 10\n" \
    "s_lshl_b32 s97, %[w], 13\n" \
    "s_add_u32 s61, s61, s97\n" \
    "s_lshl_b32 s60, %[w], 9\n" \
    "s_xor_b32 m0, s61, 0\n" \
    "s_xor_b32 s97, s60, 0\n" \
    "buffer_load_dwordx4 %[vox], %[dx], s97 offen lds\n" \
    "s_xor_b32 m0, s61, 2048\n" \
    "s_xor_b32 s97, s60, 128\n" \
    "buffer_load_dwordx4 %[vox], %[dx], s97 offen lds\n" \
    "s_xor_b32 m0, s61, 4096\n" \
    "s_xor_b32 s97, s60, 256\n" \
    "buffer_load_dwordx4 %[vox], %[dx], s97 offen lds\n" \
    "s_xor_b32 m0, s61, 6144\n" \
    "s_xor_b32 s97, s60, 384\n" \
    "buffer_load_dwordx4 %[vox], %[dx], s97 offen lds\n" \
    "s_xor_b32 m0, s61, 8192\n" \
    "s_xor_b32 s97, s60, 512\n" \
    "buffer_load_dwordx4 %[vox], %[dx], s97 offen lds\n" \
    "s_xor_b32 m0, s61, 10240\n" \
    "s_xor_b32 s97, s60, 640\n" \
    "buffer_load_dwordx4 %[vox], %[dx], s97 offen lds\n" \
    "s_xor_b32 m0, s61, 12288\n" \
    "s_xor_b32 s97, s60, 768\n" \
    "buffer_load_dwordx4 %[vox], %[dx], s97 offen lds\n" \
    "s_xor_b32 m0, s61, 14336\n" \
    "s_xor_b32 s97, s60, 896\n" \
    "buffer_load_dwordx4 %[vox], %[dx], s97 offen lds\n" \
    "s_xor_b32 m0, s61, 16384\n" \
    "s_xor_b32 s97, s60, 1024\n" \
    "buffer_load_dwordx4 %[vox], %[dx], s97 offen lds\n" \
    "s_xor_b32 m0, s61, 18432\n" \
    "s_xor_b32 s97, s60, 1152\n" \
    "buffer_load_dwordx4 %[vox], %[dx], s97 offen lds\n" \
    "s_xor_b32 m0, s61, 20480\n" \
    "s_xor_b32 s97, s60, 1280\n" \
    "buffer_load_dwordx4 %[vox], %[dx], s97 offen lds\n" \
    "s_xor_b32 m0, s61, 22528\n" \
    "s_xor_b32 s97, s60, 1408\n" \
    "buffer_load_dwordx4 %[vox], %[dx], s97 offen lds\n" \
    "s_xor_b32 m0, s61, 24576\n" \
    "s_xor_b32 s97, s60, 1536\n" \
    "buffer_load_dwordx4 %[vox], %[dx], s97 offen lds\n" \
    "s_xor_b32 m0, s61, 26624\n" \
    "s_xor_b32 s97, s60, 1664\n" \
    "buffer_load_dwordx4 %[vox], %[dx], s97 offen lds\n" \
    "s_xor_b32 m0, s61, 28672\n" \
    "s_xor_b32 s97, s60, 1792\n" \
    "buffer_load_dwordx4 %[vox], %[dx], s97 offen lds\n" \
    "s_xor_b32 m0, s61, 30720\n" \
    "s_xor_b32 s97, s60, 1920\n" \
    "buffer_load_dwordx4 %[vox], %[dx], s97 offen lds\n" \
    "s_add_u32 s60, s60, 65536\n" \
    "s_waitcnt vmcnt(0)\n" \
    "s_barrier\n" \
    "s_mul_i32 s62, 63, 0x202000\n" \
    "s_and_b32 s62, s62, 0xffe02000\n" \
    "s_xor_b32 s62, s62, %[pol]\n" \
    "ds_read_b128 v[88:91], v32 offset:0\n" \
    "ds_read_b128 v[92:95], v32 offset:32768\n" \
    "ds_read_b128 v[96:99], v33 offset:0\n" \
    "ds_read_b128 v[100:103], v33 offset:32768\n" \
    "ds_read_b128 v[104:107], v32 offset:2048\n" \
    "LS1_seg_%=:\n" \
    "s_waitcnt lgkmcnt(4)\n" \
    "v_mfma_f32_16x16x32_bf16 v[40:43], a[0:3], v[88:91], 0\n" \
    "ds_read_b128 v[108:111], v32 offset:34816\n" \
    "v_mfma_f32_16x16x32_bf16 v[48:51], a[4:7], v[88:91], 0\n" \
    "s_waitcnt lgkmcnt(4)\n" \
    "v_mfma_f32_16x16x32_bf16 v[44:47], a[0:3], v[92:95], 0\n" \
    "ds_read_b128 v[112:115], v33 offset:2048\n" \
    "v_mfma_f32_16x16x32_bf16 v[52:55], a[4:7], v[92:95], 0\n" \
    "s_barrier\n" \
    "s_waitcnt lgkmcnt(4)\n" \
    "v_mfma_f32_16x16x32_bf16 v[40:43], a[8:11], v[96:99], v[40:43]\n" \
    "ds_read_b128 v[116:119], v33 offset:34816\n" \
    "s_add_u32 s63, s62, 131072\n" \
    "v_mov_b64 v[208:209], v[200:201]\n" \
    "v_mfma_f32_16x16x32_bf16 v[48:51], a[12:15], v[96:99], v[48:51]\n" \
    "v_mov_b64 v[210:211], v[202:203]\n" \
    "v_mov_b64 v[212:213], v[204:205]\n" \
    "s_waitcnt lgkmcnt(4)\n" \
    "v_mfma_f32_16x16x32_bf16 v[44:47], a[8:11], v[100:103], v[44:47]\n" \
    "ds_read_b128 v[88:91], v32 offset:4096\n" \
    "v_mov_b64 v[214:215], v[206:207]\n" \
    "v_mul_f32 v192, v128, v56\n" \
    "v_mul_f32 v193, v129, v57\n" \
    "v_mfma_f32_16x16x32_bf16 v[52:55], a[12:15], v[100:103], v[52:55]\n" \
    "v_mul_f32 v194, v130, v58\n" \
    "s_xor_b32 m0, s61, 65536\n" \
    "s_xor_b32 s97, s60, 0\n" \
    "buffer_load_dwordx4 %[vox], %[dx], s97 offen lds\n" \
    "s_waitcnt lgkmcnt(4)\n" \
    "v_mfma_f32_16x16x32_bf16 v[40:43], a[16:19], v[104:107], v[40:43]\n" \
    "ds_read_b128 v[92:95], v32 offset:36864\n" \
    "v_mul_f32 v195, v131, v59\n" \
    "v_mul_f32 v196, v128, v64\n" \
    "v_mfma_f32_16x16x32_bf16 v[48:51], a[20:23], v[104:107], v[48:51]\n" \
    "v_mul_f32 v197, v129, v65\n" \
    "v_mul_f32 v198, v130, v66\n" \
    "s_waitcnt lgkmcnt(4)\n" \
    "v_mfma_f32_16x16x32_bf16 v[44:47], a[16:19], v[108:111], v[44:47]\n" \
    "ds_read_b128 v[96:99], v33 offset:4096\n" \
    "v_mul_f32 v199, v131, v67\n" \
    "v_fma_f32 v192, -v132, v64, v192\n" \
    "v_fma_f32 v193, -v133, v65, v193\n" \
    "v_mfma_f32_16x16x32_bf16 v[52:55], a[20:23], v[108:111], v[52:55]\n" \
    "v_fma_f32 v194, -v134, v66, v194\n" \
    "s_xor_b32 m0, s61, 67584\n" \
    "s_xor_b32 s97, s60, 128\n" \
    "buffer_load_dwordx4 %[vox], %[dx], s97 offen lds\n" \
    "s_waitcnt lgkmcnt(4)\n" \
    "v_mfma_f32_16x16x32_bf16 v[40:43], a[24:27], v[112:115], v[40:43]\n" \
    "ds_read_b128 v[100:103], v33 offset:36864\n" \
    "v_fma_f32 v195, -v135, v67, v195\n" \
    "v_fmac_f32 v196, v132, v56\n" \
    "v_mfma_f32_16x16x32_bf16 v[48:51], a[28:31], v[112:115], v[48:51]\n" \
    "v_fmac_f32 v197, v133, v57\n" \
    "v_fmac_f32 v198, v134, v58\n" \
    "s_waitcnt lgkmcnt(4)\n" \
    "v_mfma_f32_16x16x32_bf16 v[44:47], a[24:27], v[116:119], v[44:47]\n" \
    "ds_read_b128 v[104:107], v32 offset:6144\n" \
    "v_fmac_f32 v199, v135, v59\n" \
    "v_mul_f32 v200, v128, v60\n" \
    "v_mul_f32 v201, v129, v61\n" \
    "v_mfma_f32_16x16x32_bf16 v[52:55], a[28:31], v[116:119], v[52:55]\n" \
    "v_mul_f32 v202, v130, v62\n" \
    "s_xor_b32 m0, s61, 69632\n" \
    "s_xor_b32 s97, s60, 256\n" \
    "buffer_load_dwordx4 %[vox], %[dx], s97 offen lds\n" \
    "s_waitcnt lgkmcnt(4)\n" \
    "v_mfma_f32_16x16x32_bf16 v[40:43], a[32:35], v[88:91], v[40:43]\n" \
    "ds_read_b128 v[108:111], v32 offset:38912\n" \
    "v_mul_f32 v203, v131, v63\n" \
    "v_mul_f32 v204, v128, v68\n" \
    "v_mfma_f32_16x16x32_bf16 v[48:51], a[36:39], v[88:91], v[48:51]\n" \
    "v_mul_f32 v205, v129, v69\n" \
    "v_mul_f32 v206, v130, v70\n" \
    "s_waitcnt lgkmcnt(4)\n" \
    "v_mfma_f32_16x16x32_bf16 v[44:47], a[32:35], v[92:95], v[44:47]\n" \
    "ds_read_b128 v[112:115], v33 offset:6144\n" \
    "v_mul_f32 v207, v131, v71\n" \
    "v_fma_f32 v200, -v132, v68, v200\n" \
    "v_fma_f32 v201, -v133, v69, v201\n" \
    "v_mfma_f32_16x16x32_bf16 v[52:55], a[36:39], v[92:95], v[52:55]\n" \
    "v_fma_f32 v202, -v134, v70, v202\n" \
    "s_xor_b32 m0, s61, 71680\n" \
    "s_xor_b32 s97, s60, 384\n" \
    "buffer_load_dwordx4 %[vox], %[dx], s97 offen lds\n" \
    "s_waitcnt lgkmcnt(4)\n" \
    "v_mfma_f32_16x16x32_bf16 v[40:43], a[40:43], v[96:99], v[40:43]\n" \
    "ds_read_b128 v[116:119], v33 offset:38912\n" \
    "v_fma_f32 v203, -v135, v71, v203\n" \
    "v_fmac_f32 v204, v132, v60\n" \
    "v_mfma_f32_16x16x32_bf16 v[48:51], a[44:47], v[96:99], v[48:51]\n" \
    "v_fmac_f32 v205, v133, v61\n" \
    "v_fmac_f32 v206, v134, v62\n" \
    "s_waitcnt lgkmcnt(4)\n" \
    "v_mfma_f32_16x16x32_bf16 v[44:47], a[40:43], v[100:103], v[44:47]\n" \
    "ds_read_b128 v[88:91], v32 offset:8192\n" \
    "v_fmac_f32 v207, v135, v63\n" \
    "v_fmac_f32_dpp v192, v208, v136 row_ror:1 row_mask:0xf bank_mask:0xf bound_ctrl:1\n" \
    "v_fmac_f32_dpp v193, v209, v137 row_ror:1 row_mask:0xf bank_mask:0xf bound_ctrl:1\n" \
    "v_mfma_f32_16x16x32_bf16 v[52:55], a[44:47], v[100:103], v[52:55]\n" \
    "v_fmac_f32_dpp v194, v210, v138 row_ror:1 row_mask:0xf bank_mask:0xf bound_ctrl:1\n" \
    "s_xor_b32 m0, s61, 73728\n" \
    "s_xor_b32 s97, s60, 512\n" \
    "buffer_load_dwordx4 %[vox], %[dx], s97 offen lds\n" \
    "s_waitcnt lgkmcnt(4)\n" \
    "v_mfma_f32_16x16x32_bf16 v[40:43], a[48:51], v[104:107], v[40:43]\n" \
    "ds_read_b128 v[92:95], v32 offset:40960\n" \
    "v_fmac_f32_dpp v195, v211, v139 row_ror:1 row_mask:0xf bank_mask:0xf bound_ctrl:1\n" \
    "v_fmac_f32_dpp v192, -v212, v140 row_ror:1 row_mask:0xf bank_mask:0xf bound_ctrl:1\n" \
    "v_mfma_f32_16x16x32_bf16 v[48:51], a[52:55], v[104:107], v[48:51]\n" \
    "v_fmac_f32_dpp v193, -v213, v141 row_ror:1 row_mask:0xf bank_mask:0xf bound_ctrl:1\n" \
    "v_fmac_f32_dpp v194, -v214, v142 row_ror:1 row_mask:0xf bank_mask:0xf bound_ctrl:1\n" \
    "s_waitcnt lgkmcnt(4)\n" \
    "v_mfma_f32_16x16x32_bf16 v[44:47], a[48:51], v[108:111], v[44:47]\n" \
    "ds_read_b128 v[96:99], v33 offset:8192\n" \
    "v_fmac_f32_dpp v195, -v215, v143 row_ror:1 row_mask:0xf bank_mask:0xf bound_ctrl:1\n" \
    "v_fmac_f32_dpp v196, v212, v136 row_ror:1 row_mask:0xf bank_mask:0xf bound_ctrl:1\n" \
    "v_fmac_f32_dpp v197, v213, v137 row_ror:1 row_mask:0xf bank_mask:0xf bound_ctrl:1\n" \
    "v_mfma_f32_16x16x32_bf16 v[52:55], a[52:55], v[108:111], v[52:55]\n" \
    "v_fmac_f32_dpp v198, v214, v138 row_ror:1 row_mask:0xf bank_mask:0xf bound_ctrl:1\n" \
    "s_xor_b32 m0, s61, 75776\n" \
    "s_xor_b32 s97, s60, 640\n" \
    "buffer_load_dwordx4 %[vox], %[dx], s97 offen lds\n" \
    "s_waitcnt lgkmcnt(4)\n" \
    "v_mfma_f32_16x16x32_bf16 v[40:43], a[56:59], v[112:115], v[40:43]\n" \
    "ds_read_b128 v[100:103], v33 offset:40960\n" \
    "v_fmac_f32_dpp v199, v215, v139 row_ror:1 row_mask:0xf bank_mask:0xf bound_ctrl:1\n" \
    "v_fmac_f32_dpp v196, v208, v140 row_ror:1 row_mask:0xf bank_mask:0xf bound_ctrl:1\n" \
    "v_mfma_f32_16x16x32_bf16 v[48:51], a[60:63], v[112:115], v[48:51]\n" \
    "v_fmac_f32_dpp v197, v209, v141 row_ror:1 row_mask:0xf bank_mask:0xf bound_ctrl:1\n" \
    "v_fmac_f32_dpp v198, v210, v142 row_ror:1 row_mask:0xf bank_mask:0xf bound_ctrl:1\n" \
    "s_waitcnt lgkmcnt(4)\n" \
    "v_mfma_f32_16x16x32_bf16 v[44:47], a[56:59], v[116:119], v[44:47]\n" \
    "ds_read_b128 v[104:107], v32 offset:10240\n" \
    "v_fmac_f32_dpp v199, v211, v143 row_ror:1 row_mask:0xf bank_mask:0xf bound_ctrl:1\n" \
    "v_fmac_f32 v200, v192, v144\n" \
    "v_fmac_f32 v201, v193, v145\n" \
    "v_mfma_f32_16x16x32_bf16 v[52:55], a[60:63], v[116:119], v[52:55]\n" \
    "v_fmac_f32 v202, v194, v146\n" \
    "s_xor_b32 m0, s61, 77824\n" \
    "s_xor_b32 s97, s60, 768\n" \
    "buffer_load_dwordx4 %[vox], %[dx], s97 offen lds\n" \
    "s_waitcnt lgkmcnt(4)\n" \
    "v_mfma_f32_16x16x32_bf16 v[40:43], a[64:67], v[88:91], v[40:43]\n" \
    "ds_read_b128 v[108:111], v32 offset:43008\n" \
    "v_fmac_f32 v203, v195, v147\n" \
    "v_fma_f32 v200, -v196, v148, v200\n" \
    "v_mfma_f32_16x16x32_bf16 v[48:51], a[68:71], v[88:91], v[48:51]\n" \
    "v_fma_f32 v201, -v197, v149, v201\n" \
    "v_fma_f32 v202, -v198, v150, v202\n" \
    "s_waitcnt lgkmcnt(4)\n" \
    "v_mfma_f32_16x16x32_bf16 v[44:47], a[64:67], v[92:95], v[44:47]\n" \
    "ds_read_b128 v[112:115], v33 offset:10240\n" \
    "v_fma_f32 v203, -v199, v151, v203\n" \
    "v_fmac_f32 v204, v196, v144\n" \
    "v_fmac_f32 v205, v197, v145\n" \
    "v_mfma_f32_16x16x32_bf16 v[52:55], a[68:71], v[92:95], v[52:55]\n" \
    "v_fmac_f32 v206, v198, v146\n" \
    "s_xor_b32 m0, s61, 79872\n" \
    "s_xor_b32 s97, s60, 896\n" \
    "buffer_load_dwordx4 %[vox], %[dx], s97 offen lds\n" \
    "s_waitcnt lgkmcnt(4)\n" \
    "v_mfma_f32_16x16x32_bf16 v[40:43], a[72:75], v[96:99], v[40:43]\n" \
    "ds_read_b128 v[116:119], v33 offset:43008\n" \
    "v_fmac_f32 v207, v199, v147\n" \
    "v_fmac_f32 v204, v192, v148\n" \
    "v_mfma_f32_16x16x32_bf16 v[48:51], a[76:79], v[96:99], v[48:51]\n" \
    "v_fmac_f32 v205, v193, v149\n" \
    "v_fmac_f32 v206, v194, v150\n" \
    "s_waitcnt lgkmcnt(4)\n" \
    "v_mfma_f32_16x16x32_bf16 v[44:47], a[72:75], v[100:103], v[44:47]\n" \
    "ds_read_b128 v[88:91], v32 offset:12288\n" \
    "v_fmac_f32 v207, v195, v151\n" \
    "v_mov_b64 v[216:217], v[200:201]\n" \
    "v_mov_b64 v[218:219], v[202:203]\n" \
    "v_mfma_f32_16x16x32_bf16 v[52:55], a[76:79], v[100:103], v[52:55]\n" \
    "v_fmac_f32_dpp v200, v200, v152 row_shr:1 row_mask:0xf bank_mask:0xf bound_ctrl:1\n" \
    "s_xor_b32 m0, s61, 81920\n" \
    "s_xor_b32 s97, s60, 1024\n" \
    "buffer_load_dwordx4 %[vox], %[dx], s97 offen lds\n" \
    "s_waitcnt lgkmcnt(4)\n" \
    "v_mfma_f32_16x16x32_bf16 v[40:43], a[80:83], v[104:107], v[40:43]\n" \
    "ds_read_b128 v[92:95], v32 offset:45056\n" \
    "v_fmac_f32_dpp v201, v201, v153 row_shr:1 row_mask:0xf bank_mask:0xf bound_ctrl:1\n" \
    "v_fmac_f32_dpp v202, v202, v154 row_shr:1 row_mask:0xf bank_mask:0xf bound_ctrl:1\n" \
    "v_mfma_f32_16x16x32_bf16 v[48:51], a[84:87], v[104:107], v[48:51]\n" \
    "v_fmac_f32_dpp v203, v203, v155 row_shr:1 row_mask:0xf bank_mask:0xf bound_ctrl:1\n" \
    "v_fmac_f32_dpp v200, -v204, v156 row_shr:1 row_mask:0xf bank_mask:0xf bound_ctrl:1\n" \
    "s_waitcnt lgkmcnt(4)\n" \
    "v_mfma_f32_16x16x32_bf16 v[44:47], a[80:83], v[108:111], v[44:47]\n" \
    "ds_read_b128 v[96:99], v33 offset:12288\n" \
    "v_fmac_f32_dpp v201, -v205, v157 row_shr:1 row_mask:0xf bank_mask:0xf bound_ctrl:1\n" \
    "v_fmac_f32_dpp v202, -v206, v158 row_shr:1 row_mask:0xf bank_mask:0xf bound_ctrl:1\n" \
    "v_fmac_f32_dpp v203, -v207, v159 row_shr:1 row_mask:0xf bank_mask:0xf bound_ctrl:1\n" \
    "v_mfma_f32_16x16x32_bf16 v[52:55], a[84:87], v[108:111], v[52:55]\n" \
    "v_fmac_f32_dpp v204, v204, v152 row_shr:1 row_mask:0xf bank_mask:0xf bound_ctrl:1\n" \
    "s_xor_b32 m0, s61, 83968\n" \
    "s_xor_b32 s97, s60, 1152\n" \
    "buffer_load_dwordx4 %[vox], %[dx], s97 offen lds\n" \
    "s_waitcnt lgkmcnt(4)\n" \
    "v_mfma_f32_16x16x32_bf16 v[40:43], a[88:91], v[112:115], v[40:43]\n" \
    "ds_read_b128 v[100:103], v33 offset:45056\n" \
    "v_fmac_f32_dpp v205, v205, v153 row_shr:1 row_mask:0xf bank_mask:0xf bound_ctrl:1\n" \
    "v_fmac_f32_dpp v206, v206, v154 row_shr:1 row_mask:0xf bank_mask:0xf bound_ctrl:1\n" \
    "v_mfma_f32_16x16x32_bf16 v[48:51], a[92:95], v[112:115], v[48:51]\n" \
    "v_fmac_f32_dpp v207, v207, v155 row_shr:1 row_mask:0xf bank_mask:0xf bound_ctrl:1\n" \
    "v_fmac_f32_dpp v204, v216, v156 row_shr:1 row_mask:0xf bank_mask:0xf bound_ctrl:1\n" \
    "s_waitcnt lgkmcnt(4)\n" \
    "v_mfma_f32_16x16x32_bf16 v[44:47], a[88:91], v[116:119], v[44:47]\n" \
    "ds_read_b128 v[104:107], v32 offset:14336\n" \
    "v_fmac_f32_dpp v205, v217, v157 row_shr:1 row_mask:0xf bank_mask:0xf bound_ctrl:1\n" \
    "v_fmac_f32_dpp v206, v218, v158 row_shr:1 row_mask:0xf bank_mask:0xf bound_ctrl:1\n" \
    "v_fmac_f32_dpp v207, v219, v159 row_shr:1 row_mask:0xf bank_mask:0xf bound_ctrl:1\n" \
    "v_mfma_f32_16x16x32_bf16 v[52:55], a[92:95], v[116:119], v[52:55]\n" \
    "v_mov_b64 v[216:217], v[200:201]\n" \
    "s_xor_b32 m0, s61, 86016\n" \
    "s_xor_b32 s97, s60, 1280\n" \
    "buffer_load_dwordx4 %[vox], %[dx], s97 offen lds\n" \
    "s_waitcnt lgkmcnt(4)\n" \
    "v_mfma_f32_16x16x32_bf16 v[40:43], a[96:99], v[88:91], v[40:43]\n" \
    "ds_read_b128 v[108:111], v32 offset:47104\n" \
    "v_mov_b64 v[218:219], v[202:203]\n" \
    "v_fmac_f32_dpp v200, v200, v160 row_shr:2 row_mask:0xf bank_mask:0xf bound_ctrl:1\n" \
    "v_mfma_f32_16x16x32_bf16 v[48:51], a[100:103], v[88:91], v[48:51]\n" \
    "v_fmac_f32_dpp v201, v201, v161 row_shr:2 row_mask:0xf bank_mask:0xf bound_ctrl:1\n" \
    "v_fmac_f32_dpp v202, v202, v162 row_shr:2 row_mask:0xf bank_mask:0xf bound_ctrl:1\n" \
    "s_waitcnt lgkmcnt(4)\n" \
    "v_mfma_f32_16x16x32_bf16 v[44:47], a[96:99], v[92:95], v[44:47]\n" \
    "ds_read_b128 v[112:115], v33 offset:14336\n" \
    "v_fmac_f32_dpp v203, v203, v163 row_shr:2 row_mask:0xf bank_mask:0xf bound_ctrl:1\n" \
    "v_fmac_f32_dpp v200, -v204, v164 row_shr:2 row_mask:0xf bank_mask:0xf bound_ctrl:1\n" \
    "v_fmac_f32_dpp v201, -v205, v165 row_shr:2 row_mask:0xf bank_mask:0xf bound_ctrl:1\n" \
    "v_mfma_f32_16x16x32_bf16 v[52:55], a[100:103], v[92:95], v[52:55]\n" \
    "v_fmac_f32_dpp v202, -v206, v166 row_shr:2 row_mask:0xf bank_mask:0xf bound_ctrl:1\n" \
    "s_xor_b32 m0, s61, 88064\n" \
    "s_xor_b32 s97, s60, 1408\n" \
    "buffer_load_dwordx4 %[vox], %[dx], s97 offen lds\n" \
    "s_waitcnt lgkmcnt(4)\n" \
    "v_mfma_f32_16x16x32_bf16 v[40:43], a[104:107], v[96:99], v[40:43]\n" \
    "ds_read_b128 v[116:119], v33 offset:47104\n" \
    "v_fmac_f32_dpp v203, -v207, v167 row_shr:2 row_mask:0xf bank_mask:0xf bound_ctrl:1\n" \
    "v_fmac_f32_dpp v204, v204, v160 row_shr:2 row_mask:0xf bank_mask:0xf bound_ctrl:1\n" \
    "v_mfma_f32_16x16x32_bf16 v[48:51], a[108:111], v[96:99], v[48:51]\n" \
    "v_fmac_f32_dpp v205, v205, v161 row_shr:2 row_mask:0xf bank_mask:0xf bound_ctrl:1\n" \
    "v_fmac_f32_dpp v206, v206, v162 row_shr:2 row_mask:0xf bank_mask:0xf bound_ctrl:1\n" \
    "s_waitcnt lgkmcnt(4)\n" \
    "v_mfma_f32_16x16x32_bf16 v[44:47], a[104:107], v[100:103], v[44:47]\n" \
    "ds_read_b128 v[88:91], v32 offset:16384\n" \
    "v_fmac_f32_dpp v207, v207, v163 row_shr:2 row_mask:0xf bank_mask:0xf bound_ctrl:1\n" \
    "v_fmac_f32_dpp v204, v216, v164 row_shr:2 row_mask:0xf bank_mask:0xf bound_ctrl:1\n" \
    "v_fmac_f32_dpp v205, v217, v165 row_shr:2 row_mask:0xf bank_mask:0xf bound_ctrl:1\n" \
    "v_mfma_f32_16x16x32_bf16 v[52:55], a[108:111], v[100:103], v[52:55]\n" \
    "v_fmac_f32_dpp v206, v218, v166 row_shr:2 row_mask:0xf bank_mask:0xf bound_ctrl:1\n" \
    "s_xor_b32 m0, s61, 90112\n" \
    "s_xor_b32 s97, s60, 1536\n" \
    "buffer_load_dwordx4 %[vox], %[dx], s97 offen lds\n" \
    "s_waitcnt lgkmcnt(4)\n" \
    "v_mfma_f32_16x16x32_bf16 v[40:43], a[112:115], v[104:107], v[40:43]\n" \
    "ds_read_b128 v[92:95], v32 offset:49152\n" \
    "v_fmac_f32_dpp v207, v219, v167 row_shr:2 row_mask:0xf bank_mask:0xf bound_ctrl:1\n" \
    "v_mov_b64 v[216:217], v[200:201]\n" \
    "v_mfma_f32_16x16x32_bf16 v[48:51], a[116:119], v[104:107], v[48:51]\n" \
    "v_mov_b64 v[218:219], v[202:203]\n" \
    "v_fmac_f32_dpp v200, v200, v168 row_shr:4 row_mask:0xf bank_mask:0xf bound_ctrl:1\n" \
    "s_waitcnt lgkmcnt(4)\n" \
    "v_mfma_f32_16x16x32_bf16 v[44:47], a[112:115], v[108:111], v[44:47]\n" \
    "ds_read_b128 v[96:99], v33 offset:16384\n" \
    "v_fmac_f32_dpp v201, v201, v169 row_shr:4 row_mask:0xf bank_mask:0xf bound_ctrl:1\n" \
    "v_fmac_f32_dpp v202, v202, v170 row_shr:4 row_mask:0xf bank_mask:0xf bound_ctrl:1\n" \
    "v_fmac_f32_dpp v203, v203, v171 row_shr:4 row_mask:0xf bank_mask:0xf bound_ctrl:1\n" \
    "v_mfma_f32_16x16x32_bf16 v[52:55], a[116:119], v[108:111], v[52:55]\n" \
    "v_fmac_f32_dpp v200, -v204, v172 row_shr:4 row_mask:0xf bank_mask:0xf bound_ctrl:1\n" \
    "s_xor_b32 m0, s61, 92160\n" \
    "s_xor_b32 s97, s60, 1664\n" \
    "buffer_load_dwordx4 %[vox], %[dx], s97 offen lds\n" \
    "s_waitcnt lgkmcnt(4)\n" \
    "v_mfma_f32_16x16x32_bf16 v[40:43], a[120:123], v[112:115], v[40:43]\n" \
    "ds_read_b128 v[100:103], v33 offset:49152\n" \
    "v_fmac_f32_dpp v201, -v205, v173 row_shr:4 row_mask:0xf bank_mask:0xf bound_ctrl:1\n" \
    "v_fmac_f32_dpp v202, -v206, v174 row_shr:4 row_mask:0xf bank_mask:0xf bound_ctrl:1\n" \
    "v_mfma_f32_16x16x32_bf16 v[48:51], a[124:127], v[112:115], v[48:51]\n" \
    "v_fmac_f32_dpp v203, -v207, v175 row_shr:4 row_mask:0xf bank_mask:0xf bound_ctrl:1\n" \
    "v_fmac_f32_dpp v204, v204, v168 row_shr:4 row_mask:0xf bank_mask:0xf bound_ctrl:1\n" \
    "s_waitcnt lgkmcnt(4)\n" \
    "v_mfma_f32_16x16x32_bf16 v[44:47], a[120:123], v[116:119], v[44:47]\n" \
    "ds_read_b128 v[104:107], v32 offset:18432\n" \
    "v_fmac_f32_dpp v205, v205, v169 row_shr:4 row_mask:0xf bank_mask:0xf bound_ctrl:1\n" \
    "v_fmac_f32_dpp v206, v206, v170 row_shr:4 row_mask:0xf bank_mask:0xf bound_ctrl:1\n" \
    "v_fmac_f32_dpp v207, v207, v171 row_shr:4 row_mask:0xf bank_mask:0xf bound_ctrl:1\n" \
    "v_mfma_f32_16x16x32_bf16 v[52:55], a[124:127], v[116:119], v[52:55]\n" \
    "v_fmac_f32_dpp v204, v216, v172 row_shr:4 row_mask:0xf bank_mask:0xf bound_ctrl:1\n" \
    "s_xor_b32 m0, s61, 94208\n" \
    "s_xor_b32 s97, s60, 1792\n" \
    "buffer_load_dwordx4 %[vox], %[dx], s97 offen lds\n" \
    "s_waitcnt lgkmcnt(4)\n" \
    "v_mfma_f32_16x16x32_bf16 v[40:43], a[128:131], v[88:91], v[40:43]\n" \
    "ds_read_b128 v[108:111], v32 offset:51200\n" \
    "v_fmac_f32_dpp v205, v217, v173 row_shr:4 row_mask:0xf bank_mask:0xf bound_ctrl:1\n" \
    "v_fmac_f32_dpp v206, v218, v174 row_shr:4 row_mask:0xf bank_mask:0xf bound_ctrl:1\n" \
    "v_mfma_f32_16x16x32_bf16 v[48:51], a[132:135], v[88:91], v[48:51]\n" \
    "v_fmac_f32_dpp v207, v219, v175 row_shr:4 row_mask:0xf bank_mask:0xf bound_ctrl:1\n" \
    "v_mov_b64 v[216:217], v[200:201]\n" \
    "s_waitcnt lgkmcnt(4)\n" \
    "v_mfma_f32_16x16x32_bf16 v[44:47], a[128:131], v[92:95], v[44:47]\n" \
    "ds_read_b128 v[112:115], v33 offset:18432\n" \
    "v_mov_b64 v[218:219], v[202:203]\n" \
    "v_fmac_f32_dpp v200, v200, v176 row_shr:8 row_mask:0xf bank_mask:0xf bound_ctrl:1\n" \
    "v_fmac_f32_dpp v201, v201, v177 row_shr:8 row_mask:0xf bank_mask:0xf bound_ctrl:1\n" \
    "v_mfma_f32_16x16x32_bf16 v[52:55], a[132:135], v[92:95], v[52:55]\n" \
    "v_fmac_f32_dpp v202, v202, v178 row_shr:8 row_mask:0xf bank_mask:0xf bound_ctrl:1\n" \
    "s_xor_b32 m0, s61, 96256\n" \
    "s_xor_b32 s97, s60, 1920\n" \
    "buffer_load_dwordx4 %[vox], %[dx], s97 offen lds\n" \
    "s_waitcnt lgkmcnt(4)\n" \
    "v_mfma_f32_16x16x32_bf16 v[40:43], a[136:139], v[96:99], v[40:43]\n" \
    "ds_read_b128 v[116:119], v33 offset:51200\n" \
    "v_fmac_f32_dpp v203, v203, v179 row_shr:8 row_mask:0xf bank_mask:0xf bound_ctrl:1\n" \
    "v_fmac_f32_dpp v200, -v204, v180 row_shr:8 row_mask:0xf bank_mask:0xf bound_ctrl:1\n" \
    "v_mfma_f32_16x16x32_bf16 v[48:51], a[140:143], v[96:99], v[48:51]\n" \
    "v_fmac_f32_dpp v201, -v205, v181 row_shr:8 row_mask:0xf bank_mask:0xf bound_ctrl:1\n" \
    "v_fmac_f32_dpp v202, -v206, v182 row_shr:8 row_mask:0xf bank_mask:0xf bound_ctrl:1\n" \
    "s_waitcnt lgkmcnt(4)\n" \
    "v_mfma_f32_16x16x32_bf16 v[44:47], a[136:139], v[100:103], v[44:47]\n" \
    "ds_read_b128 v[88:91], v32 offset:20480\n" \
    "v_fmac_f32_dpp v203, -v207, v183 row_shr:8 row_mask:0xf bank_mask:0xf bound_ctrl:1\n" \
    "v_fmac_f32_dpp v204, v204, v176 row_shr:8 row_mask:0xf bank_mask:0xf bound_ctrl:1\n" \
    "v_mfma_f32_16x16x32_bf16 v[52:55], a[140:143], v[100:103], v[52:55]\n" \
    "v_fmac_f32_dpp v205, v205, v177 row_shr:8 row_mask:0xf bank_mask:0xf bound_ctrl:1\n" \
    "v_fmac_f32_dpp v206, v206, v178 row_shr:8 row_mask:0xf bank_mask:0xf bound_ctrl:1\n" \
    "s_waitcnt lgkmcnt(4)\n" \
    "v_mfma_f32_16x16x32_bf16 v[40:43], a[144:147], v[104:107], v[40:43]\n" \
    "ds_read_b128 v[92:95], v32 offset:53248\n" \
    "v_fmac_f32_dpp v207, v207, v179 row_shr:8 row_mask:0xf bank_mask:0xf bound_ctrl:1\n" \
    "v_fmac_f32_dpp v204, v216, v180 row_shr:8 row_mask:0xf bank_mask:0xf bound_ctrl:1\n" \
    "v_mfma_f32_16x16x32_bf16 v[48:51], a[148:151], v[104:107], v[48:51]\n" \
    "v_fmac_f32_dpp v205, v217, v181 row_shr:8 row_mask:0xf bank_mask:0xf bound_ctrl:1\n" \
    "v_fmac_f32_dpp v206, v218, v182 row_shr:8 row_mask:0xf bank_mask:0xf bound_ctrl:1\n" \
    "s_waitcnt lgkmcnt(4)\n" \
    "v_mfma_f32_16x16x32_bf16 v[44:47], a[144:147], v[108:111], v[44:47]\n" \
    "ds_read_b128 v[96:99], v33 offset:20480\n" \
    "v_fmac_f32_dpp v207, v219, v183 row_shr:8 row_mask:0xf bank_mask:0xf bound_ctrl:1\n" \
    "v_fmac_f32_dpp v192, v200, v144 row_shr:1 row_mask:0xf bank_mask:0xf bound_ctrl:1\n" \
    "v_mfma_f32_16x16x32_bf16 v[52:55], a[148:151], v[108:111], v[52:55]\n" \
    "v_fmac_f32_dpp v193, v201, v145 row_shr:1 row_mask:0xf bank_mask:0xf bound_ctrl:1\n" \
    "v_fmac_f32_dpp v194, v202, v146 row_shr:1 row_mask:0xf bank_mask:0xf bound_ctrl:1\n" \
    "s_waitcnt lgkmcnt(4)\n" \
    "v_mfma_f32_16x16x32_bf16 v[40:43], a[152:155], v[112:115], v[40:43]\n" \
    "ds_read_b128 v[100:103], v33 offset:53248\n" \
    "v_fmac_f32_dpp v195, v203, v147 row_shr:1 row_mask:0xf bank_mask:0xf bound_ctrl:1\n" \
    "v_fmac_f32_dpp v192, -v204, v148 row_shr:1 row_mask:0xf bank_mask:0xf bound_ctrl:1\n" \
    "v_mfma_f32_16x16x32_bf16 v[48:51], a[156:159], v[112:115], v[48:51]\n" \
    "v_fmac_f32_dpp v193, -v205, v149 row_shr:1 row_mask:0xf bank_mask:0xf bound_ctrl:1\n" \
    "v_fmac_f32_dpp v194, -v206, v150 row_shr:1 row_mask:0xf bank_mask:0xf bound_ctrl:1\n" \
    "s_waitcnt lgkmcnt(4)\n" \
    "v_mfma_f32_16x16x32_bf16 v[44:47], a[152:155], v[116:119], v[44:47]\n" \
    "ds_read_b128 v[104:107], v32 offset:22528\n" \
    "v_fmac_f32_dpp v195, -v207, v151 row_shr:1 row_mask:0xf bank_mask:0xf bound_ctrl:1\n" \
    "v_fmac_f32_dpp v196, v204, v144 row_shr:1 row_mask:0xf bank_mask:0xf bound_ctrl:1\n" \
    "v_mfma_f32_16x16x32_bf16 v[52:55], a[156:159], v[116:119], v[52:55]\n" \
    "v_fmac_f32_dpp v197, v205, v145 row_shr:1 row_mask:0xf bank_mask:0xf bound_ctrl:1\n" \
    "v_fmac_f32_dpp v198, v206, v146 row_shr:1 row_mask:0xf bank_mask:0xf bound_ctrl:1\n" \
    "s_waitcnt lgkmcnt(4)\n" \
    "v_mfma_f32_16x16x32_bf16 v[40:43], a[160:163], v[88:91], v[40:43]\n" \
    "ds_read_b128 v[108:111], v32 offset:55296\n" \
    "v_fmac_f32_dpp v199, v207, v147 row_shr:1 row_mask:0xf bank_mask:0xf bound_ctrl:1\n" \
    "v_fmac_f32_dpp v196, v200, v148 row_shr:1 row_mask:0xf bank_mask:0xf bound_ctrl:1\n" \
    "v_mfma_f32_16x16x32_bf16 v[48:51], a[164:167], v[88:91], v[48:51]\n" \
    "v_fmac_f32_dpp v197, v201, v149 row_shr:1 row_mask:0xf bank_mask:0xf bound_ctrl:1\n" \
    "v_fmac_f32_dpp v198, v202, v150 row_shr:1 row_mask:0xf bank_mask:0xf bound_ctrl:1\n" \
    "s_waitcnt lgkmcnt(4)\n" \
    "v_mfma_f32_16x16x32_bf16 v[44:47], a[160:163], v[92:95], v[44:47]\n" \
    "ds_read_b128 v[112:115], v33 offset:22528\n" \
    "v_fmac_f32_dpp v199, v203, v151 row_shr:1 row_mask:0xf bank_mask:0xf bound_ctrl:1\n" \
    "v_cvt_pk_bf16_f32 v120, v192, v193\n" \
    "v_mfma_f32_16x16x32_bf16 v[52:55], a[164:167], v[92:95], v[52:55]\n" \
    "v_cvt_pk_bf16_f32 v121, v194, v195\n" \
    "v_cvt_pk_bf16_f32 v122, -v196, -v197\n" \
    "s_waitcnt lgkmcnt(4)\n" \
    "v_mfma_f32_16x16x32_bf16 v[40:43], a[168:171], v[96:99], v[40:43]\n" \
    "ds_read_b128 v[116:119], v33 offset:55296\n" \
    "v_cvt_pk_bf16_f32 v123, -v198, -v199\n" \
    "buffer_store_dwordx4 v[120:123], %[voa], %[ws], s62 offen sc1\n" \
    "v_mfma_f32_16x16x32_bf16 v[48:51], a[172:175], v[96:99], v[48:51]\n" \
    "v_cvt_pk_bf16_f32 v124, v200, v201\n" \
    "v_cvt_pk_bf16_f32 v125, v202, v203\n" \
    "s_waitcnt lgkmcnt(4)\n" \
    "v_mfma_f32_16x16x32_bf16 v[44:47], a[168:171], v[100:103], v[44:47]\n" \
    "ds_read_b128 v[88:91], v32 offset:24576\n" \
    "v_cvt_pk_bf16_f32 v126, -v204, -v205\n" \
    "v_cvt_pk_bf16_f32 v127, -v206, -v207\n" \
    "v_mfma_f32_16x16x32_bf16 v[52:55], a[172:175], v[100:103], v[52:55]\n" \
    "buffer_store_dwordx4 v[124:127], %[voa], %[ws], s63 offen sc1\n" \
    "s_mul_i32 s62, s95, 0x202000\n" \
    "s_waitcnt lgkmcnt(4)\n" \
    "v_mfma_f32_16x16x32_bf16 v[40:43], a[176:179], v[104:107], v[40:43]\n" \
    "ds_read_b128 v[92:95], v32 offset:57344\n" \
    "s_and_b32 s62, s62, 0xffe02000\n" \
    "v_mfma_f32_16x16x32_bf16 v[48:51], a[180:183], v[104:107], v[48:51]\n" \
    "s_xor_b32 s62, s62, %[pol]\n" \
    "s_waitcnt lgkmcnt(4)\n" \
    "v_mfma_f32_16x16x32_bf16 v[44:47], a[176:179], v[108:111], v[44:47]\n" \
    "ds_read_b128 v[96:99], v33 offset:24576\n" \
    "v_mfma_f32_16x16x32_bf16 v[52:55], a[180:183], v[108:111], v[52:55]\n" \
    "s_waitcnt lgkmcnt(4)\n" \
    "v_mfma_f32_16x16x32_bf16 v[40:43], a[184:187], v[112:115], v[40:43]\n" \
    "ds_read_b128 v[100:103], v33 offset:57344\n" \
    "v_mfma_f32_16x16x32_bf16 v[48:51], a[188:191], v[112:115], v[48:51]\n" \
    "s_waitcnt lgkmcnt(4)\n" \
    "v_mfma_f32_16x16x32_bf16 v[44:47], a[184:187], v[116:119], v[44:47]\n" \
    "ds_read_b128 v[104:107], v32 offset:26624\n" \
    "v_mfma_f32_16x16x32_bf16 v[52:55], a[188:191], v[116:119], v[52:55]\n" \
    "s_waitcnt lgkmcnt(4)\n" \
    "v_mfma_f32_16x16x32_bf16 v[40:43], a[192:195], v[88:91], v[40:43]\n" \
    "ds_read_b128 v[108:111], v32 offset:59392\n" \
    "v_mfma_f32_16x16x32_bf16 v[48:51], a[196:199], v[88:91], v[48:51]\n" \
    "s_waitcnt lgkmcnt(4)\n" \
    "v_mfma_f32_16x16x32_bf16 v[44:47], a[192:195], v[92:95], v[44:47]\n" \
    "ds_read_b128 v[112:115], v33 offset:26624\n" \
    "v_mfma_f32_16x16x32_bf16 v[52:55], a[196:199], v[92:95], v[52:55]\n" \
    "s_waitcnt lgkmcnt(4)\n" \
    "v_mfma_f32_16x16x32_bf16 v[40:43], a[200:203], v[96:99], v[40:43]\n" \
    "ds_read_b128 v[116:119], v33 offset:59392\n" \
    "v_mfma_f32_16x16x32_bf16 v[48:51], a[204:207], v[96:99], v[48:51]\n" \
    "s_waitcnt lgkmcnt(4)\n" \
    "v_mfma_f32_16x16x32_bf16 v[44:47], a[200:203], v[100:103], v[44:47]\n" \
    "ds_read_b128 v[88:91], v32 offset:28672\n" \
    "v_mfma_f32_16x16x32_bf16 v[52:55], a[204:207], v[100:103], v[52:55]\n" \
    "s_waitcnt lgkmcnt(4)\n" \
    "v_mfma_f32_16x16x32_bf16 v[40:43], a[208:211], v[104:107], v[40:43]\n" \
    "ds_read_b128 v[92:95], v32 offset:61440\n" \
    "v_mfma_f32_16x16x32_bf16 v[48:51], a[212:215], v[104:107], v[48:51]\n" \
    "s_waitcnt lgkmcnt(4)\n" \
    "v_mfma_f32_16x16x32_bf16 v[44:47], a[208:211], v[108:111], v[44:47]\n" \
    "ds_read_b128 v[96:99], v33 offset:28672\n" \
    "v_mfma_f32_16x16x32_bf16 v[52:55], a[212:215], v[108:111], v[52:55]\n" \
    "s_waitcnt lgkmcnt(4)\n" \
    "v_mfma_f32_16x16x32_bf16 v[40:43], a[216:219], v[112:115], v[40:43]\n" \
    "ds_read_b128 v[100:103], v33 offset:61440\n" \
    "v_mfma_f32_16x16x32_bf16 v[48:51], a[220:223], v[112:115], v[48:51]\n" \
    "s_waitcnt lgkmcnt(4)\n" \
    "v_mfma_f32_16x16x32_bf16 v[44:47], a[216:219], v[116:119], v[44:47]\n" \
    "ds_read_b128 v[104:107], v32 offset:30720\n" \
    "v_mfma_f32_16x16x32_bf16 v[52:55], a[220:223], v[116:119], v[52:55]\n" \
    "s_waitcnt lgkmcnt(4)\n" \
    "v_mfma_f32_16x16x32_bf16 v[40:43], a[224:227], v[88:91], v[40:43]\n" \
    "ds_read_b128 v[108:111], v32 offset:63488\n" \
    "v_mfma_f32_16x16x32_bf16 v[48:51], a[228:231], v[88:91], v[48:51]\n" \
    "s_waitcnt lgkmcnt(4)\n" \
    "v_mfma_f32_16x16x32_bf16 v[44:47], a[224:227], v[92:95], v[44:47]\n" \
    "ds_read_b128 v[112:115], v33 offset:30720\n" \
    "v_mfma_f32_16x16x32_bf16 v[52:55], a[228:231], v[92:95], v[52:55]\n" \
    "s_waitcnt lgkmcnt(4)\n" \
    "v_mfma_f32_16x16x32_bf16 v[40:43], a[232:235], v[96:99], v[40:43]\n" \
    "ds_read_b128 v[116:119], v33 offset:63488\n" \
    "s_waitcnt vmcnt(0)\n" \
    "v_mfma_f32_16x16x32_bf16 v[48:51], a[236:239], v[96:99], v[48:51]\n" \
    "s_barrier\n" \
    "s_waitcnt lgkmcnt(4)\n" \
    "v_mfma_f32_16x16x32_bf16 v[44:47], a[232:235], v[100:103], v[44:47]\n" \
    "ds_read_b128 v[88:91], v34 offset:0\n" \
    "s_sub_u32 s96, s95, 1\n" \
    "v_mfma_f32_16x16x32_bf16 v[52:55], a[236:239], v[100:103], v[52:55]\n" \
    "v_mov_b32 v36, s96\n" \
    "buffer_store_dword v36, %[vfl], %[ws], %[pol] offen sc1\n" \
    "s_waitcnt lgkmcnt(4)\n" \
    "v_mfma_f32_16x16x32_bf16 v[40:43], a[240:243], v[104:107], v[40:43]\n" \
    "ds_read_b128 v[92:95], v34 offset:32768\n" \
    "s_add_u32 s95, s95, 1\n" \
    "v_mfma_f32_16x16x32_bf16 v[48:51], a[244:247], v[104:107], v[48:51]\n" \
    "s_add_u32 s60, s60, 65536\n" \
    "s_and_b32 s60, s60, 0x3fffff\n" \
    "s_waitcnt lgkmcnt(4)\n" \
    "v_mfma_f32_16x16x32_bf16 v[44:47], a[240:243], v[108:111], v[44:47]\n" \
    "ds_read_b128 v[96:99], v35 offset:0\n" \
    "s_add_u32 s97, s60, 65536\n" \
    "v_mfma_f32_16x16x32_bf16 v[52:55], a[244:247], v[108:111], v[52:55]\n" \
    "s_and_b32 s97, s97, 0x3f0000\n" \
    "buffer_load_dword v38, %[vxt], %[dx], s97 offen\n" \
    "s_waitcnt lgkmcnt(4)\n" \
    "v_mfma_f32_16x16x32_bf16 v[40:43], a[248:251], v[112:115], v[40:43]\n" \
    "ds_read_b128 v[100:103], v35 offset:32768\n" \
    "v_mfma_f32_16x16x32_bf16 v[48:51], a[252:255], v[112:115], v[48:51]\n" \
    "s_waitcnt lgkmcnt(4)\n" \
    "v_mfma_f32_16x16x32_bf16 v[44:47], a[248:251], v[116:119], v[44:47]\n" \
    "ds_read_b128 v[104:107], v34 offset:2048\n" \
    "v_mfma_f32_16x16x32_bf16 v[52:55], a[252:255], v[116:119], v[52:55]\n" \
    "s_waitcnt lgkmcnt(4)\n" \
    "v_mfma_f32_16x16x32_bf16 v[56:59], a[0:3], v[88:91], 0\n" \
    "ds_read_b128 v[108:111], v34 offset:34816\n" \
    "v_mfma_f32_16x16x32_bf16 v[64:67], a[4:7], v[88:91], 0\n" \
    "s_waitcnt lgkmcnt(4)\n" \
    "v_mfma_f32_16x16x32_bf16 v[60:63], a[0:3], v[92:95], 0\n" \
    "ds_read_b128 v[112:115], v35 offset:2048\n" \
    "v_mfma_f32_16x16x32_bf16 v[68:71], a[4:7], v[92:95], 0\n" \
    "s_barrier\n" \
    "s_waitcnt lgkmcnt(4)\n" \
    "v_mfma_f32_16x16x32_bf16 v[56:59], a[8:11], v[96:99], v[56:59]\n" \
    "ds_read_b128 v[116:119], v35 offset:34816\n" \
    "s_add_u32 s63, s62, 131072\n" \
    "v_mov_b64 v[208:209], v[200:201]\n" \
    "v_mfma_f32_16x16x32_bf16 v[64:67], a[12:15], v[96:99], v[64:67]\n" \
    "v_mov_b64 v[210:211], v[202:203]\n" \
    "v_mov_b64 v[212:213], v[204:205]\n" \
    "s_waitcnt lgkmcnt(4)\n" \
    "v_mfma_f32_16x16x32_bf16 v[60:63], a[8:11], v[100:103], v[60:63]\n" \
    "ds_read_b128 v[88:91], v34 offset:4096\n" \
    "v_mov_b64 v[214:215], v[206:207]\n" \
    "v_mul_f32 v192, v128, v40\n" \
    "v_mul_f32 v193, v129, v41\n" \
    "v_mfma_f32_16x16x32_bf16 v[68:71], a[12:15], v[100:103], v[68:71]\n" \
    "v_mul_f32 v194, v130, v42\n" \
    "s_xor_b32 m0, s61, 0\n" \
    "s_xor_b32 s97, s60, 0\n" \
    "buffer_load_dwordx4 %[vox], %[dx], s97 offen lds\n" \
    "s_waitcnt lgkmcnt(4)\n" \
    "v_mfma_f32_16x16x32_bf16 v[56:59], a[16:19], v[104:107], v[56:59]\n" \
    "ds_read_b128 v[92:95], v34 offset:36864\n" \
    "v_mul_f32 v195, v131, v43\n" \
    "v_mul_f32 v196, v128, v48\n" \
    "v_mfma_f32_16x16x32_bf16 v[64:67], a[20:23], v[104:107], v[64:67]\n" \
    "v_mul_f32 v197, v129, v49\n" \
    "v_mul_f32 v198, v130, v50\n" \
    "s_waitcnt lgkmcnt(4)\n" \
    "v_mfma_f32_16x16x32_bf16 v[60:63], a[16:19], v[108:111], v[60:63]\n" \
    "ds_read_b128 v[96:99], v35 offset:4096\n" \
    "v_mul_f32 v199, v131, v51\n" \
    "v_fma_f32 v192, -v132, v48, v192\n" \
    "v_fma_f32 v193, -v133, v49, v193\n" \
    "v_mfma_f32_16x16x32_bf16 v[68:71], a[20:23], v[108:111], v[68:71]\n" \
    "v_fma_f32 v194, -v134, v50, v194\n" \
    "s_xor_b32 m0, s61, 2048\n" \
    "s_xor_b32 s97, s60, 128\n" \
    "buffer_load_dwordx4 %[vox], %[dx], s97 offen lds\n" \
    "s_waitcnt lgkmcnt(4)\n" \
    "v_mfma_f32_16x16x32_bf16 v[56:59], a[24:27], v[112:115], v[56:59]\n" \
    "ds_read_b128 v[100:103], v35 offset:36864\n" \
    "v_fma_f32 v195, -v135, v51, v195\n" \
    "v_fmac_f32 v196, v132, v40\n" \
    "v_mfma_f32_16x16x32_bf16 v[64:67], a[28:31], v[112:115], v[64:67]\n" \
    "v_fmac_f32 v197, v133, v41\n" \
    "v_fmac_f32 v198, v134, v42\n" \
    "s_waitcnt lgkmcnt(4)\n" \
    "v_mfma_f32_16x16x32_bf16 v[60:63], a[24:27], v[116:119], v[60:63]\n" \
    "ds_read_b128 v[104:107], v34 offset:6144\n" \
    "v_fmac_f32 v199, v135, v43\n" \
    "v_mul_f32 v200, v128, v44\n" \
    "v_mul_f32 v201, v129, v45\n" \
    "v_mfma_f32_16x16x32_bf16 v[68:71], a[28:31], v[116:119], v[68:71]\n" \
    "v_mul_f32 v202, v130, v46\n" \
    "s_xor_b32 m0, s61, 4096\n" \
    "s_xor_b32 s97, s60, 256\n" \
    "buffer_load_dwordx4 %[vox], %[dx], s97 offen lds\n" \
    "s_waitcnt lgkmcnt(4)\n" \
    "v_mfma_f32_16x16x32_bf16 v[56:59], a[32:35], v[88:91], v[56:59]\n" \
    "ds_read_b128 v[108:111], v34 offset:38912\n" \
    "v_mul_f32 v203, v131, v47\n" \
    "v_mul_f32 v204, v128, v52\n" \
    "v_mfma_f32_16x16x32_bf16 v[64:67], a[36:39], v[88:91], v[64:67]\n" \
    "v_mul_f32 v205, v129, v53\n" \
    "v_mul_f32 v206, v130, v54\n" \
    "s_waitcnt lgkmcnt(4)\n" \
    "v_mfma_f32_16x16x32_bf16 v[60:63], a[32:35], v[92:95], v[60:63]\n" \
    "ds_read_b128 v[112:115], v35 offset:6144\n" \
    "v_mul_f32 v207, v131, v55\n" \
    "v_fma_f32 v200, -v132, v52, v200\n" \
    "v_fma_f32 v201, -v133, v53, v201\n" \
    "v_mfma_f32_16x16x32_bf16 v[68:71], a[36:39], v[92:95], v[68:71]\n" \
    "v_fma_f32 v202, -v134, v54, v202\n" \
    "s_xor_b32 m0, s61, 6144\n" \
    "s_xor_b32 s97, s60, 384\n" \
    "buffer_load_dwordx4 %[vox], %[dx], s97 offen lds\n" \
    "s_waitcnt lgkmcnt(4)\n" \
    "v_mfma_f32_16x16x32_bf16 v[56:59], a[40:43], v[96:99], v[56:59]\n" \
    "ds_read_b128 v[116:119], v35 offset:38912\n" \
    "v_fma_f32 v203, -v135, v55, v203\n" \
    "v_fmac_f32 v204, v132, v44\n" \
    "v_mfma_f32_16x16x32_bf16 v[64:67], a[44:47], v[96:99], v[64:67]\n" \
    "v_fmac_f32 v205, v133, v45\n" \
    "v_fmac_f32 v206, v134, v46\n" \
    "s_waitcnt lgkmcnt(4)\n" \
    "v_mfma_f32_16x16x32_bf16 v[60:63], a[40:43], v[100:103], v[60:63]\n" \
    "ds_read_b128 v[88:91], v34 offset:8192\n" \
    "v_fmac_f32 v207, v135, v47\n" \
    "v_fmac_f32_dpp v192, v208, v136 row_ror:1 row_mask:0xf bank_mask:0xf bound_ctrl:1\n" \
    "v_fmac_f32_dpp v193, v209, v137 row_ror:1 row_mask:0xf bank_mask:0xf bound_ctrl:1\n" \
    "v_mfma_f32_16x16x32_bf16 v[68:71], a[44:47], v[100:103], v[68:71]\n" \
    "v_fmac_f32_dpp v194, v210, v138 row_ror:1 row_mask:0xf bank_mask:0xf bound_ctrl:1\n" \
    "s_xor_b32 m0, s61, 8192\n" \
    "s_xor_b32 s97, s60, 512\n" \
    "buffer_load_dwordx4 %[vox], %[dx], s97 offen lds\n" \
    "s_waitcnt lgkmcnt(4)\n" \
    "v_mfma_f32_16x16x32_bf16 v[56:59], a[48:51], v[104:107], v[56:59]\n" \
    "ds_read_b128 v[92:95], v34 offset:40960\n" \
    "v_fmac_f32_dpp v195, v211, v139 row_ror:1 row_mask:0xf bank_mask:0xf bound_ctrl:1\n" \
    "v_fmac_f32_dpp v192, -v212, v140 row_ror:1 row_mask:0xf bank_mask:0xf bound_ctrl:1\n" \
    "v_mfma_f32_16x16x32_bf16 v[64:67], a[52:55], v[104:107], v[64:67]\n" \
    "v_fmac_f32_dpp v193, -v213, v141 row_ror:1 row_mask:0xf bank_mask:0xf bound_ctrl:1\n" \
    "v_fmac_f32_dpp v194, -v214, v142 row_ror:1 row_mask:0xf bank_mask:0xf bound_ctrl:1\n" \
    "s_waitcnt lgkmcnt(4)\n" \
    "v_mfma_f32_16x16x32_bf16 v[60:63], a[48:51], v[108:111], v[60:63]\n" \
    "ds_read_b128 v[96:99], v35 offset:8192\n" \
    "v_fmac_f32_dpp v195, -v215, v143 row_ror:1 row_mask:0xf bank_mask:0xf bound_ctrl:1\n" \
    "v_fmac_f32_dpp v196, v212, v136 row_ror:1 row_mask:0xf bank_mask:0xf bound_ctrl:1\n" \
    "v_fmac_f32_dpp v197, v213, v137 row_ror:1 row_mask:0xf bank_mask:0xf bound_ctrl:1\n" \
    "v_mfma_f32_16x16x32_bf16 v[68:71], a[52:55], v[108:111], v[68:71]\n" \
    "v_fmac_f32_dpp v198, v214, v138 row_ror:1 row_mask:0xf bank_mask:0xf bound_ctrl:1\n" \
    "s_xor_b32 m0, s61, 10240\n" \
    "s_xor_b32 s97, s60, 640\n" \
    "buffer_load_dwordx4 %[vox], %[dx], s97 offen lds\n" \
    "s_waitcnt lgkmcnt(4)\n" \
    "v_mfma_f32_16x16x32_bf16 v[56:59], a[56:59], v[112:115], v[56:59]\n" \
    "ds_read_b128 v[100:103], v35 offset:40960\n" \
    "v_fmac_f32_dpp v199, v215, v139 row_ror:1 row_mask:0xf bank_mask:0xf bound_ctrl:1\n" \
    "v_fmac_f32_dpp v196, v208, v140 row_ror:1 row_mask:0xf bank_mask:0xf bound_ctrl:1\n" \
    "v_mfma_f32_16x16x32_bf16 v[64:67], a[60:63], v[112:115], v[64:67]\n" \
    "v_fmac_f32_dpp v197, v209, v141 row_ror:1 row_mask:0xf bank_mask:0xf bound_ctrl:1\n" \
    "v_fmac_f32_dpp v198, v210, v142 row_ror:1 row_mask:0xf bank_mask:0xf bound_ctrl:1\n" \
    "s_waitcnt lgkmcnt(4)\n" \
    "v_mfma_f32_16x16x32_bf16 v[60:63], a[56:59], v[116:119], v[60:63]\n" \
    "ds_read_b128 v[104:107], v34 offset:10240\n" \
    "v_fmac_f32_dpp v199, v211, v143 row_ror:1 row_mask:0xf bank_mask:0xf bound_ctrl:1\n" \
    "v_fmac_f32 v200, v192, v144\n" \
    "v_fmac_f32 v201, v193, v145\n" \
    "v_mfma_f32_16x16x32_bf16 v[68:71], a[60:63], v[116:119], v[68:71]\n" \
    "v_fmac_f32 v202, v194, v146\n" \
    "s_xor_b32 m0, s61, 12288\n" \
    "s_xor_b32 s97, s60, 768\n" \
    "buffer_load_dwordx4 %[vox], %[dx], s97 offen lds\n" \
    "s_waitcnt lgkmcnt(4)\n" \
    "v_mfma_f32_16x16x32_bf16 v[56:59], a[64:67], v[88:91], v[56:59]\n" \
    "ds_read_b128 v[108:111], v34 offset:43008\n" \
    "v_fmac_f32 v203, v195, v147\n" \
    "v_fma_f32 v200, -v196, v148, v200\n" \
    "v_mfma_f32_16x16x32_bf16 v[64:67], a[68:71], v[88:91], v[64:67]\n" \
    "v_fma_f32 v201, -v197, v149, v201\n" \
    "v_fma_f32 v202, -v198, v150, v202\n" \
    "s_waitcnt lgkmcnt(4)\n" \
    "v_mfma_f32_16x16x32_bf16 v[60:63], a[64:67], v[92:95], v[60:63]\n" \
    "ds_read_b128 v[112:115], v35 offset:10240\n" \
    "v_fma_f32 v203, -v199, v151, v203\n" \
    "v_fmac_f32 v204, v196, v144\n" \
    "v_fmac_f32 v205, v197, v145\n" \
    "v_mfma_f32_16x16x32_bf16 v[68:71], a[68:71], v[92:95], v[68:71]\n" \
    "v_fmac_f32 v206, v198, v146\n" \
    "s_xor_b32 m0, s61, 14336\n" \
    "s_xor_b32 s97, s60, 896\n" \
    "buffer_load_dwordx4 %[vox], %[dx], s97 offen lds\n" \
    "s_waitcnt lgkmcnt(4)\n" \
    "v_mfma_f32_16x16x32_bf16 v[56:59], a[72:75], v[96:99], v[56:59]\n" \
    "ds_read_b128 v[116:119], v35 offset:43008\n" \
    "v_fmac_f32 v207, v199, v147\n" \
    "v_fmac_f32 v204, v192, v148\n" \
    "v_mfma_f32_16x16x32_bf16 v[64:67], a[76:79], v[96:99], v[64:67]\n" \
    "v_fmac_f32 v205, v193, v149\n" \
    "v_fmac_f32 v206, v194, v150\n" \
    "s_waitcnt lgkmcnt(4)\n" \
    "v_mfma_f32_16x16x32_bf16 v[60:63], a[72:75], v[100:103], v[60:63]\n" \
    "ds_read_b128 v[88:91], v34 offset:12288\n" \
    "v_fmac_f32 v207, v195, v151\n" \
    "v_mov_b64 v[216:217], v[200:201]\n" \
    "v_mov_b64 v[218:219], v[202:203]\n" \
    "v_mfma_f32_16x16x32_bf16 v[68:71], a[76:79], v[100:103], v[68:71]\n" \
    "v_fmac_f32_dpp v200, v200, v152 row_shr:1 row_mask:0xf bank_mask:0xf bound_ctrl:1\n" \
    "s_xor_b32 m0, s61, 16384\n" \
    "s_xor_b32 s97, s60, 1024\n" \
    "buffer_load_dwordx4 %[vox], %[dx], s97 offen lds\n" \
    "s_waitcnt lgkmcnt(4)\n" \
    "v_mfma_f32_16x16x32_bf16 v[56:59], a[80:83], v[104:107], v[56:59]\n" \
    "ds_read_b128 v[92:95], v34 offset:45056\n" \
    "v_fmac_f32_dpp v201, v201, v153 row_shr:1 row_mask:0xf bank_mask:0xf bound_ctrl:1\n" \
    "v_fmac_f32_dpp v202, v202, v154 row_shr:1 row_mask:0xf bank_mask:0xf bound_ctrl:1\n" \
    "v_mfma_f32_16x16x32_bf16 v[64:67], a[84:87], v[104:107], v[64:67]\n" \
    "v_fmac_f32_dpp v203, v203, v155 row_shr:1 row_mask:0xf bank_mask:0xf bound_ctrl:1\n" \
    "v_fmac_f32_dpp v200, -v204, v156 row_shr:1 row_mask:0xf bank_mask:0xf bound_ctrl:1\n" \
    "s_waitcnt lgkmcnt(4)\n" \
    "v_mfma_f32_16x16x32_bf16 v[60:63], a[80:83], v[108:111], v[60:63]\n" \
    "ds_read_b128 v[96:99], v35 offset:12288\n" \
    "v_fmac_f32_dpp v201, -v205, v157 row_shr:1 row_mask:0xf bank_mask:0xf bound_ctrl:1\n" \
    "v_fmac_f32_dpp v202, -v206, v158 row_shr:1 row_mask:0xf bank_mask:0xf bound_ctrl:1\n" \
    "v_fmac_f32_dpp v203, -v207, v159 row_shr:1 row_mask:0xf bank_mask:0xf bound_ctrl:1\n" \
    "v_mfma_f32_16x16x32_bf16 v[68:71], a[84:87], v[108:111], v[68:71]\n" \
    "v_fmac_f32_dpp v204, v204, v152 row_shr:1 row_mask:0xf bank_mask:0xf bound_ctrl:1\n" \
    "s_xor_b32 m0, s61, 18432\n" \
    "s_xor_b32 s97, s60, 1152\n" \
    "buffer_load_dwordx4 %[vox], %[dx], s97 offen lds\n" \
    "s_waitcnt lgkmcnt(4)\n" \
    "v_mfma_f32_16x16x32_bf16 v[56:59], a[88:91], v[112:115], v[56:59]\n" \
    "ds_read_b128 v[100:103], v35 offset:45056\n" \
    "v_fmac_f32_dpp v205, v205, v153 row_shr:1 row_mask:0xf bank_mask:0xf bound_ctrl:1\n" \
    "v_fmac_f32_dpp v206, v206, v154 row_shr:1 row_mask:0xf bank_mask:0xf bound_ctrl:1\n" \
    "v_mfma_f32_16x16x32_bf16 v[64:67], a[92:95], v[112:115], v[64:67]\n" \
    "v_fmac_f32_dpp v207, v207, v155 row_shr:1 row_mask:0xf bank_mask:0xf bound_ctrl:1\n" \
    "v_fmac_f32_dpp v204, v216, v156 row_shr:1 row_mask:0xf bank_mask:0xf bound_ctrl:1\n" \
    "s_waitcnt lgkmcnt(4)\n" \
    "v_mfma_f32_16x16x32_bf16 v[60:63], a[88:91], v[116:119], v[60:63]\n" \
    "ds_read_b128 v[104:107], v34 offset:14336\n" \
    "v_fmac_f32_dpp v205, v217, v157 row_shr:1 row_mask:0xf bank_mask:0xf bound_ctrl:1\n" \
    "v_fmac_f32_dpp v206, v218, v158 row_shr:1 row_mask:0xf bank_mask:0xf bound_ctrl:1\n" \
    "v_fmac_f32_dpp v207, v219, v159 row_shr:1 row_mask:0xf bank_mask:0xf bound_ctrl:1\n" \
    "v_mfma_f32_16x16x32_bf16 v[68:71], a[92:95], v[116:119], v[68:71]\n" \
    "v_mov_b64 v[216:217], v[200:201]\n" \
    "s_xor_b32 m0, s61, 20480\n" \
    "s_xor_b32 s97, s60, 1280\n" \
    "buffer_load_dwordx4 %[vox], %[dx], s97 offen lds\n" \
    "s_waitcnt lgkmcnt(4)\n" \
    "v_mfma_f32_16x16x32_bf16 v[56:59], a[96:99], v[88:91], v[56:59]\n" \
    "ds_read_b128 v[108:111], v34 offset:47104\n" \
    "v_mov_b64 v[218:219], v[202:203]\n" \
    "v_fmac_f32_dpp v200, v200, v160 row_shr:2 row_mask:0xf bank_mask:0xf bound_ctrl:1\n" \
    "v_mfma_f32_16x16x32_bf16 v[64:67], a[100:103], v[88:91], v[64:67]\n" \
    "v_fmac_f32_dpp v201, v201, v161 row_shr:2 row_mask:0xf bank_mask:0xf bound_ctrl:1\n" \
    "v_fmac_f32_dpp v202, v202, v162 row_shr:2 row_mask:0xf bank_mask:0xf bound_ctrl:1\n" \
    "s_waitcnt lgkmcnt(4)\n" \
    "v_mfma_f32_16x16x32_bf16 v[60:63], a[96:99], v[92:95], v[60:63]\n" \
    "ds_read_b128 v[112:115], v35 offset:14336\n" \
    "v_fmac_f32_dpp v203, v203, v163 row_shr:2 row_mask:0xf bank_mask:0xf bound_ctrl:1\n" \
    "v_fmac_f32_dpp v200, -v204, v164 row_shr:2 row_mask:0xf bank_mask:0xf bound_ctrl:1\n" \
    "v_fmac_f32_dpp v201, -v205, v165 row_shr:2 row_mask:0xf bank_mask:0xf bound_ctrl:1\n" \
    "v_mfma_f32_16x16x32_bf16 v[68:71], a[100:103], v[92:95], v[68:71]\n" \
    "v_fmac_f32_dpp v202, -v206, v166 row_shr:2 row_mask:0xf bank_mask:0xf bound_ctrl:1\n" \
    "s_xor_b32 m0, s61, 22528\n" \
    "s_xor_b32 s97, s60, 1408\n" \
    "buffer_load_dwordx4 %[vox], %[dx], s97 offen lds\n" \
    "s_waitcnt lgkmcnt(4)\n" \
    "v_mfma_f32_16x16x32_bf16 v[56:59], a[104:107], v[96:99], v[56:59]\n" \
    "ds_read_b128 v[116:119], v35 offset:47104\n" \
    "v_fmac_f32_dpp v203, -v207, v167 row_shr:2 row_mask:0xf bank_mask:0xf bound_ctrl:1\n" \
    "v_fmac_f32_dpp v204, v204, v160 row_shr:2 row_mask:0xf bank_mask:0xf bound_ctrl:1\n" \
    "v_mfma_f32_16x16x32_bf16 v[64:67], a[108:111], v[96:99], v[64:67]\n" \
    "v_fmac_f32_dpp v205, v205, v161 row_shr:2 row_mask:0xf bank_mask:0xf bound_ctrl:1\n" \
    "v_fmac_f32_dpp v206, v206, v162 row_shr:2 row_mask:0xf bank_mask:0xf bound_ctrl:1\n" \
    "s_waitcnt lgkmcnt(4)\n" \
    "v_mfma_f32_16x16x32_bf16 v[60:63], a[104:107], v[100:103], v[60:63]\n" \
    "ds_read_b128 v[88:91], v34 offset:16384\n" \
    "v_fmac_f32_dpp v207, v207, v163 row_shr:2 row_mask:0xf bank_mask:0xf bound_ctrl:1\n" \
    "v_fmac_f32_dpp v204, v216, v164 row_shr:2 row_mask:0xf bank_mask:0xf bound_ctrl:1\n" \
    "v_fmac_f32_dpp v205, v217, v165 row_shr:2 row_mask:0xf bank_mask:0xf bound_ctrl:1\n" \
    "v_mfma_f32_16x16x32_bf16 v[68:71], a[108:111], v[100:103], v[68:71]\n" \
    "v_fmac_f32_dpp v206, v218, v166 row_shr:2 row_mask:0xf bank_mask:0xf bound_ctrl:1\n" \
    "s_xor_b32 m0, s61, 24576\n" \
    "s_xor_b32 s97, s60, 1536\n" \
    "buffer_load_dwordx4 %[vox], %[dx], s97 offen lds\n" \
    "s_waitcnt lgkmcnt(4)\n" \
    "v_mfma_f32_16x16x32_bf16 v[56:59], a[112:115], v[104:107], v[56:59]\n" \
    "ds_read_b128 v[92:95], v34 offset:49152\n" \
    "v_fmac_f32_dpp v207, v219, v167 row_shr:2 row_mask:0xf bank_mask:0xf bound_ctrl:1\n" \
    "v_mov_b64 v[216:217], v[200:201]\n" \
    "v_mfma_f32_16x16x32_bf16 v[64:67], a[116:119], v[104:107], v[64:67]\n" \
    "v_mov_b64 v[218:219], v[202:203]\n" \
    "v_fmac_f32_dpp v200, v200, v168 row_shr:4 row_mask:0xf bank_mask:0xf bound_ctrl:1\n" \
    "s_waitcnt lgkmcnt(4)\n" \
    "v_mfma_f32_16x16x32_bf16 v[60:63], a[112:115], v[108:111], v[60:63]\n" \
    "ds_read_b128 v[96:99], v35 offset:16384\n" \
    "v_fmac_f32_dpp v201, v201, v169 row_shr:4 row_mask:0xf bank_mask:0xf bound_ctrl:1\n" \
    "v_fmac_f32_dpp v202, v202, v170 row_shr:4 row_mask:0xf bank_mask:0xf bound_ctrl:1\n" \
    "v_fmac_f32_dpp v203, v203, v171 row_shr:4 row_mask:0xf bank_mask:0xf bound_ctrl:1\n" \
    "v_mfma_f32_16x16x32_bf16 v[68:71], a[116:119], v[108:111], v[68:71]\n" \
    "v_fmac_f32_dpp v200, -v204, v172 row_shr:4 row_mask:0xf bank_mask:0xf bound_ctrl:1\n" \
    "s_xor_b32 m0, s61, 26624\n" \
    "s_xor_b32 s97, s60, 1664\n" \
    "buffer_load_dwordx4 %[vox], %[dx], s97 offen lds\n" \
    "s_waitcnt lgkmcnt(4)\n" \
    "v_mfma_f32_16x16x32_bf16 v[56:59], a[120:123], v[112:115], v[56:59]\n" \
    "ds_read_b128 v[100:103], v35 offset:49152\n" \
    "v_fmac_f32_dpp v201, -v205, v173 row_shr:4 row_mask:0xf bank_mask:0xf bound_ctrl:1\n" \
    "v_fmac_f32_dpp v202, -v206, v174 row_shr:4 row_mask:0xf bank_mask:0xf bound_ctrl:1\n" \
    "v_mfma_f32_16x16x32_bf16 v[64:67], a[124:127], v[112:115], v[64:67]\n" \
    "v_fmac_f32_dpp v203, -v207, v175 row_shr:4 row_mask:0xf bank_mask:0xf bound_ctrl:1\n" \
    "v_fmac_f32_dpp v204, v204, v168 row_shr:4 row_mask:0xf bank_mask:0xf bound_ctrl:1\n" \
    "s_waitcnt lgkmcnt(4)\n" \
    "v_mfma_f32_16x16x32_bf16 v[60:63], a[120:123], v[116:119], v[60:63]\n" \
    "ds_read_b128 v[104:107], v34 offset:18432\n" \
    "v_fmac_f32_dpp v205, v205, v169 row_shr:4 row_mask:0xf bank_mask:0xf bound_ctrl:1\n" \
    "v_fmac_f32_dpp v206, v206, v170 row_shr:4 row_mask:0xf bank_mask:0xf bound_ctrl:1\n" \
    "v_fmac_f32_dpp v207, v207, v171 row_shr:4 row_mask:0xf bank_mask:0xf bound_ctrl:1\n" \
    "v_mfma_f32_16x16x32_bf16 v[68:71], a[124:127], v[116:119], v[68:71]\n" \
    "v_fmac_f32_dpp v204, v216, v172 row_shr:4 row_mask:0xf bank_mask:0xf bound_ctrl:1\n" \
    "s_xor_b32 m0, s61, 28672\n" \
    "s_xor_b32 s97, s60, 1792\n" \
    "buffer_load_dwordx4 %[vox], %[dx], s97 offen lds\n" \
    "s_waitcnt lgkmcnt(4)\n" \
    "v_mfma_f32_16x16x32_bf16 v[56:59], a[128:131], v[88:91], v[56:59]\n" \
    "ds_read_b128 v[108:111], v34 offset:51200\n" \
    "v_fmac_f32_dpp v205, v217, v173 row_shr:4 row_mask:0xf bank_mask:0xf bound_ctrl:1\n" \
    "v_fmac_f32_dpp v206, v218, v174 row_shr:4 row_mask:0xf bank_mask:0xf bound_ctrl:1\n" \
    "v_mfma_f32_16x16x32_bf16 v[64:67], a[132:135], v[88:91], v[64:67]\n" \
    "v_fmac_f32_dpp v207, v219, v175 row_shr:4 row_mask:0xf bank_mask:0xf bound_ctrl:1\n" \
    "v_mov_b64 v[216:217], v[200:201]\n" \
    "s_waitcnt lgkmcnt(4)\n" \
    "v_mfma_f32_16x16x32_bf16 v[60:63], a[128:131], v[92:95], v[60:63]\n" \
    "ds_read_b128 v[112:115], v35 offset:18432\n" \
    "v_mov_b64 v[218:219], v[202:203]\n" \
    "v_fmac_f32_dpp v200, v200, v176 row_shr:8 row_mask:0xf bank_mask:0xf bound_ctrl:1\n" \
    "v_fmac_f32_dpp v201, v201, v177 row_shr:8 row_mask:0xf bank_mask:0xf bound_ctrl:1\n" \
    "v_mfma_f32_16x16x32_bf16 v[68:71], a[132:135], v[92:95], v[68:71]\n" \
    "v_fmac_f32_dpp v202, v202, v178 row_shr:8 row_mask:0xf bank_mask:0xf bound_ctrl:1\n" \
    "s_xor_b32 m0, s61, 30720\n" \
    "s_xor_b32 s97, s60, 1920\n" \
    "buffer_load_dwordx4 %[vox], %[dx], s97 offen lds\n" \
    "s_waitcnt lgkmcnt(4)\n" \
    "v_mfma_f32_16x16x32_bf16 v[56:59], a[136:139], v[96:99], v[56:59]\n" \
    "ds_read_b128 v[116:119], v35 offset:51200\n" \
    "v_fmac_f32_dpp v203, v203, v179 row_shr:8 row_mask:0xf bank_mask:0xf bound_ctrl:1\n" \
    "v_fmac_f32_dpp v200, -v204, v180 row_shr:8 row_mask:0xf bank_mask:0xf bound_ctrl:1\n" \
    "v_mfma_f32_16x16x32_bf16 v[64:67], a[140:143], v[96:99], v[64:67]\n" \
    "v_fmac_f32_dpp v201, -v205, v181 row_shr:8 row_mask:0xf bank_mask:0xf bound_ctrl:1\n" \
    "v_fmac_f32_dpp v202, -v206, v182 row_shr:8 row_mask:0xf bank_mask:0xf bound_ctrl:1\n" \
    "s_waitcnt lgkmcnt(4)\n" \
    "v_mfma_f32_16x16x32_bf16 v[60:63], a[136:139], v[100:103], v[60:63]\n" \
    "ds_read_b128 v[88:91], v34 offset:20480\n" \
    "v_fmac_f32_dpp v203, -v207, v183 row_shr:8 row_mask:0xf bank_mask:0xf bound_ctrl:1\n" \
    "v_fmac_f32_dpp v204, v204, v176 row_shr:8 row_mask:0xf bank_mask:0xf bound_ctrl:1\n" \
    "v_mfma_f32_16x16x32_bf16 v[68:71], a[140:143], v[100:103], v[68:71]\n" \
    "v_fmac_f32_dpp v205, v205, v177 row_shr:8 row_mask:0xf bank_mask:0xf bound_ctrl:1\n" \
    "v_fmac_f32_dpp v206, v206, v178 row_shr:8 row_mask:0xf bank_mask:0xf bound_ctrl:1\n" \
    "s_waitcnt lgkmcnt(4)\n" \
    "v_mfma_f32_16x16x32_bf16 v[56:59], a[144:147], v[104:107], v[56:59]\n" \
    "ds_read_b128 v[92:95], v34 offset:53248\n" \
    "v_fmac_f32_dpp v207, v207, v179 row_shr:8 row_mask:0xf bank_mask:0xf bound_ctrl:1\n" \
    "v_fmac_f32_dpp v204, v216, v180 row_shr:8 row_mask:0xf bank_mask:0xf bound_ctrl:1\n" \
    "v_mfma_f32_16x16x32_bf16 v[64:67], a[148:151], v[104:107], v[64:67]\n" \
    "v_fmac_f32_dpp v205, v217, v181 row_shr:8 row_mask:0xf bank_mask:0xf bound_ctrl:1\n" \
    "v_fmac_f32_dpp v206, v218, v182 row_shr:8 row_mask:0xf bank_mask:0xf bound_ctrl:1\n" \
    "s_waitcnt lgkmcnt(4)\n" \
    "v_mfma_f32_16x16x32_bf16 v[60:63], a[144:147], v[108:111], v[60:63]\n" \
    "ds_read_b128 v[96:99], v35 offset:20480\n" \
    "v_fmac_f32_dpp v207, v219, v183 row_shr:8 row_mask:0xf bank_mask:0xf bound_ctrl:1\n" \
    "v_fmac_f32_dpp v192, v200, v144 row_shr:1 row_mask:0xf bank_mask:0xf bound_ctrl:1\n" \
    "v_mfma_f32_16x16x32_bf16 v[68:71], a[148:151], v[108:111], v[68:71]\n" \
    "v_fmac_f32_dpp v193, v201, v145 row_shr:1 row_mask:0xf bank_mask:0xf bound_ctrl:1\n" \
    "v_fmac_f32_dpp v194, v202, v146 row_shr:1 row_mask:0xf bank_mask:0xf bound_ctrl:1\n" \
    "s_waitcnt lgkmcnt(4)\n" \
    "v_mfma_f32_16x16x32_bf16 v[56:59], a[152:155], v[112:115], v[56:59]\n" \
    "ds_read_b128 v[100:103], v35 offset:53248\n" \
    "v_fmac_f32_dpp v195, v203, v147 row_shr:1 row_mask:0xf bank_mask:0xf bound_ctrl:1\n" \
    "v_fmac_f32_dpp v192, -v204, v148 row_shr:1 row_mask:0xf bank_mask:0xf bound_ctrl:1\n" \
    "v_mfma_f32_16x16x32_bf16 v[64:67], a[156:159], v[112:115], v[64:67]\n" \
    "v_fmac_f32_dpp v193, -v205, v149 row_shr:1 row_mask:0xf bank_mask:0xf bound_ctrl:1\n" \
    "v_fmac_f32_dpp v194, -v206, v150 row_shr:1 row_mask:0xf bank_mask:0xf bound_ctrl:1\n" \
    "s_waitcnt lgkmcnt(4)\n" \
    "v_mfma_f32_16x16x32_bf16 v[60:63], a[152:155], v[116:119], v[60:63]\n" \
    "ds_read_b128 v[104:107], v34 offset:22528\n" \
    "v_fmac_f32_dpp v195, -v207, v151 row_shr:1 row_mask:0xf bank_mask:0xf bound_ctrl:1\n" \
    "v_fmac_f32_dpp v196, v204, v144 row_shr:1 row_mask:0xf bank_mask:0xf bound_ctrl:1\n" \
    "v_mfma_f32_16x16x32_bf16 v[68:71], a[156:159], v[116:119], v[68:71]\n" \
    "v_fmac_f32_dpp v197, v205, v145 row_shr:1 row_mask:0xf bank_mask:0xf bound_ctrl:1\n" \
    "v_fmac_f32_dpp v198, v206, v146 row_shr:1 row_mask:0xf bank_mask:0xf bound_ctrl:1\n" \
    "s_waitcnt lgkmcnt(4)\n" \
    "v_mfma_f32_16x16x32_bf16 v[56:59], a[160:163], v[88:91], v[56:59]\n" \
    "ds_read_b128 v[108:111], v34 offset:55296\n" \
    "v_fmac_f32_dpp v199, v207, v147 row_shr:1 row_mask:0xf bank_mask:0xf bound_ctrl:1\n" \
    "v_fmac_f32_dpp v196, v200, v148 row_shr:1 row_mask:0xf bank_mask:0xf bound_ctrl:1\n" \
    "v_mfma_f32_16x16x32_bf16 v[64:67], a[164:167], v[88:91], v[64:67]\n" \
    "v_fmac_f32_dpp v197, v201, v149 row_shr:1 row_mask:0xf bank_mask:0xf bound_ctrl:1\n" \
    "v_fmac_f32_dpp v198, v202, v150 row_shr:1 row_mask:0xf bank_mask:0xf bound_ctrl:1\n" \
    "s_waitcnt lgkmcnt(4)\n" \
    "v_mfma_f32_16x16x32_bf16 v[60:63], a[160:163], v[92:95], v[60:63]\n" \
    "ds_read_b128 v[112:115], v35 offset:22528\n" \
    "v_fmac_f32_dpp v199, v203, v151 row_shr:1 row_mask:0xf bank_mask:0xf bound_ctrl:1\n" \
    "v_cvt_pk_bf16_f32 v120, v192, v193\n" \
    "v_mfma_f32_16x16x32_bf16 v[68:71], a[164:167], v[92:95], v[68:71]\n" \
    "v_cvt_pk_bf16_f32 v121, v194, v195\n" \
    "v_cvt_pk_bf16_f32 v122, -v196, -v197\n" \
    "s_waitcnt lgkmcnt(4)\n" \
    "v_mfma_f32_16x16x32_bf16 v[56:59], a[168:171], v[96:99], v[56:59]\n" \
    "ds_read_b128 v[116:119], v35 offset:55296\n" \
    "v_cvt_pk_bf16_f32 v123, -v198, -v199\n" \
    "buffer_store_dwordx4 v[120:123], %[voa], %[ws], s62 offen sc1\n" \
    "v_mfma_f32_16x16x32_bf16 v[64:67], a[172:175], v[96:99], v[64:67]\n" \
    "v_cvt_pk_bf16_f32 v124, v200, v201\n" \
    "v_cvt_pk_bf16_f32 v125, v202, v203\n" \
    "s_waitcnt lgkmcnt(4)\n" \
    "v_mfma_f32_16x16x32_bf16 v[60:63], a[168:171], v[100:103], v[60:63]\n" \
    "ds_read_b128 v[88:91], v34 offset:24576\n" \
    "v_cvt_pk_bf16_f32 v126, -v204, -v205\n" \
    "v_cvt_pk_bf16_f32 v127, -v206, -v207\n" \
    "v_mfma_f32_16x16x32_bf16 v[68:71], a[172:175], v[100:103], v[68:71]\n" \
    "buffer_store_dwordx4 v[124:127], %[voa], %[ws], s63 offen sc1\n" \
    "s_mul_i32 s62, s95, 0x202000\n" \
    "s_waitcnt lgkmcnt(4)\n" \
    "v_mfma_f32_16x16x32_bf16 v[56:59], a[176:179], v[104:107], v[56:59]\n" \
    "ds_read_b128 v[92:95], v34 offset:57344\n" \
    "s_and_b32 s62, s62, 0xffe02000\n" \
    "v_mfma_f32_16x16x32_bf16 v[64:67], a[180:183], v[104:107], v[64:67]\n" \
    "s_xor_b32 s62, s62, %[pol]\n" \
    "s_waitcnt lgkmcnt(4)\n" \
    "v_mfma_f32_16x16x32_bf16 v[60:63], a[176:179], v[108:111], v[60:63]\n" \
    "ds_read_b128 v[96:99], v35 offset:24576\n" \
    "v_mfma_f32_16x16x32_bf16 v[68:71], a[180:183], v[108:111], v[68:71]\n" \
    "s_waitcnt lgkmcnt(4)\n" \
    "v_mfma_f32_16x16x32_bf16 v[56:59], a[184:187], v[112:115], v[56:59]\n" \
    "ds_read_b128 v[100:103], v35 offset:57344\n" \
    "v_mfma_f32_16x16x32_bf16 v[64:67], a[188:191], v[112:115], v[64:67]\n" \
    "s_waitcnt lgkmcnt(4)\n" \
    "v_mfma_f32_16x16x32_bf16 v[60:63], a[184:187], v[116:119], v[60:63]\n" \
    "ds_read_b128 v[104:107], v34 offset:26624\n" \
    "v_mfma_f32_16x16x32_bf16 v[68:71], a[188:191], v[116:119], v[68:71]\n" \
    "s_waitcnt lgkmcnt(4)\n" \
    "v_mfma_f32_16x16x32_bf16 v[56:59], a[192:195], v[88:91], v[56:59]\n" \
    "ds_read_b128 v[108:111], v34 offset:59392\n" \
    "v_mfma_f32_16x16x32_bf16 v[64:67], a[196:199], v[88:91], v[64:67]\n" \
    "s_waitcnt lgkmcnt(4)\n" \
    "v_mfma_f32_16x16x32_bf16 v[60:63], a[192:195], v[92:95], v[60:63]\n" \
    "ds_read_b128 v[112:115], v35 offset:26624\n" \
    "v_mfma_f32_16x16x32_bf16 v[68:71], a[196:199], v[92:95], v[68:71]\n" \
    "s_waitcnt lgkmcnt(4)\n" \
    "v_mfma_f32_16x16x32_bf16 v[56:59], a[200:203], v[96:99], v[56:59]\n" \
    "ds_read_b128 v[116:119], v35 offset:59392\n" \
    "v_mfma_f32_16x16x32_bf16 v[64:67], a[204:207], v[96:99], v[64:67]\n" \
    "s_waitcnt lgkmcnt(4)\n" \
    "v_mfma_f32_16x16x32_bf16 v[60:63], a[200:203], v[100:103], v[60:63]\n" \
    "ds_read_b128 v[88:91], v34 offset:28672\n" \
    "v_mfma_f32_16x16x32_bf16 v[68:71], a[204:207], v[100:103], v[68:71]\n" \
    "s_waitcnt lgkmcnt(4)\n" \
    "v_mfma_f32_16x16x32_bf16 v[56:59], a[208:211], v[104:107], v[56:59]\n" \
    "ds_read_b128 v[92:95], v34 offset:61440\n" \
    "v_mfma_f32_16x16x32_bf16 v[64:67], a[212:215], v[104:107], v[64:67]\n" \
    "s_waitcnt lgkmcnt(4)\n" \
    "v_mfma_f32_16x16x32_bf16 v[60:63], a[208:211], v[108:111], v[60:63]\n" \
    "ds_read_b128 v[96:99], v35 offset:28672\n" \
    "v_mfma_f32_16x16x32_bf16 v[68:71], a[212:215], v[108:111], v[68:71]\n" \
    "s_waitcnt lgkmcnt(4)\n" \
    "v_mfma_f32_16x16x32_bf16 v[56:59], a[216:219], v[112:115], v[56:59]\n" \
    "ds_read_b128 v[100:103], v35 offset:61440\n" \
    "v_mfma_f32_16x16x32_bf16 v[64:67], a[220:223], v[112:115], v[64:67]\n" \
    "s_waitcnt lgkmcnt(4)\n" \
    "v_mfma_f32_16x16x32_bf16 v[60:63], a[216:219], v[116:119], v[60:63]\n" \
    "ds_read_b128 v[104:107], v34 offset:30720\n" \
    "v_mfma_f32_16x16x32_bf16 v[68:71], a[220:223], v[116:119], v[68:71]\n" \
    "s_waitcnt lgkmcnt(4)\n" \
    "v_mfma_f32_16x16x32_bf16 v[56:59], a[224:227], v[88:91], v[56:59]\n" \
    "ds_read_b128 v[108:111], v34 offset:63488\n" \
    "v_mfma_f32_16x16x32_bf16 v[64:67], a[228:231], v[88:91], v[64:67]\n" \
    "s_waitcnt lgkmcnt(4)\n" \
    "v_mfma_f32_16x16x32_bf16 v[60:63], a[224:227], v[92:95], v[60:63]\n" \
    "ds_read_b128 v[112:115], v35 offset:30720\n" \
    "v_mfma_f32_16x16x32_bf16 v[68:71], a[228:231], v[92:95], v[68:71]\n" \
    "s_waitcnt lgkmcnt(4)\n" \
    "v_mfma_f32_16x16x32_bf16 v[56:59], a[232:235], v[96:99], v[56:59]\n" \
    "ds_read_b128 v[116:119], v35 offset:63488\n" \
    "s_waitcnt vmcnt(0)\n" \
    "v_mfma_f32_16x16x32_bf16 v[64:67], a[236:239], v[96:99], v[64:67]\n" \
    "s_barrier\n" \
    "s_waitcnt lgkmcnt(4)\n" \
    "v_mfma_f32_16x16x32_bf16 v[60:63], a[232:235], v[100:103], v[60:63]\n" \
    "ds_read_b128 v[88:91], v32 offset:0\n" \
    "s_sub_u32 s96, s95, 1\n" \
    "v_mfma_f32_16x16x32_bf16 v[68:71], a[236:239], v[100:103], v[68:71]\n" \
    "v_mov_b32 v36, s96\n" \
    "buffer_store_dword v36, %[vfl], %[ws], %[pol] offen sc1\n" \
    "s_waitcnt lgkmcnt(4)\n" \
    "v_mfma_f32_16x16x32_bf16 v[56:59], a[240:243], v[104:107], v[56:59]\n" \
    "ds_read_b128 v[92:95], v32 offset:32768\n" \
    "s_add_u32 s95, s95, 1\n" \
    "v_mfma_f32_16x16x32_bf16 v[64:67], a[244:247], v[104:107], v[64:67]\n" \
    "s_add_u32 s60, s60, 65536\n" \
    "s_and_b32 s60, s60, 0x3fffff\n" \
    "s_waitcnt lgkmcnt(4)\n" \
    "v_mfma_f32_16x16x32_bf16 v[60:63], a[240:243], v[108:111], v[60:63]\n" \
    "ds_read_b128 v[96:99], v33 offset:0\n" \
    "s_add_u32 s97, s60, 65536\n" \
    "v_mfma_f32_16x16x32_bf16 v[68:71], a[244:247], v[108:111], v[68:71]\n" \
    "s_and_b32 s97, s97, 0x3f0000\n" \
    "buffer_load_dword v38, %[vxt], %[dx], s97 offen\n" \
    "s_waitcnt lgkmcnt(4)\n" \
    "v_mfma_f32_16x16x32_bf16 v[56:59], a[248:251], v[112:115], v[56:59]\n" \
    "ds_read_b128 v[100:103], v33 offset:32768\n" \
    "v_mfma_f32_16x16x32_bf16 v[64:67], a[252:255], v[112:115], v[64:67]\n" \
    "s_waitcnt lgkmcnt(4)\n" \
    "v_mfma_f32_16x16x32_bf16 v[60:63], a[248:251], v[116:119], v[60:63]\n" \
    "ds_read_b128 v[104:107], v32 offset:2048\n" \
    "v_mfma_f32_16x16x32_bf16 v[68:71], a[252:255], v[116:119], v[68:71]\n" \
    "s_cmp_lt_u32 s95, 64\n" \
    "s_cbranch_scc1 LS1_seg_%=\n" \
    "s_nop 7\n" \
    "s_nop 1\n" \
    "s_add_u32 s63, s62, 131072\n" \
    "v_mov_b64 v[208:209], v[200:201]\n" \
    "v_mov_b64 v[210:211], v[202:203]\n" \
    "v_mov_b64 v[212:213], v[204:205]\n" \
    "v_mov_b64 v[214:215], v[206:207]\n" \
    "v_mul_f32 v192, v128, v56\n" \
    "v_mul_f32 v193, v129, v57\n" \
    "v_mul_f32 v194, v130, v58\n" \
    "v_mul_f32 v195, v131, v59\n" \
    "v_mul_f32 v196, v128, v64\n" \
    "v_mul_f32 v197, v129, v65\n" \
    "v_mul_f32 v198, v130, v66\n" \
    "v_mul_f32 v199, v131, v67\n" \
    "v_fma_f32 v192, -v132, v64, v192\n" \
    "v_fma_f32 v193, -v133, v65, v193\n" \
    "v_fma_f32 v194, -v134, v66, v194\n" \
    "v_fma_f32 v195, -v135, v67, v195\n" \
    "v_fmac_f32 v196, v132, v56\n" \
    "v_fmac_f32 v197, v133, v57\n" \
    "v_fmac_f32 v198, v134, v58\n" \
    "v_fmac_f32 v199, v135, v59\n" \
    "v_mul_f32 v200, v128, v60\n" \
    "v_mul_f32 v201, v129, v61\n" \
    "v_mul_f32 v202, v130, v62\n" \
    "v_mul_f32 v203, v131, v63\n" \
    "v_mul_f32 v204, v128, v68\n" \
    "v_mul_f32 v205, v129, v69\n" \
    "v_mul_f32 v206, v130, v70\n" \
    "v_mul_f32 v207, v131, v71\n" \
    "v_fma_f32 v200, -v132, v68, v200\n" \
    "v_fma_f32 v201, -v133, v69, v201\n" \
    "v_fma_f32 v202, -v134, v70, v202\n" \
    "v_fma_f32 v203, -v135, v71, v203\n" \
    "v_fmac_f32 v204, v132, v60\n" \
    "v_fmac_f32 v205, v133, v61\n" \
    "v_fmac_f32 v206, v134, v62\n" \
    "v_fmac_f32 v207, v135, v63\n" \
    "v_fmac_f32_dpp v192, v208, v136 row_ror:1 row_mask:0xf bank_mask:0xf bound_ctrl:1\n" \
    "v_fmac_f32_dpp v193, v209, v137 row_ror:1 row_mask:0xf bank_mask:0xf bound_ctrl:1\n" \
    "v_fmac_f32_dpp v194, v210, v138 row_ror:1 row_mask:0xf bank_mask:0xf bound_ctrl:1\n" \
    "v_fmac_f32_dpp v195, v211, v139 row_ror:1 row_mask:0xf bank_mask:0xf bound_ctrl:1\n" \
    "v_fmac_f32_dpp v192, -v212, v140 row_ror:1 row_mask:0xf bank_mask:0xf bound_ctrl:1\n" \
    "v_fmac_f32_dpp v193, -v213, v141 row_ror:1 row_mask:0xf bank_mask:0xf bound_ctrl:1\n" \
    "v_fmac_f32_dpp v194, -v214, v142 row_ror:1 row_mask:0xf bank_mask:0xf bound_ctrl:1\n" \
    "v_fmac_f32_dpp v195, -v215, v143 row_ror:1 row_mask:0xf bank_mask:0xf bound_ctrl:1\n" \
    "v_fmac_f32_dpp v196, v212, v136 row_ror:1 row_mask:0xf bank_mask:0xf bound_ctrl:1\n" \
    "v_fmac_f32_dpp v197, v213, v137 row_ror:1 row_mask:0xf bank_mask:0xf bound_ctrl:1\n" \
    "v_fmac_f32_dpp v198, v214, v138 row_ror:1 row_mask:0xf bank_mask:0xf bound_ctrl:1\n" \
    "v_fmac_f32_dpp v199, v215, v139 row_ror:1 row_mask:0xf bank_mask:0xf bound_ctrl:1\n" \
    "v_fmac_f32_dpp v196, v208, v140 row_ror:1 row_mask:0xf bank_mask:0xf bound_ctrl:1\n" \
    "v_fmac_f32_dpp v197, v209, v141 row_ror:1 row_mask:0xf bank_mask:0xf bound_ctrl:1\n" \
    "v_fmac_f32_dpp v198, v210, v142 row_ror:1 row_mask:0xf bank_mask:0xf bound_ctrl:1\n" \
    "v_fmac_f32_dpp v199, v211, v143 row_ror:1 row_mask:0xf bank_mask:0xf bound_ctrl:1\n" \
    "v_fmac_f32 v200, v192, v144\n" \
    "v_fmac_f32 v201, v193, v145\n" \
    "v_fmac_f32 v202, v194, v146\n" \
    "v_fmac_f32 v203, v195, v147\n" \
    "v_fma_f32 v200, -v196, v148, v200\n" \
    "v_fma_f32 v201, -v197, v149, v201\n" \
    "v_fma_f32 v202, -v198, v150, v202\n" \
    "v_fma_f32 v203, -v199, v151, v203\n" \
    "v_fmac_f32 v204, v196, v144\n" \
    "v_fmac_f32 v205, v197, v145\n" \
    "v_fmac_f32 v206, v198, v146\n" \
    "v_fmac_f32 v207, v199, v147\n" \
    "v_fmac_f32 v204, v192, v148\n" \
    "v_fmac_f32 v205, v193, v149\n" \
    "v_fmac_f32 v206, v194, v150\n" \
    "v_fmac_f32 v207, v195, v151\n" \
    "v_mov_b64 v[216:217], v[200:201]\n" \
    "v_mov_b64 v[218:219], v[202:203]\n" \
    "v_fmac_f32_dpp v200, v200, v152 row_shr:1 row_mask:0xf bank_mask:0xf bound_ctrl:1\n" \
    "v_fmac_f32_dpp v201, v201, v153 row_shr:1 row_mask:0xf bank_mask:0xf bound_ctrl:1\n" \
    "v_fmac_f32_dpp v202, v202, v154 row_shr:1 row_mask:0xf bank_mask:0xf bound_ctrl:1\n" \
    "v_fmac_f32_dpp v203, v203, v155 row_shr:1 row_mask:0xf bank_mask:0xf bound_ctrl:1\n" \
    "v_fmac_f32_dpp v200, -v204, v156 row_shr:1 row_mask:0xf bank_mask:0xf bound_ctrl:1\n" \
    "v_fmac_f32_dpp v201, -v205, v157 row_shr:1 row_mask:0xf bank_mask:0xf bound_ctrl:1\n" \
    "v_fmac_f32_dpp v202, -v206, v158 row_shr:1 row_mask:0xf bank_mask:0xf bound_ctrl:1\n" \
    "v_fmac_f32_dpp v203, -v207, v159 row_shr:1 row_mask:0xf bank_mask:0xf bound_ctrl:1\n" \
    "v_fmac_f32_dpp v204, v204, v152 row_shr:1 row_mask:0xf bank_mask:0xf bound_ctrl:1\n" \
    "v_fmac_f32_dpp v205, v205, v153 row_shr:1 row_mask:0xf bank_mask:0xf bound_ctrl:1\n" \
    "v_fmac_f32_dpp v206, v206, v154 row_shr:1 row_mask:0xf bank_mask:0xf bound_ctrl:1\n" \
    "v_fmac_f32_dpp v207, v207, v155 row_shr:1 row_mask:0xf bank_mask:0xf bound_ctrl:1\n" \
    "v_fmac_f32_dpp v204, v216, v156 row_shr:1 row_mask:0xf bank_mask:0xf bound_ctrl:1\n" \
    "v_fmac_f32_dpp v205, v217, v157 row_shr:1 row_mask:0xf bank_mask:0xf bound_ctrl:1\n" \
    "v_fmac_f32_dpp v206, v218, v158 row_shr:1 row_mask:0xf bank_mask:0xf bound_ctrl:1\n" \
    "v_fmac_f32_dpp v207, v219, v159 row_shr:1 row_mask:0xf bank_mask:0xf bound_ctrl:1\n" \
    "v_mov_b64 v[216:217], v[200:201]\n" \
    "v_mov_b64 v[218:219], v[202:203]\n" \
    "v_fmac_f32_dpp v200, v200, v160 row_shr:2 row_mask:0xf bank_mask:0xf bound_ctrl:1\n" \
    "v_fmac_f32_dpp v201, v201, v161 row_shr:2 row_mask:0xf bank_mask:0xf bound_ctrl:1\n" \
    "v_fmac_f32_dpp v202, v202, v162 row_shr:2 row_mask:0xf bank_mask:0xf bound_ctrl:1\n" \
    "v_fmac_f32_dpp v203, v203, v163 row_shr:2 row_mask:0xf bank_mask:0xf bound_ctrl:1\n" \
    "v_fmac_f32_dpp v200, -v204, v164 row_shr:2 row_mask:0xf bank_mask:0xf bound_ctrl:1\n" \
    "v_fmac_f32_dpp v201, -v205, v165 row_shr:2 row_mask:0xf bank_mask:0xf bound_ctrl:1\n" \
    "v_fmac_f32_dpp v202, -v206, v166 row_shr:2 row_mask:0xf bank_mask:0xf bound_ctrl:1\n" \
    "v_fmac_f32_dpp v203, -v207, v167 row_shr:2 row_mask:0xf bank_mask:0xf bound_ctrl:1\n" \
    "v_fmac_f32_dpp v204, v204, v160 row_shr:2 row_mask:0xf bank_mask:0xf bound_ctrl:1\n" \
    "v_fmac_f32_dpp v205, v205, v161 row_shr:2 row_mask:0xf bank_mask:0xf bound_ctrl:1\n" \
    "v_fmac_f32_dpp v206, v206, v162 row_shr:2 row_mask:0xf bank_mask:0xf bound_ctrl:1\n" \
    "v_fmac_f32_dpp v207, v207, v163 row_shr:2 row_mask:0xf bank_mask:0xf bound_ctrl:1\n" \
    "v_fmac_f32_dpp v204, v216, v164 row_shr:2 row_mask:0xf bank_mask:0xf bound_ctrl:1\n" \
    "v_fmac_f32_dpp v205, v217, v165 row_shr:2 row_mask:0xf bank_mask:0xf bound_ctrl:1\n" \
    "v_fmac_f32_dpp v206, v218, v166 row_shr:2 row_mask:0xf bank_mask:0xf bound_ctrl:1\n" \
    "v_fmac_f32_dpp v207, v219, v167 row_shr:2 row_mask:0xf bank_mask:0xf bound_ctrl:1\n" \
    "v_mov_b64 v[216:217], v[200:201]\n" \
    "v_mov_b64 v[218:219], v[202:203]\n" \
    "v_fmac_f32_dpp v200, v200, v168 row_shr:4 row_mask:0xf bank_mask:0xf bound_ctrl:1\n" \
    "v_fmac_f32_dpp v201, v201, v169 row_shr:4 row_mask:0xf bank_mask:0xf bound_ctrl:1\n" \
    "v_fmac_f32_dpp v202, v202, v170 row_shr:4 row_mask:0xf bank_mask:0xf bound_ctrl:1\n" \
    "v_fmac_f32_dpp v203, v203, v171 row_shr:4 row_mask:0xf bank_mask:0xf bound_ctrl:1\n" \
    "v_fmac_f32_dpp v200, -v204, v172 row_shr:4 row_mask:0xf bank_mask:0xf bound_ctrl:1\n" \
    "v_fmac_f32_dpp v201, -v205, v173 row_shr:4 row_mask:0xf bank_mask:0xf bound_ctrl:1\n" \
    "v_fmac_f32_dpp v202, -v206, v174 row_shr:4 row_mask:0xf bank_mask:0xf bound_ctrl:1\n" \
    "v_fmac_f32_dpp v203, -v207, v175 row_shr:4 row_mask:0xf bank_mask:0xf bound_ctrl:1\n" \
    "v_fmac_f32_dpp v204, v204, v168 row_shr:4 row_mask:0xf bank_mask:0xf bound_ctrl:1\n" \
    "v_fmac_f32_dpp v205, v205, v169 row_shr:4 row_mask:0xf bank_mask:0xf bound_ctrl:1\n" \
    "v_fmac_f32_dpp v206, v206, v170 row_shr:4 row_mask:0xf bank_mask:0xf bound_ctrl:1\n" \
    "v_fmac_f32_dpp v207, v207, v171 row_shr:4 row_mask:0xf bank_mask:0xf bound_ctrl:1\n" \
    "v_fmac_f32_dpp v204, v216, v172 row_shr:4 row_mask:0xf bank_mask:0xf bound_ctrl:1\n" \
    "v_fmac_f32_dpp v205, v217, v173 row_shr:4 row_mask:0xf bank_mask:0xf bound_ctrl:1\n" \
    "v_fmac_f32_dpp v206, v218, v174 row_shr:4 row_mask:0xf bank_mask:0xf bound_ctrl:1\n" \
    "v_fmac_f32_dpp v207, v219, v175 row_shr:4 row_mask:0xf bank_mask:0xf bound_ctrl:1\n" \
    "v_mov_b64 v[216:217], v[200:201]\n" \
    "v_mov_b64 v[218:219], v[202:203]\n" \
    "v_fmac_f32_dpp v200, v200, v176 row_shr:8 row_mask:0xf bank_mask:0xf bound_ctrl:1\n" \
    "v_fmac_f32_dpp v201, v201, v177 row_shr:8 row_mask:0xf bank_mask:0xf bound_ctrl:1\n" \
    "v_fmac_f32_dpp v202, v202, v178 row_shr:8 row_mask:0xf bank_mask:0xf bound_ctrl:1\n" \
    "v_fmac_f32_dpp v203, v203, v179 row_shr:8 row_mask:0xf bank_mask:0xf bound_ctrl:1\n" \
    "v_fmac_f32_dpp v200, -v204, v180 row_shr:8 row_mask:0xf bank_mask:0xf bound_ctrl:1\n" \
    "v_fmac_f32_dpp v201, -v205, v181 row_shr:8 row_mask:0xf bank_mask:0xf bound_ctrl:1\n" \
    "v_fmac_f32_dpp v202, -v206, v182 row_shr:8 row_mask:0xf bank_mask:0xf bound_ctrl:1\n" \
    "v_fmac_f32_dpp v203, -v207, v183 row_shr:8 row_mask:0xf bank_mask:0xf bound_ctrl:1\n" \
    "v_fmac_f32_dpp v204, v204, v176 row_shr:8 row_mask:0xf bank_mask:0xf bound_ctrl:1\n" \
    "v_fmac_f32_dpp v205, v205, v177 row_shr:8 row_mask:0xf bank_mask:0xf bound_ctrl:1\n" \
    "v_fmac_f32_dpp v206, v206, v178 row_shr:8 row_mask:0xf bank_mask:0xf bound_ctrl:1\n" \
    "v_fmac_f32_dpp v207, v207, v179 row_shr:8 row_mask:0xf bank_mask:0xf bound_ctrl:1\n" \
    "v_fmac_f32_dpp v204, v216, v180 row_shr:8 row_mask:0xf bank_mask:0xf bound_ctrl:1\n" \
    "v_fmac_f32_dpp v205, v217, v181 row_shr:8 row_mask:0xf bank_mask:0xf bound_ctrl:1\n" \
    "v_fmac_f32_dpp v206, v218, v182 row_shr:8 row_mask:0xf bank_mask:0xf bound_ctrl:1\n" \
    "v_fmac_f32_dpp v207, v219, v183 row_shr:8 row_mask:0xf bank_mask:0xf bound_ctrl:1\n" \
    "v_fmac_f32_dpp v192, v200, v144 row_shr:1 row_mask:0xf bank_mask:0xf bound_ctrl:1\n" \
    "v_fmac_f32_dpp v193, v201, v145 row_shr:1 row_mask:0xf bank_mask:0xf bound_ctrl:1\n" \
    "v_fmac_f32_dpp v194, v202, v146 row_shr:1 row_mask:0xf bank_mask:0xf bound_ctrl:1\n" \
    "v_fmac_f32_dpp v195, v203, v147 row_shr:1 row_mask:0xf bank_mask:0xf bound_ctrl:1\n" \
    "v_fmac_f32_dpp v192, -v204, v148 row_shr:1 row_mask:0xf bank_mask:0xf bound_ctrl:1\n" \
    "v_fmac_f32_dpp v193, -v205, v149 row_shr:1 row_mask:0xf bank_mask:0xf bound_ctrl:1\n" \
    "v_fmac_f32_dpp v194, -v206, v150 row_shr:1 row_mask:0xf bank_mask:0xf bound_ctrl:1\n" \
    "v_fmac_f32_dpp v195, -v207, v151 row_shr:1 row_mask:0xf bank_mask:0xf bound_ctrl:1\n" \
    "v_fmac_f32_dpp v196, v204, v144 row_shr:1 row_mask:0xf bank_mask:0xf bound_ctrl:1\n" \
    "v_fmac_f32_dpp v197, v205, v145 row_shr:1 row_mask:0xf bank_mask:0xf bound_ctrl:1\n" \
    "v_fmac_f32_dpp v198, v206, v146 row_shr:1 row_mask:0xf bank_mask:0xf bound_ctrl:1\n" \
    "v_fmac_f32_dpp v199, v207, v147 row_shr:1 row_mask:0xf bank_mask:0xf bound_ctrl:1\n" \
    "v_fmac_f32_dpp v196, v200, v148 row_shr:1 row_mask:0xf bank_mask:0xf bound_ctrl:1\n" \
    "v_fmac_f32_dpp v197, v201, v149 row_shr:1 row_mask:0xf bank_mask:0xf bound_ctrl:1\n" \
    "v_fmac_f32_dpp v198, v202, v150 row_shr:1 row_mask:0xf bank_mask:0xf bound_ctrl:1\n" \
    "v_fmac_f32_dpp v199, v203, v151 row_shr:1 row_mask:0xf bank_mask:0xf bound_ctrl:1\n" \
    "v_cvt_pk_bf16_f32 v120, v192, v193\n" \
    "v_cvt_pk_bf16_f32 v121, v194, v195\n" \
    "v_cvt_pk_bf16_f32 v122, -v196, -v197\n" \
    "v_cvt_pk_bf16_f32 v123, -v198, -v199\n" \
    "buffer_store_dwordx4 v[120:123], %[voa], %[ws], s62 offen sc1\n" \
    "v_cvt_pk_bf16_f32 v124, v200, v201\n" \
    "v_cvt_pk_bf16_f32 v125, v202, v203\n" \
    "v_cvt_pk_bf16_f32 v126, -v204, -v205\n" \
    "v_cvt_pk_bf16_f32 v127, -v206, -v207\n" \
    "buffer_store_dwordx4 v[124:127], %[voa], %[ws], s63 offen sc1\n" \
    "s_mul_i32 s62, s95, 0x202000\n" \
    "s_and_b32 s62, s62, 0xffe02000\n" \
    "s_xor_b32 s62, s62, %[pol]\n" \
    "s_waitcnt vmcnt(0) lgkmcnt(0)\n" \
    "s_barrier\n" \
    "v_mov_b32 v36, 63\n" \
    "buffer_store_dword v36, %[vfl], %[ws], %[pol] offen sc1\n" \
    "s_branch LS1_end_%=\n" \
    "LS1_hsr_%=:\n" \
    "s_sleep 2\n" \
    "s_add_u32 s98, s98, 1\n" \
    "s_cmp_gt_u32 s98, 131072\n" \
    "s_cbranch_scc1 LS1_fail_%=\n" \
    "s_branch LS1_hs_%=\n" \
    "LS1_redo_%=:\n" \
    "s_waitcnt vmcnt(0) lgkmcnt(0)\n" \
    "s_barrier\n" \
    "s_or_b32 %[cfg], s97, 256\n" \
    "s_branch LS1_end_%=\n" \
    "LS1_fail_%=:\n" \
    "v_mov_b32 v36, 2043904\n" \
    "v_mov_b32 v37, 1\n" \
    "buffer_store_dword v37, v36, %[ws], 0 offen sc1\n" \
    "s_endpgm\n" \
    "LS1_end_%=:\n"
#define ASM_ROLE_C \
    "s_nop 4\n" \
    "s_mov_b32 s98, 0\n" \
    "s_mov_b32 s95, 0\n" \
    "s_add_u32 s97, %[hsa], 32768\n" \
    "buffer_load_dword v31, off, %[ws], s97 sc1\n" \
    "s_mul_i32 s64, %[w], 24576\n" \
    "s_add_u32 s64, s64, 65536\n" \
    "s_lshl_b32 s65, %[w], 21\n" \
    "v_xor_b32 v88, 0, %[rt]\n" \
    "v_xor_b32 v89, 32, %[rt]\n" \
    "v_xor_b32 v90, 64, %[rt]\n" \
    "v_xor_b32 v91, 96, %[rt]\n" \
    "s_add_u32 m0, s64, 0\n" \
    "s_add_u32 s97, s65, 0\n" \
    "buffer_load_dwordx4 %[vow], %[dw], s97 offen lds\n" \
    "s_add_u32 m0, s64, 1024\n" \
    "s_add_u32 s97, s65, 16384\n" \
    "buffer_load_dwordx4 %[vow], %[dw], s97 offen lds\n" \
    "s_add_u32 m0, s64, 2048\n" \
    "s_add_u32 s97, s65, 32768\n" \
    "buffer_load_dwordx4 %[vow], %[dw], s97 offen lds\n" \
    "s_add_u32 m0, s64, 3072\n" \
    "s_add_u32 s97, s65, 49152\n" \
    "buffer_load_dwordx4 %[vow], %[dw], s97 offen lds\n" \
    "s_add_u32 m0, s64, 4096\n" \
    "s_add_u32 s97, s65, 65536\n" \
    "buffer_load_dwordx4 %[vow], %[dw], s97 offen lds\n" \
    "s_add_u32 m0, s64, 5120\n" \
    "s_add_u32 s97, s65, 81920\n" \
    "buffer_load_dwordx4 %[vow], %[dw], s97 offen lds\n" \
    "s_add_u32 m0, s64, 6144\n" \
    "s_add_u32 s97, s65, 98304\n" \
    "buffer_load_dwordx4 %[vow], %[dw], s97 offen lds\n" \
    "s_add_u32 m0, s64, 7168\n" \
    "s_add_u32 s97, s65, 114688\n" \
    "buffer_load_dwordx4 %[vow], %[dw], s97 offen lds\n" \
    "s_add_u32 m0, s64, 8192\n" \
    "s_add_u32 s97, s65, 131072\n" \
    "buffer_load_dwordx4 %[vow], %[dw], s97 offen lds\n" \
    "s_add_u32 m0, s64, 9216\n" \
    "s_add_u32 s97, s65, 147456\n" \
    "buffer_load_dwordx4 %[vow], %[dw], s97 offen lds\n" \
    "s_add_u32 m0, s64, 10240\n" \
    "s_add_u32 s97, s65, 163840\n" \
    "buffer_load_dwordx4 %[vow], %[dw], s97 offen lds\n" \
    "s_add_u32 m0, s64, 11264\n" \
    "s_add_u32 s97, s65, 180224\n" \
    "buffer_load_dwordx4 %[vow], %[dw], s97 offen lds\n" \
    "s_add_u32 m0, s64, 12288\n" \
    "s_add_u32 s97, s65, 196608\n" \
    "buffer_load_dwordx4 %[vow], %[dw], s97 offen lds\n" \
    "s_add_u32 m0, s64, 13312\n" \
    "s_add_u32 s97, s65, 212992\n" \
    "buffer_load_dwordx4 %[vow], %[dw], s97 offen lds\n" \
    "s_add_u32 m0, s64, 14336\n" \
    "s_add_u32 s97, s65, 229376\n" \
    "buffer_load_dwordx4 %[vow], %[dw], s97 offen lds\n" \
    "s_add_u32 m0, s64, 15360\n" \
    "s_add_u32 s97, s65, 245760\n" \
    "buffer_load_dwordx4 %[vow], %[dw], s97 offen lds\n" \
    "s_add_u32 m0, s64, 16384\n" \
    "s_add_u32 s97, s65, 262144\n" \
    "buffer_load_dwordx4 %[vow], %[dw], s97 offen lds\n" \
    "s_add_u32 m0, s64, 17408\n" \
    "s_add_u32 s97, s65, 278528\n" \
    "buffer_load_dwordx4 %[vow], %[dw], s97 offen lds\n" \
    "s_add_u32 m0, s64, 18432\n" \
    "s_add_u32 s97, s65, 294912\n" \
    "buffer_load_dwordx4 %[vow], %[dw], s97 offen lds\n" \
    "s_add_u32 m0, s64, 19456\n" \
    "s_add_u32 s97, s65, 311296\n" \
    "buffer_load_dwordx4 %[vow], %[dw], s97 offen lds\n" \
    "s_add_u32 m0, s64, 20480\n" \
    "s_add_u32 s97, s65, 327680\n" \
    "buffer_load_dwordx4 %[vow], %[dw], s97 offen lds\n" \
    "s_add_u32 m0, s64, 21504\n" \
    "s_add_u32 s97, s65, 344064\n" \
    "buffer_load_dwordx4 %[vow], %[dw], s97 offen lds\n" \
    "s_add_u32 m0, s64, 22528\n" \
    "s_add_u32 s97, s65, 360448\n" \
    "buffer_load_dwordx4 %[vow], %[dw], s97 offen lds\n" \
    "s_add_u32 m0, s64, 23552\n" \
    "s_add_u32 s97, s65, 376832\n" \
    "buffer_load_dwordx4 %[vow], %[dw], s97 offen lds\n" \
    "s_waitcnt vmcnt(16)\n" \
    "v_readfirstlane_b32 %[cfg], v31\n" \
    "s_nop 1\n" \
    "s_add_u32 %[cfg], %[cfg], 256\n" \
    "s_and_b32 %[cfg], %[cfg], 0xffffff00\n" \
    "v_mov_b32 v31, %[cfg]\n" \
    "buffer_store_dword v31, off, %[ws], %[hsa] sc1\n" \
    "ds_read_b64_tr_b16 a[0:1], v88 offset:0\n" \
    "ds_read_b64_tr_b16 a[2:3], v88 offset:2048\n" \
    "ds_read_b64_tr_b16 a[4:5], v89 offset:0\n" \
    "ds_read_b64_tr_b16 a[6:7], v89 offset:2048\n" \
    "ds_read_b64_tr_b16 a[8:9], v90 offset:0\n" \
    "ds_read_b64_tr_b16 a[10:11], v90 offset:2048\n" \
    "ds_read_b64_tr_b16 a[12:13], v91 offset:0\n" \
    "ds_read_b64_tr_b16 a[14:15], v91 offset:2048\n" \
    "ds_read_b64_tr_b16 a[16:17], v88 offset:4096\n" \
    "ds_read_b64_tr_b16 a[18:19], v88 offset:6144\n" \
    "ds_read_b64_tr_b16 a[20:21], v89 offset:4096\n" \
    "ds_read_b64_tr_b16 a[22:23], v89 offset:6144\n" \
    "ds_read_b64_tr_b16 a[24:25], v90 offset:4096\n" \
    "ds_read_b64_tr_b16 a[26:27], v90 offset:6144\n" \
    "ds_read_b64_tr_b16 a[28:29], v91 offset:4096\n" \
    "ds_read_b64_tr_b16 a[30:31], v91 offset:6144\n" \
    "s_waitcnt lgkmcnt(0)\n" \
    "s_add_u32 m0, s64, 0\n" \
    "s_add_u32 s97, s65, 393216\n" \
    "buffer_load_dwordx4 %[vow], %[dw], s97 offen lds\n" \
    "s_add_u32 m0, s64, 1024\n" \
    "s_add_u32 s97, s65, 409600\n" \
    "buffer_load_dwordx4 %[vow], %[dw], s97 offen lds\n" \
    "s_add_u32 m0, s64, 2048\n" \
    "s_add_u32 s97, s65, 425984\n" \
    "buffer_load_dwordx4 %[vow], %[dw], s97 offen lds\n" \
    "s_add_u32 m0, s64, 3072\n" \
    "s_add_u32 s97, s65, 442368\n" \
    "buffer_load_dwordx4 %[vow], %[dw], s97 offen lds\n" \
    "s_add_u32 m0, s64, 4096\n" \
    "s_add_u32 s97, s65, 458752\n" \
    "buffer_load_dwordx4 %[vow], %[dw], s97 offen lds\n" \
    "s_add_u32 m0, s64, 5120\n" \
    "s_add_u32 s97, s65, 475136\n" \
    "buffer_load_dwordx4 %[vow], %[dw], s97 offen lds\n" \
    "s_add_u32 m0, s64, 6144\n" \
    "s_add_u32 s97, s65, 491520\n" \
    "buffer_load_dwordx4 %[vow], %[dw], s97 offen lds\n" \
    "s_add_u32 m0, s64, 7168\n" \
    "s_add_u32 s97, s65, 507904\n" \
    "buffer_load_dwordx4 %[vow], %[dw], s97 offen lds\n" \
    "s_waitcnt vmcnt(16)\n" \
    "ds_read_b64_tr_b16 a[32:33], v88 offset:8192\n" \
    "ds_read_b64_tr_b16 a[34:35], v88 offset:10240\n" \
    "ds_read_b64_tr_b16 a[36:37], v89 offset:8192\n" \
    "ds_read_b64_tr_b16 a[38:39], v89 offset:10240\n" \
    "ds_read_b64_tr_b16 a[40:41], v90 offset:8192\n" \
    "ds_read_b64_tr_b16 a[42:43], v90 offset:10240\n" \
    "ds_read_b64_tr_b16 a[44:45], v91 offset:8192\n" \
    "ds_read_b64_tr_b16 a[46:47], v91 offset:10240\n" \
    "ds_read_b64_tr_b16 a[48:49], v88 offset:12288\n" \
    "ds_read_b64_tr_b16 a[50:51], v88 offset:14336\n" \
    "ds_read_b64_tr_b16 a[52:53], v89 offset:12288\n" \
    "ds_read_b64_tr_b16 a[54:55], v89 offset:14336\n" \
    "ds_read_b64_tr_b16 a[56:57], v90 offset:12288\n" \
    "ds_read_b64_tr_b16 a[58:59], v90 offset:14336\n" \
    "ds_read_b64_tr_b16 a[60:61], v91 offset:12288\n" \
    "ds_read_b64_tr_b16 a[62:63], v91 offset:14336\n" \
    "s_waitcnt lgkmcnt(0)\n" \
    "s_add_u32 m0, s64, 8192\n" \
    "s_add_u32 s97, s65, 524288\n" \
    "buffer_load_dwordx4 %[vow], %[dw], s97 offen lds\n" \
    "s_add_u32 m0, s64, 9216\n" \
    "s_add_u32 s97, s65, 540672\n" \
    "buffer_load_dwordx4 %[vow], %[dw], s97 offen lds\n" \
    "s_add_u32 m0, s64, 10240\n" \
    "s_add_u32 s97, s65, 557056\n" \
    "buffer_load_dwordx4 %[vow], %[dw], s97 offen lds\n" \
    "s_add_u32 m0, s64, 11264\n" \
    "s_add_u32 s97, s65, 573440\n" \
    "buffer_load_dwordx4 %[vow], %[dw], s97 offen lds\n" \
    "s_add_u32 m0, s64, 12288\n" \
    "s_add_u32 s97, s65, 589824\n" \
    "buffer_load_dwordx4 %[vow], %[dw], s97 offen lds\n" \
    "s_add_u32 m0, s64, 13312\n" \
    "s_add_u32 s97, s65, 606208\n" \
    "buffer_load_dwordx4 %[vow], %[dw], s97 offen lds\n" \
    "s_add_u32 m0, s64, 14336\n" \
    "s_add_u32 s97, s65, 622592\n" \
    "buffer_load_dwordx4 %[vow], %[dw], s97 offen lds\n" \
    "s_add_u32 m0, s64, 15360\n" \
    "s_add_u32 s97, s65, 638976\n" \
    "buffer_load_dwordx4 %[vow], %[dw], s97 offen lds\n" \
    "s_waitcnt vmcnt(16)\n" \
    "ds_read_b64_tr_b16 a[64:65], v88 offset:16384\n" \
    "ds_read_b64_tr_b16 a[66:67], v88 offset:18432\n" \
    "ds_read_b64_tr_b16 a[68:69], v89 offset:16384\n" \
    "ds_read_b64_tr_b16 a[70:71], v89 offset:18432\n" \
    "ds_read_b64_tr_b16 a[72:73], v90 offset:16384\n" \
    "ds_read_b64_tr_b16 a[74:75], v90 offset:18432\n" \
    "ds_read_b64_tr_b16 a[76:77], v91 offset:16384\n" \
    "ds_read_b64_tr_b16 a[78:79], v91 offset:18432\n" \
    "ds_read_b64_tr_b16 a[80:81], v88 offset:20480\n" \
    "ds_read_b64_tr_b16 a[82:83], v88 offset:22528\n" \
    "ds_read_b64_tr_b16 a[84:85], v89 offset:20480\n" \
    "ds_read_b64_tr_b16 a[86:87], v89 offset:22528\n" \
    "ds_read_b64_tr_b16 a[88:89], v90 offset:20480\n" \
    "ds_read_b64_tr_b16 a[90:91], v90 offset:22528\n" \
    "ds_read_b64_tr_b16 a[92:93], v91 offset:20480\n" \
    "ds_read_b64_tr_b16 a[94:95], v91 offset:22528\n" \
    "s_waitcnt lgkmcnt(0)\n" \
    "s_add_u32 m0, s64, 16384\n" \
    "s_add_u32 s97, s65, 655360\n" \
    "buffer_load_dwordx4 %[vow], %[dw], s97 offen lds\n" \
    "s_add_u32 m0, s64, 17408\n" \
    "s_add_u32 s97, s65, 671744\n" \
    "buffer_load_dwordx4 %[vow], %[dw], s97 offen lds\n" \
    "s_add_u32 m0, s64, 18432\n" \
    "s_add_u32 s97, s65, 688128\n" \
    "buffer_load_dwordx4 %[vow], %[dw], s97 offen lds\n" \
    "s_add_u32 m0, s64, 19456\n" \
    "s_add_u32 s97, s65, 704512\n" \
    "buffer_load_dwordx4 %[vow], %[dw], s97 offen lds\n" \
    "s_add_u32 m0, s64, 20480\n" \
    "s_add_u32 s97, s65, 720896\n" \
    "buffer_load_dwordx4 %[vow], %[dw], s97 offen lds\n" \
    "s_add_u32 m0, s64, 21504\n" \
    "s_add_u32 s97, s65, 737280\n" \
    "buffer_load_dwordx4 %[vow], %[dw], s97 offen lds\n" \
    "s_add_u32 m0, s64, 22528\n" \
    "s_add_u32 s97, s65, 753664\n" \
    "buffer_load_dwordx4 %[vow], %[dw], s97 offen lds\n" \
    "s_add_u32 m0, s64, 23552\n" \
    "s_add_u32 s97, s65, 770048\n" \
    "buffer_load_dwordx4 %[vow], %[dw], s97 offen lds\n" \
    "s_waitcnt vmcnt(16)\n" \
    "ds_read_b64_tr_b16 a[96:97], v88 offset:0\n" \
    "ds_read_b64_tr_b16 a[98:99], v88 offset:2048\n" \
    "ds_read_b64_tr_b16 a[100:101], v89 offset:0\n" \
    "ds_read_b64_tr_b16 a[102:103], v89 offset:2048\n" \
    "ds_read_b64_tr_b16 a[104:105], v90 offset:0\n" \
    "ds_read_b64_tr_b16 a[106:107], v90 offset:2048\n" \
    "ds_read_b64_tr_b16 a[108:109], v91 offset:0\n" \
    "ds_read_b64_tr_b16 a[110:111], v91 offset:2048\n" \
    "ds_read_b64_tr_b16 a[112:113], v88 offset:4096\n" \
    "ds_read_b64_tr_b16 a[114:115], v88 offset:6144\n" \
    "ds_read_b64_tr_b16 a[116:117], v89 offset:4096\n" \
    "ds_read_b64_tr_b16 a[118:119], v89 offset:6144\n" \
    "ds_read_b64_tr_b16 a[120:121], v90 offset:4096\n" \
    "ds_read_b64_tr_b16 a[122:123], v90 offset:6144\n" \
    "ds_read_b64_tr_b16 a[124:125], v91 offset:4096\n" \
    "ds_read_b64_tr_b16 a[126:127], v91 offset:6144\n" \
    "s_waitcnt lgkmcnt(0)\n" \
    "s_add_u32 m0, s64, 0\n" \
    "s_add_u32 s97, s65, 786432\n" \
    "buffer_load_dwordx4 %[vow], %[dw], s97 offen lds\n" \
    "s_add_u32 m0, s64, 1024\n" \
    "s_add_u32 s97, s65, 802816\n" \
    "buffer_load_dwordx4 %[vow], %[dw], s97 offen lds\n" \
    "s_add_u32 m0, s64, 2048\n" \
    "s_add_u32 s97, s65, 819200\n" \
    "buffer_load_dwordx4 %[vow], %[dw], s97 offen lds\n" \
    "s_add_u32 m0, s64, 3072\n" \
    "s_add_u32 s97, s65, 835584\n" \
    "buffer_load_dwordx4 %[vow], %[dw], s97 offen lds\n" \
    "s_add_u32 m0, s64, 4096\n" \
    "s_add_u32 s97, s65, 851968\n" \
    "buffer_load_dwordx4 %[vow], %[dw], s97 offen lds\n" \
    "s_add_u32 m0, s64, 5120\n" \
    "s_add_u32 s97, s65, 868352\n" \
    "buffer_load_dwordx4 %[vow], %[dw], s97 offen lds\n" \
    "s_add_u32 m0, s64, 6144\n" \
    "s_add_u32 s97, s65, 884736\n" \
    "buffer_load_dwordx4 %[vow], %[dw], s97 offen lds\n" \
    "s_add_u32 m0, s64, 7168\n" \
    "s_add_u32 s97, s65, 901120\n" \
    "buffer_load_dwordx4 %[vow], %[dw], s97 offen lds\n" \
    "s_waitcnt vmcnt(16)\n" \
    "ds_read_b64_tr_b16 a[128:129], v88 offset:8192\n" \
    "ds_read_b64_tr_b16 a[130:131], v88 offset:10240\n" \
    "ds_read_b64_tr_b16 a[132:133], v89 offset:8192\n" \
    "ds_read_b64_tr_b16 a[134:135], v89 offset:10240\n" \
    "ds_read_b64_tr_b16 a[136:137], v90 offset:8192\n" \
    "ds_read_b64_tr_b16 a[138:139], v90 offset:10240\n" \
    "ds_read_b64_tr_b16 a[140:141], v91 offset:8192\n" \
    "ds_read_b64_tr_b16 a[142:143], v91 offset:10240\n" \
    "ds_read_b64_tr_b16 a[144:145], v88 offset:12288\n" \
    "ds_read_b64_tr_b16 a[146:147], v88 offset:14336\n" \
    "ds_read_b64_tr_b16 a[148:149], v89 offset:12288\n" \
    "ds_read_b64_tr_b16 a[150:151], v89 offset:14336\n" \
    "ds_read_b64_tr_b16 a[152:153], v90 offset:12288\n" \
    "ds_read_b64_tr_b16 a[154:155], v90 offset:14336\n" \
    "ds_read_b64_tr_b16 a[156:157], v91 offset:12288\n" \
    "ds_read_b64_tr_b16 a[158:159], v91 offset:14336\n" \
    "s_waitcnt lgkmcnt(0)\n" \
    "s_add_u32 m0, s64, 8192\n" \
    "s_add_u32 s97, s65, 917504\n" \
    "buffer_load_dwordx4 %[vow], %[dw], s97 offen lds\n" \
    "s_add_u32 m0, s64, 9216\n" \
    "s_add_u32 s97, s65, 933888\n" \
    "buffer_load_dwordx4 %[vow], %[dw], s97 offen lds\n" \
    "s_add_u32 m0, s64, 10240\n" \
    "s_add_u32 s97, s65, 950272\n" \
    "buffer_load_dwordx4 %[vow], %[dw], s97 offen lds\n" \
    "s_add_u32 m0, s64, 11264\n" \
    "s_add_u32 s97, s65, 966656\n" \
    "buffer_load_dwordx4 %[vow], %[dw], s97 offen lds\n" \
    "s_add_u32 m0, s64, 12288\n" \
    "s_add_u32 s97, s65, 983040\n" \
    "buffer_load_dwordx4 %[vow], %[dw], s97 offen lds\n" \
    "s_add_u32 m0, s64, 13312\n" \
    "s_add_u32 s97, s65, 999424\n" \
    "buffer_load_dwordx4 %[vow], %[dw], s97 offen lds\n" \
    "s_add_u32 m0, s64, 14336\n" \
    "s_add_u32 s97, s65, 1015808\n" \
    "buffer_load_dwordx4 %[vow], %[dw], s97 offen lds\n" \
    "s_add_u32 m0, s64, 15360\n" \
    "s_add_u32 s97, s65, 1032192\n" \
    "buffer_load_dwordx4 %[vow], %[dw], s97 offen lds\n" \
    "s_waitcnt vmcnt(16)\n" \
    "s_cmp_ge_i32 %[pre], 0\n" \
    "s_cselect_b32 %[cfg], %[pre], %[cfg]\n" \
    "s_cbranch_scc1 LC_hsok_%=\n" \
    "LC_hs_%=:\n" \
    "s_add_u32 s97, %[hsa], 32768\n" \
    "buffer_load_dword v31, off, %[ws], s97 sc1\n" \
    "s_waitcnt vmcnt(0)\n" \
    "v_readfirstlane_b32 s97, v31\n" \
    "s_nop 1\n" \
    "s_xor_b32 s97, s97, %[cfg]\n" \
    "s_cmp_gt_u32 s97, 255\n" \
    "s_cbranch_scc1 LC_hsr_%=\n" \
    "s_and_b32 s96, s97, 3\n" \
    "s_cmp_lg_u32 s96, %[est]\n" \
    "s_cbranch_scc1 LC_redo_%=\n" \
    "s_bitcmp0_b32 s97, 3\n" \
    "s_cbranch_scc1 LC_hsr_%=\n" \
    "s_mov_b32 %[cfg], s97\n" \
    "LC_hsok_%=:\n" \
    "s_and_b32 %[pol], %[cfg], 4\n" \
    "s_lshl_b32 %[pol], %[pol], 11\n" \
    "s_xor_b32 %[npol], %[pol], 8192\n" \
    "s_waitcnt vmcnt(0)\n" \
    "v_mov_b32 v31, 0\n" \
    "s_add_u32 s97, %[hsa], 69632\n" \
    "buffer_store_dword v31, off, %[ws], s97 sc1\n" \
    "ds_read_b64_tr_b16 a[160:161], v88 offset:16384\n" \
    "ds_read_b64_tr_b16 a[162:163], v88 offset:18432\n" \
    "ds_read_b64_tr_b16 a[164:165], v89 offset:16384\n" \
    "ds_read_b64_tr_b16 a[166:167], v89 offset:18432\n" \
    "ds_read_b64_tr_b16 a[168:169], v90 offset:16384\n" \
    "ds_read_b64_tr_b16 a[170:171], v90 offset:18432\n" \
    "ds_read_b64_tr_b16 a[172:173], v91 offset:16384\n" \
    "ds_read_b64_tr_b16 a[174:175], v91 offset:18432\n" \
    "ds_read_b64_tr_b16 a[176:177], v88 offset:20480\n" \
    "ds_read_b64_tr_b16 a[178:179], v88 offset:22528\n" \
    "ds_read_b64_tr_b16 a[180:181], v89 offset:20480\n" \
    "ds_read_b64_tr_b16 a[182:183], v89 offset:22528\n" \
    "ds_read_b64_tr_b16 a[184:185], v90 offset:20480\n" \
    "ds_read_b64_tr_b16 a[186:187], v90 offset:22528\n" \
    "ds_read_b64_tr_b16 a[188:189], v91 offset:20480\n" \
    "ds_read_b64_tr_b16 a[190:191], v91 offset:22528\n" \
    "s_waitcnt lgkmcnt(0)\n" \
    "s_add_u32 m0, s64, 16384\n" \
    "s_add_u32 s97, s65, 1048576\n" \
    "buffer_load_dwordx4 %[vow], %[dw], s97 offen lds\n" \
    "s_add_u32 m0, s64, 17408\n" \
    "s_add_u32 s97, s65, 1064960\n" \
    "buffer_load_dwordx4 %[vow], %[dw], s97 offen lds\n" \
    "s_add_u32 m0, s64, 18432\n" \
    "s_add_u32 s97, s65, 1081344\n" \
    "buffer_load_dwordx4 %[vow], %[dw], s97 offen lds\n" \
    "s_add_u32 m0, s64, 19456\n" \
    "s_add_u32 s97, s65, 1097728\n" \
    "buffer_load_dwordx4 %[vow], %[dw], s97 offen lds\n" \
    "s_add_u32 m0, s64, 20480\n" \
    "s_add_u32 s97, s65, 1114112\n" \
    "buffer_load_dwordx4 %[vow], %[dw], s97 offen lds\n" \
    "s_add_u32 m0, s64, 21504\n" \
    "s_add_u32 s97, s65, 1130496\n" \
    "buffer_load_dwordx4 %[vow], %[dw], s97 offen lds\n" \
    "s_add_u32 m0, s64, 22528\n" \
    "s_add_u32 s97, s65, 1146880\n" \
    "buffer_load_dwordx4 %[vow], %[dw], s97 offen lds\n" \
    "s_add_u32 m0, s64, 23552\n" \
    "s_add_u32 s97, s65, 1163264\n" \
    "buffer_load_dwordx4 %[vow], %[dw], s97 offen lds\n" \
    "s_waitcnt vmcnt(16)\n" \
    "ds_read_b64_tr_b16 a[192:193], v88 offset:0\n" \
    "ds_read_b64_tr_b16 a[194:195], v88 offset:2048\n" \
    "ds_read_b64_tr_b16 a[196:197], v89 offset:0\n" \
    "ds_read_b64_tr_b16 a[198:199], v89 offset:2048\n" \
    "ds_read_b64_tr_b16 a[200:201], v90 offset:0\n" \
    "ds_read_b64_tr_b16 a[202:203], v90 offset:2048\n" \
    "ds_read_b64_tr_b16 a[204:205], v91 offset:0\n" \
    "ds_read_b64_tr_b16 a[206:207], v91 offset:2048\n" \
    "ds_read_b64_tr_b16 a[208:209], v88 offset:4096\n" \
    "ds_read_b64_tr_b16 a[210:211], v88 offset:6144\n" \
    "ds_read_b64_tr_b16 a[212:213], v89 offset:4096\n" \
    "ds_read_b64_tr_b16 a[214:215], v89 offset:6144\n" \
    "ds_read_b64_tr_b16 a[216:217], v90 offset:4096\n" \
    "ds_read_b64_tr_b16 a[218:219], v90 offset:6144\n" \
    "ds_read_b64_tr_b16 a[220:221], v91 offset:4096\n" \
    "ds_read_b64_tr_b16 a[222:223], v91 offset:6144\n" \
    "s_waitcnt lgkmcnt(0)\n" \
    "s_mov_b32 s97, 0\n" \
    "buffer_load_dword v36, %[vxt], %[db], s97 offen\n" \
    "buffer_load_dword v37, %[vxt], %[da], s97 offen\n" \
    "s_add_u32 m0, s64, 0\n" \
    "s_add_u32 s97, s65, 1179648\n" \
    "buffer_load_dwordx4 %[vow], %[dw], s97 offen lds\n" \
    "s_add_u32 m0, s64, 1024\n" \
    "s_add_u32 s97, s65, 1196032\n" \
    "buffer_load_dwordx4 %[vow], %[dw], s97 offen lds\n" \
    "s_add_u32 m0, s64, 2048\n" \
    "s_add_u32 s97, s65, 1212416\n" \
    "buffer_load_dwordx4 %[vow], %[dw], s97 offen lds\n" \
    "s_add_u32 m0, s64, 3072\n" \
    "s_add_u32 s97, s65, 1228800\n" \
    "buffer_load_dwordx4 %[vow], %[dw], s97 offen lds\n" \
    "s_add_u32 m0, s64, 4096\n" \
    "s_add_u32 s97, s65, 1245184\n" \
    "buffer_load_dwordx4 %[vow], %[dw], s97 offen lds\n" \
    "s_add_u32 m0, s64, 5120\n" \
    "s_add_u32 s97, s65, 1261568\n" \
    "buffer_load_dwordx4 %[vow], %[dw], s97 offen lds\n" \
    "s_add_u32 m0, s64, 6144\n" \
    "s_add_u32 s97, s65, 1277952\n" \
    "buffer_load_dwordx4 %[vow], %[dw], s97 offen lds\n" \
    "s_add_u32 m0, s64, 7168\n" \
    "s_add_u32 s97, s65, 1294336\n" \
    "buffer_load_dwordx4 %[vow], %[dw], s97 offen lds\n" \
    "s_waitcnt vmcnt(16)\n" \
    "ds_read_b64_tr_b16 a[224:225], v88 offset:8192\n" \
    "ds_read_b64_tr_b16 a[226:227], v88 offset:10240\n" \
    "ds_read_b64_tr_b16 a[228:229], v89 offset:8192\n" \
    "ds_read_b64_tr_b16 a[230:231], v89 offset:10240\n" \
    "ds_read_b64_tr_b16 a[232:233], v90 offset:8192\n" \
    "ds_read_b64_tr_b16 a[234:235], v90 offset:10240\n" \
    "ds_read_b64_tr_b16 a[236:237], v91 offset:8192\n" \
    "ds_read_b64_tr_b16 a[238:239], v91 offset:10240\n" \
    "ds_read_b64_tr_b16 a[240:241], v88 offset:12288\n" \
    "ds_read_b64_tr_b16 a[242:243], v88 offset:14336\n" \
    "ds_read_b64_tr_b16 a[244:245], v89 offset:12288\n" \
    "ds_read_b64_tr_b16 a[246:247], v89 offset:14336\n" \
    "ds_read_b64_tr_b16 a[248:249], v90 offset:12288\n" \
    "ds_read_b64_tr_b16 a[250:251], v90 offset:14336\n" \
    "ds_read_b64_tr_b16 a[252:253], v91 offset:12288\n" \
    "ds_read_b64_tr_b16 a[254:255], v91 offset:14336\n" \
    "s_waitcnt lgkmcnt(0)\n" \
    "s_mov_b32 s97, 1048576\n" \
    "buffer_load_dword v36, %[vxt], %[db], s97 offen\n" \
    "buffer_load_dword v37, %[vxt], %[da], s97 offen\n" \
    "s_add_u32 m0, s64, 8192\n" \
    "s_add_u32 s97, s65, 1310720\n" \
    "buffer_load_dwordx4 %[vow], %[dw], s97 offen lds\n" \
    "s_add_u32 m0, s64, 9216\n" \
    "s_add_u32 s97, s65, 1327104\n" \
    "buffer_load_dwordx4 %[vow], %[dw], s97 offen lds\n" \
    "s_add_u32 m0, s64, 10240\n" \
    "s_add_u32 s97, s65, 1343488\n" \
    "buffer_load_dwordx4 %[vow], %[dw], s97 offen lds\n" \
    "s_add_u32 m0, s64, 11264\n" \
    "s_add_u32 s97, s65, 1359872\n" \
    "buffer_load_dwordx4 %[vow], %[dw], s97 offen lds\n" \
    "s_add_u32 m0, s64, 12288\n" \
    "s_add_u32 s97, s65, 1376256\n" \
    "buffer_load_dwordx4 %[vow], %[dw], s97 offen lds\n" \
    "s_add_u32 m0, s64, 13312\n" \
    "s_add_u32 s97, s65, 1392640\n" \
    "buffer_load_dwordx4 %[vow], %[dw], s97 offen lds\n" \
    "s_add_u32 m0, s64, 14336\n" \
    "s_add_u32 s97, s65, 1409024\n" \
    "buffer_load_dwordx4 %[vow], %[dw], s97 offen lds\n" \
    "s_add_u32 m0, s64, 15360\n" \
    "s_add_u32 s97, s65, 1425408\n" \
    "buffer_load_dwordx4 %[vow], %[dw], s97 offen lds\n" \
    "s_waitcnt vmcnt(16)\n" \
    "ds_read_b64_tr_b16 v[96:97], v88 offset:16384\n" \
    "ds_read_b64_tr_b16 v[98:99], v88 offset:18432\n" \
    "ds_read_b64_tr_b16 v[100:101], v89 offset:16384\n" \
    "ds_read_b64_tr_b16 v[102:103], v89 offset:18432\n" \
    "ds_read_b64_tr_b16 v[104:105], v90 offset:16384\n" \
    "ds_read_b64_tr_b16 v[106:107], v90 offset:18432\n" \
    "ds_read_b64_tr_b16 v[108:109], v91 offset:16384\n" \
    "ds_read_b64_tr_b16 v[110:111], v91 offset:18432\n" \
    "ds_read_b64_tr_b16 v[112:113], v88 offset:20480\n" \
    "ds_read_b64_tr_b16 v[114:115], v88 offset:22528\n" \
    "ds_read_b64_tr_b16 v[116:117], v89 offset:20480\n" \
    "ds_read_b64_tr_b16 v[118:119], v89 offset:22528\n" \
    "ds_read_b64_tr_b16 v[120:121], v90 offset:20480\n" \
    "ds_read_b64_tr_b16 v[122:123], v90 offset:22528\n" \
    "ds_read_b64_tr_b16 v[124:125], v91 offset:20480\n" \
    "ds_read_b64_tr_b16 v[126:127], v91 offset:22528\n" \
    "s_waitcnt lgkmcnt(0)\n" \
    "s_mov_b32 s97, 2097152\n" \
    "buffer_load_dword v36, %[vxt], %[db], s97 offen\n" \
    "buffer_load_dword v37, %[vxt], %[da], s97 offen\n" \
    "s_add_u32 m0, s64, 16384\n" \
    "s_add_u32 s97, s65, 1441792\n" \
    "buffer_load_dwordx4 %[vow], %[dw], s97 offen lds\n" \
    "s_add_u32 m0, s64, 17408\n" \
    "s_add_u32 s97, s65, 1458176\n" \
    "buffer_load_dwordx4 %[vow], %[dw], s97 offen lds\n" \
    "s_add_u32 m0, s64, 18432\n" \
    "s_add_u32 s97, s65, 1474560\n" \
    "buffer_load_dwordx4 %[vow], %[dw], s97 offen lds\n" \
    "s_add_u32 m0, s64, 19456\n" \
    "s_add_u32 s97, s65, 1490944\n" \
    "buffer_load_dwordx4 %[vow], %[dw], s97 offen lds\n" \
    "s_add_u32 m0, s64, 20480\n" \
    "s_add_u32 s97, s65, 1507328\n" \
    "buffer_load_dwordx4 %[vow], %[dw], s97 offen lds\n" \
    "s_add_u32 m0, s64, 21504\n" \
    "s_add_u32 s97, s65, 1523712\n" \
    "buffer_load_dwordx4 %[vow], %[dw], s97 offen lds\n" \
    "s_add_u32 m0, s64, 22528\n" \
    "s_add_u32 s97, s65, 1540096\n" \
    "buffer_load_dwordx4 %[vow], %[dw], s97 offen lds\n" \
    "s_add_u32 m0, s64, 23552\n" \
    "s_add_u32 s97, s65, 1556480\n" \
    "buffer_load_dwordx4 %[vow], %[dw], s97 offen lds\n" \
    "s_waitcnt vmcnt(16)\n" \
    "ds_read_b64_tr_b16 v[128:129], v88 offset:0\n" \
    "ds_read_b64_tr_b16 v[130:131], v88 offset:2048\n" \
    "ds_read_b64_tr_b16 v[132:133], v89 offset:0\n" \
    "ds_read_b64_tr_b16 v[134:135], v89 offset:2048\n" \
    "ds_read_b64_tr_b16 v[136:137], v90 offset:0\n" \
    "ds_read_b64_tr_b16 v[138:139], v90 offset:2048\n" \
    "ds_read_b64_tr_b16 v[140:141], v91 offset:0\n" \
    "ds_read_b64_tr_b16 v[142:143], v91 offset:2048\n" \
    "ds_read_b64_tr_b16 v[160:161], v88 offset:4096\n" \
    "ds_read_b64_tr_b16 v[162:163], v88 offset:6144\n" \
    "ds_read_b64_tr_b16 v[164:165], v89 offset:4096\n" \
    "ds_read_b64_tr_b16 v[166:167], v89 offset:6144\n" \
    "ds_read_b64_tr_b16 v[168:169], v90 offset:4096\n" \
    "ds_read_b64_tr_b16 v[170:171], v90 offset:6144\n" \
    "ds_read_b64_tr_b16 v[172:173], v91 offset:4096\n" \
    "ds_read_b64_tr_b16 v[174:175], v91 offset:6144\n" \
    "s_waitcnt lgkmcnt(0)\n" \
    "s_mov_b32 s97, 3145728\n" \
    "buffer_load_dword v36, %[vxt], %[db], s97 offen\n" \
    "buffer_load_dword v37, %[vxt], %[da], s97 offen\n" \
    "s_add_u32 m0, s64, 0\n" \
    "s_add_u32 s97, s65, 1572864\n" \
    "buffer_load_dwordx4 %[vow], %[dw], s97 offen lds\n" \
    "s_add_u32 m0, s64, 1024\n" \
    "s_add_u32 s97, s65, 1589248\n" \
    "buffer_load_dwordx4 %[vow], %[dw], s97 offen lds\n" \
    "s_add_u32 m0, s64, 2048\n" \
    "s_add_u32 s97, s65, 1605632\n" \
    "buffer_load_dwordx4 %[vow], %[dw], s97 offen lds\n" \
    "s_add_u32 m0, s64, 3072\n" \
    "s_add_u32 s97, s65, 1622016\n" \
    "buffer_load_dwordx4 %[vow], %[dw], s97 offen lds\n" \
    "s_add_u32 m0, s64, 4096\n" \
    "s_add_u32 s97, s65, 1638400\n" \
    "buffer_load_dwordx4 %[vow], %[dw], s97 offen lds\n" \
    "s_add_u32 m0, s64, 5120\n" \
    "s_add_u32 s97, s65, 1654784\n" \
    "buffer_load_dwordx4 %[vow], %[dw], s97 offen lds\n" \
    "s_add_u32 m0, s64, 6144\n" \
    "s_add_u32 s97, s65, 1671168\n" \
    "buffer_load_dwordx4 %[vow], %[dw], s97 offen lds\n" \
    "s_add_u32 m0, s64, 7168\n" \
    "s_add_u32 s97, s65, 1687552\n" \
    "buffer_load_dwordx4 %[vow], %[dw], s97 offen lds\n" \
    "s_waitcnt vmcnt(16)\n" \
    "ds_read_b64_tr_b16 v[176:177], v88 offset:8192\n" \
    "ds_read_b64_tr_b16 v[178:179], v88 offset:10240\n" \
    "ds_read_b64_tr_b16 v[180:181], v89 offset:8192\n" \
    "ds_read_b64_tr_b16 v[182:183], v89 offset:10240\n" \
    "ds_read_b64_tr_b16 v[184:185], v90 offset:8192\n" \
    "ds_read_b64_tr_b16 v[186:187], v90 offset:10240\n" \
    "ds_read_b64_tr_b16 v[188:189], v91 offset:8192\n" \
    "ds_read_b64_tr_b16 v[190:191], v91 offset:10240\n" \
    "ds_read_b64_tr_b16 v[192:193], v88 offset:12288\n" \
    "ds_read_b64_tr_b16 v[194:195], v88 offset:14336\n" \
    "ds_read_b64_tr_b16 v[196:197], v89 offset:12288\n" \
    "ds_read_b64_tr_b16 v[198:199], v89 offset:14336\n" \
    "ds_read_b64_tr_b16 v[200:201], v90 offset:12288\n" \
    "ds_read_b64_tr_b16 v[202:203], v90 offset:14336\n" \
    "ds_read_b64_tr_b16 v[204:205], v91 offset:12288\n" \
    "ds_read_b64_tr_b16 v[206:207], v91 offset:14336\n" \
    "s_waitcnt lgkmcnt(0)\n" \
    "s_add_u32 m0, s64, 8192\n" \
    "s_add_u32 s97, s65, 1703936\n" \
    "buffer_load_dwordx4 %[vow], %[dw], s97 offen lds\n" \
    "s_add_u32 m0, s64, 9216\n" \
    "s_add_u32 s97, s65, 1720320\n" \
    "buffer_load_dwordx4 %[vow], %[dw], s97 offen lds\n" \
    "s_add_u32 m0, s64, 10240\n" \
    "s_add_u32 s97, s65, 1736704\n" \
    "buffer_load_dwordx4 %[vow], %[dw], s97 offen lds\n" \
    "s_add_u32 m0, s64, 11264\n" \
    "s_add_u32 s97, s65, 1753088\n" \
    "buffer_load_dwordx4 %[vow], %[dw], s97 offen lds\n" \
    "s_add_u32 m0, s64, 12288\n" \
    "s_add_u32 s97, s65, 1769472\n" \
    "buffer_load_dwordx4 %[vow], %[dw], s97 offen lds\n" \
    "s_add_u32 m0, s64, 13312\n" \
    "s_add_u32 s97, s65, 1785856\n" \
    "buffer_load_dwordx4 %[vow], %[dw], s97 offen lds\n" \
    "s_add_u32 m0, s64, 14336\n" \
    "s_add_u32 s97, s65, 1802240\n" \
    "buffer_load_dwordx4 %[vow], %[dw], s97 offen lds\n" \
    "s_add_u32 m0, s64, 15360\n" \
    "s_add_u32 s97, s65, 1818624\n" \
    "buffer_load_dwordx4 %[vow], %[dw], s97 offen lds\n" \
    "s_waitcnt vmcnt(16)\n" \
    "ds_read_b64_tr_b16 v[208:209], v88 offset:16384\n" \
    "ds_read_b64_tr_b16 v[210:211], v88 offset:18432\n" \
    "ds_read_b64_tr_b16 v[212:213], v89 offset:16384\n" \
    "ds_read_b64_tr_b16 v[214:215], v89 offset:18432\n" \
    "ds_read_b64_tr_b16 v[216:217], v90 offset:16384\n" \
    "ds_read_b64_tr_b16 v[218:219], v90 offset:18432\n" \
    "ds_read_b64_tr_b16 v[220:221], v91 offset:16384\n" \
    "ds_read_b64_tr_b16 v[222:223], v91 offset:18432\n" \
    "ds_read_b64_tr_b16 v[224:225], v88 offset:20480\n" \
    "ds_read_b64_tr_b16 v[226:227], v88 offset:22528\n" \
    "ds_read_b64_tr_b16 v[228:229], v89 offset:20480\n" \
    "ds_read_b64_tr_b16 v[230:231], v89 offset:22528\n" \
    "ds_read_b64_tr_b16 v[232:233], v90 offset:20480\n" \
    "ds_read_b64_tr_b16 v[234:235], v90 offset:22528\n" \
    "ds_read_b64_tr_b16 v[236:237], v91 offset:20480\n" \
    "ds_read_b64_tr_b16 v[238:239], v91 offset:22528\n" \
    "s_waitcnt lgkmcnt(0)\n" \
    "s_add_u32 m0, s64, 16384\n" \
    "s_add_u32 s97, s65, 1835008\n" \
    "buffer_load_dwordx4 %[vow], %[dw], s97 offen lds\n" \
    "s_add_u32 m0, s64, 17408\n" \
    "s_add_u32 s97, s65, 1851392\n" \
    "buffer_load_dwordx4 %[vow], %[dw], s97 offen lds\n" \
    "s_add_u32 m0, s64, 18432\n" \
    "s_add_u32 s97, s65, 1867776\n" \
    "buffer_load_dwordx4 %[vow], %[dw], s97 offen lds\n" \
    "s_add_u32 m0, s64, 19456\n" \
    "s_add_u32 s97, s65, 1884160\n" \
    "buffer_load_dwordx4 %[vow], %[dw], s97 offen lds\n" \
    "s_add_u32 m0, s64, 20480\n" \
    "s_add_u32 s97, s65, 1900544\n" \
    "buffer_load_dwordx4 %[vow], %[dw], s97 offen lds\n" \
    "s_add_u32 m0, s64, 21504\n" \
    "s_add_u32 s97, s65, 1916928\n" \
    "buffer_load_dwordx4 %[vow], %[dw], s97 offen lds\n" \
    "s_add_u32 m0, s64, 22528\n" \
    "s_add_u32 s97, s65, 1933312\n" \
    "buffer_load_dwordx4 %[vow], %[dw], s97 offen lds\n" \
    "s_add_u32 m0, s64, 23552\n" \
    "s_add_u32 s97, s65, 1949696\n" \
    "buffer_load_dwordx4 %[vow], %[dw], s97 offen lds\n" \
    "s_waitcnt vmcnt(16)\n" \
    "ds_read_b64_tr_b16 v[240:241], v88 offset:0\n" \
    "ds_read_b64_tr_b16 v[242:243], v88 offset:2048\n" \
    "ds_read_b64_tr_b16 v[244:245], v89 offset:0\n" \
    "ds_read_b64_tr_b16 v[246:247], v89 offset:2048\n" \
    "ds_read_b64_tr_b16 v[248:249], v90 offset:0\n" \
    "ds_read_b64_tr_b16 v[250:251], v90 offset:2048\n" \
    "ds_read_b64_tr_b16 v[252:253], v91 offset:0\n" \
    "ds_read_b64_tr_b16 v[254:255], v91 offset:2048\n" \
    "ds_read_b64_tr_b16 v[144:145], v88 offset:4096\n" \
    "ds_read_b64_tr_b16 v[146:147], v88 offset:6144\n" \
    "ds_read_b64_tr_b16 v[148:149], v89 offset:4096\n" \
    "ds_read_b64_tr_b16 v[150:151], v89 offset:6144\n" \
    "ds_read_b64_tr_b16 v[152:153], v90 offset:4096\n" \
    "ds_read_b64_tr_b16 v[154:155], v90 offset:6144\n" \
    "ds_read_b64_tr_b16 v[156:157], v91 offset:4096\n" \
    "ds_read_b64_tr_b16 v[158:159], v91 offset:6144\n" \
    "s_waitcnt lgkmcnt(0)\n" \
    "s_add_u32 m0, s64, 0\n" \
    "s_add_u32 s97, s65, 1966080\n" \
    "buffer_load_dwordx4 %[vow], %[dw], s97 offen lds\n" \
    "s_add_u32 m0, s64, 1024\n" \
    "s_add_u32 s97, s65, 1982464\n" \
    "buffer_load_dwordx4 %[vow], %[dw], s97 offen lds\n" \
    "s_add_u32 m0, s64, 2048\n" \
    "s_add_u32 s97, s65, 1998848\n" \
    "buffer_load_dwordx4 %[vow], %[dw], s97 offen lds\n" \
    "s_add_u32 m0, s64, 3072\n" \
    "s_add_u32 s97, s65, 2015232\n" \
    "buffer_load_dwordx4 %[vow], %[dw], s97 offen lds\n" \
    "s_add_u32 m0, s64, 4096\n" \
    "s_add_u32 s97, s65, 2031616\n" \
    "buffer_load_dwordx4 %[vow], %[dw], s97 offen lds\n" \
    "s_add_u32 m0, s64, 5120\n" \
    "s_add_u32 s97, s65, 2048000\n" \
    "buffer_load_dwordx4 %[vow], %[dw], s97 offen lds\n" \
    "s_add_u32 m0, s64, 6144\n" \
    "s_add_u32 s97, s65, 2064384\n" \
    "buffer_load_dwordx4 %[vow], %[dw], s97 offen lds\n" \
    "s_add_u32 m0, s64, 7168\n" \
    "s_add_u32 s97, s65, 2080768\n" \
    "buffer_load_dwordx4 %[vow], %[dw], s97 offen lds\n" \
    "ds_write_b128 %[w25], v[144:147] offset:0\n" \
    "ds_write_b128 %[w25], v[148:151] offset:1024\n" \
    "ds_write_b128 %[w25], v[152:155] offset:32768\n" \
    "ds_write_b128 %[w25], v[156:159] offset:33792\n" \
    "s_waitcnt vmcnt(0) lgkmcnt(0)\n" \
    "s_add_u32 s97, %[w], 0\n" \
    "s_and_b32 s97, s97, 3\n" \
    "s_lshl_b32 s97, s97, 10\n" \
    "v_add_u32 v32, s97, %[vo]\n" \
    "s_add_u32 s97, %[w], 1\n" \
    "s_and_b32 s97, s97, 3\n" \
    "s_lshl_b32 s97, s97, 10\n" \
    "v_add_u32 v33, s97, %[vo]\n" \
    "s_add_u32 s97, %[w], 2\n" \
    "s_and_b32 s97, s97, 3\n" \
    "s_lshl_b32 s97, s97, 10\n" \
    "v_add_u32 v34, s97, %[vo]\n" \
    "s_add_u32 s97, %[w], 3\n" \
    "s_and_b32 s97, s97, 3\n" \
    "s_lshl_b32 s97, s97, 10\n" \
    "v_add_u32 v35, s97, %[vo]\n" \
    "v_mov_b32 v86, %[wp]\n" \
    "v_mov_b32 v87, %[rp]\n" \
    "v_mov_b32 v84, -1\n" \
    "s_mov_b32 s63, 0\n" \
    "s_nop 0\n" \
    "v_cmp_gt_i32 vcc, s63, v84\n" \
    "s_mul_i32 s61, s63, 0x202000\n" \
    "s_and_b32 s61, s61, 0xffe02000\n" \
    "s_xor_b32 s61, s61, %[pol]\n" \
    "s_cbranch_vccnz LC_c0r_%=\n" \
    "LC_c0_%=:\n" \
    "s_add_u32 s97, s61, 0\n" \
    "buffer_load_dwordx4 v[36:39], v32, %[ws], s97 offen sc1 ;F0\n" \
    "s_add_u32 s97, s61, 0\n" \
    "buffer_load_dwordx4 v[40:43], v33, %[ws], s97 offen sc1 ;F1\n" \
    "s_add_u32 s97, s61, 0\n" \
    "buffer_load_dwordx4 v[44:47], v34, %[ws], s97 offen sc1 ;F2\n" \
    "s_add_u32 s97, s61, 0\n" \
    "buffer_load_dwordx4 v[48:51], v35, %[ws], s97 offen sc1 ;F3\n" \
    "s_add_u32 s97, s61, 4096\n" \
    "buffer_load_dwordx4 v[52:55], v32, %[ws], s97 offen sc1 ;F4\n" \
    "s_add_u32 s97, s61, 4096\n" \
    "buffer_load_dwordx4 v[56:59], v33, %[ws], s97 offen sc1 ;F5\n" \
    "s_add_u32 s97, s61, 4096\n" \
    "buffer_load_dwordx4 v[60:63], v34, %[ws], s97 offen sc1 ;F6\n" \
    "s_add_u32 s97, s61, 4096\n" \
    "buffer_load_dwordx4 v[64:67], v35, %[ws], s97 offen sc1 ;F7\n" \
    "s_mov_b32 s95, s63\n" \
    "s_mov_b32 s60, s61\n" \
    "s_waitcnt vmcnt(0)\n" \
    "s_branch LC_first_%=\n" \
    "LC_trip_%=:\n" \
    "s_waitcnt vmcnt(8)\n" \
    "v_mfma_f32_16x16x32_bf16 v[68:71], a[0:3], v[36:39], 0\n" \
    "v_mfma_f32_16x16x32_bf16 v[72:75], a[4:7], v[36:39], 0\n" \
    "v_mfma_f32_16x16x32_bf16 v[76:79], a[8:11], v[36:39], 0\n" \
    "v_mfma_f32_16x16x32_bf16 v[80:83], a[12:15], v[36:39], 0\n" \
    "s_add_u32 s97, s60, 16384\n" \
    "buffer_load_dwordx4 v[36:39], v32, %[ws], s97 offen sc1\n" \
    "s_waitcnt vmcnt(8)\n" \
    "v_mfma_f32_16x16x32_bf16 v[68:71], a[16:19], v[40:43], v[68:71]\n" \
    "s_waitcnt lgkmcnt(0)\n" \
    "s_barrier\n" \
    "v_mfma_f32_16x16x32_bf16 v[72:75], a[20:23], v[40:43], v[72:75]\n" \
    "ds_read_b128 v[144:147], v87 offset:0\n" \
    "ds_read_b128 v[148:151], v87 offset:4352\n" \
    "v_mfma_f32_16x16x32_bf16 v[76:79], a[24:27], v[40:43], v[76:79]\n" \
    "ds_read_b128 v[152:155], v87 offset:8704\n" \
    "ds_read_b128 v[156:159], v87 offset:13056\n" \
    "v_mfma_f32_16x16x32_bf16 v[80:83], a[28:31], v[40:43], v[80:83]\n" \
    "s_add_u32 s97, s60, 16384\n" \
    "buffer_load_dwordx4 v[40:43], v33, %[ws], s97 offen sc1\n" \
    "s_waitcnt vmcnt(8)\n" \
    "v_mfma_f32_16x16x32_bf16 v[68:71], a[32:35], v[44:47], v[68:71]\n" \
    "v_xor_b32 v87, 32768, v87\n" \
    "s_nop 0\n" \
    "v_mfma_f32_16x16x32_bf16 v[72:75], a[36:39], v[44:47], v[72:75]\n" \
    "s_nop 0\n" \
    "s_nop 0\n" \
    "v_mfma_f32_16x16x32_bf16 v[76:79], a[40:43], v[44:47], v[76:79]\n" \
    "s_nop 0\n" \
    "s_nop 0\n" \
    "v_mfma_f32_16x16x32_bf16 v[80:83], a[44:47], v[44:47], v[80:83]\n" \
    "s_add_u32 s97, s60, 16384\n" \
    "buffer_load_dwordx4 v[44:47], v34, %[ws], s97 offen sc1\n" \
    "s_waitcnt vmcnt(8)\n" \
    "v_mfma_f32_16x16x32_bf16 v[68:71], a[48:51], v[48:51], v[68:71]\n" \
    "s_waitcnt lgkmcnt(0)\n" \
    "v_pk_add_f32 v[144:145], v[144:145], v[148:149]\n" \
    "v_mfma_f32_16x16x32_bf16 v[72:75], a[52:55], v[48:51], v[72:75]\n" \
    "v_pk_add_f32 v[146:147], v[146:147], v[150:151]\n" \
    "v_pk_add_f32 v[152:153], v[152:153], v[156:157]\n" \
    "v_mfma_f32_16x16x32_bf16 v[76:79], a[56:59], v[48:51], v[76:79]\n" \
    "v_pk_add_f32 v[154:155], v[154:155], v[158:159]\n" \
    "v_pk_add_f32 v[144:145], v[144:145], v[152:153]\n" \
    "v_mfma_f32_16x16x32_bf16 v[80:83], a[60:63], v[48:51], v[80:83]\n" \
    "s_add_u32 s97, s60, 16384\n" \
    "buffer_load_dwordx4 v[48:51], v35, %[ws], s97 offen sc1\n" \
    "s_waitcnt vmcnt(8)\n" \
    "v_mfma_f32_16x16x32_bf16 v[68:71], a[64:67], v[52:55], v[68:71]\n" \
    "v_pk_add_f32 v[146:147], v[146:147], v[154:155]\n" \
    "buffer_store_dwordx4 v[144:147], %[vout], %[od], s62 offen sc1\n" \
    "v_mfma_f32_16x16x32_bf16 v[72:75], a[68:71], v[52:55], v[72:75]\n" \
    "v_mfma_f32_16x16x32_bf16 v[76:79], a[72:75], v[52:55], v[76:79]\n" \
    "v_mfma_f32_16x16x32_bf16 v[80:83], a[76:79], v[52:55], v[80:83]\n" \
    "s_add_u32 s97, s60, 20480\n" \
    "buffer_load_dwordx4 v[52:55], v32, %[ws], s97 offen sc1\n" \
    "s_waitcnt vmcnt(9)\n" \
    "v_mfma_f32_16x16x32_bf16 v[68:71], a[80:83], v[56:59], v[68:71]\n" \
    "v_mfma_f32_16x16x32_bf16 v[72:75], a[84:87], v[56:59], v[72:75]\n" \
    "v_mfma_f32_16x16x32_bf16 v[76:79], a[88:91], v[56:59], v[76:79]\n" \
    "v_mfma_f32_16x16x32_bf16 v[80:83], a[92:95], v[56:59], v[80:83]\n" \
    "s_add_u32 s97, s60, 20480\n" \
    "buffer_load_dwordx4 v[56:59], v33, %[ws], s97 offen sc1\n" \
    "s_waitcnt vmcnt(8)\n" \
    "v_mfma_f32_16x16x32_bf16 v[68:71], a[96:99], v[60:63], v[68:71]\n" \
    "v_mfma_f32_16x16x32_bf16 v[72:75], a[100:103], v[60:63], v[72:75]\n" \
    "v_mfma_f32_16x16x32_bf16 v[76:79], a[104:107], v[60:63], v[76:79]\n" \
    "v_mfma_f32_16x16x32_bf16 v[80:83], a[108:111], v[60:63], v[80:83]\n" \
    "s_add_u32 s97, s60, 20480\n" \
    "buffer_load_dwordx4 v[60:63], v34, %[ws], s97 offen sc1\n" \
    "s_waitcnt vmcnt(8)\n" \
    "v_mfma_f32_16x16x32_bf16 v[68:71], a[112:115], v[64:67], v[68:71]\n" \
    "v_mfma_f32_16x16x32_bf16 v[72:75], a[116:119], v[64:67], v[72:75]\n" \
    "v_mfma_f32_16x16x32_bf16 v[76:79], a[120:123], v[64:67], v[76:79]\n" \
    "v_mfma_f32_16x16x32_bf16 v[80:83], a[124:127], v[64:67], v[80:83]\n" \
    "s_add_u32 s97, s60, 20480\n" \
    "buffer_load_dwordx4 v[64:67], v35, %[ws], s97 offen sc1\n" \
    "LC_join_%=:\n" \
    "s_waitcnt vmcnt(8)\n" \
    "v_mfma_f32_16x16x32_bf16 v[68:71], a[128:131], v[36:39], v[68:71]\n" \
    "v_mfma_f32_16x16x32_bf16 v[72:75], a[132:135], v[36:39], v[72:75]\n" \
    "v_mfma_f32_16x16x32_bf16 v[76:79], a[136:139], v[36:39], v[76:79]\n" \
    "v_mfma_f32_16x16x32_bf16 v[80:83], a[140:143], v[36:39], v[80:83]\n" \
    "s_add_u32 s97, s60, 32768\n" \
    "buffer_load_dwordx4 v[36:39], v32, %[ws], s97 offen sc1\n" \
    "s_waitcnt vmcnt(8)\n" \
    "v_mfma_f32_16x16x32_bf16 v[68:71], a[144:147], v[40:43], v[68:71]\n" \
    "s_add_u32 s63, s95, 1\n" \
    "v_mfma_f32_16x16x32_bf16 v[72:75], a[148:151], v[40:43], v[72:75]\n" \
    "s_cmp_lt_u32 s63, 64\n" \
    "v_mfma_f32_16x16x32_bf16 v[76:79], a[152:155], v[40:43], v[76:79]\n" \
    "s_cselect_b32 s63, s63, s95\n" \
    "v_mfma_f32_16x16x32_bf16 v[80:83], a[156:159], v[40:43], v[80:83]\n" \
    "s_add_u32 s97, s60, 32768\n" \
    "buffer_load_dwordx4 v[40:43], v33, %[ws], s97 offen sc1\n" \
    "s_waitcnt vmcnt(8)\n" \
    "v_mfma_f32_16x16x32_bf16 v[68:71], a[160:163], v[44:47], v[68:71]\n" \
    "v_mfma_f32_16x16x32_bf16 v[72:75], a[164:167], v[44:47], v[72:75]\n" \
    "v_mfma_f32_16x16x32_bf16 v[76:79], a[168:171], v[44:47], v[76:79]\n" \
    "v_mfma_f32_16x16x32_bf16 v[80:83], a[172:175], v[44:47], v[80:83]\n" \
    "s_add_u32 s97, s60, 32768\n" \
    "buffer_load_dwordx4 v[44:47], v34, %[ws], s97 offen sc1\n" \
    "s_waitcnt vmcnt(8)\n" \
    "v_mfma_f32_16x16x32_bf16 v[68:71], a[176:179], v[48:51], v[68:71]\n" \
    "v_mfma_f32_16x16x32_bf16 v[72:75], a[180:183], v[48:51], v[72:75]\n" \
    "v_mfma_f32_16x16x32_bf16 v[76:79], a[184:187], v[48:51], v[76:79]\n" \
    "v_mfma_f32_16x16x32_bf16 v[80:83], a[188:191], v[48:51], v[80:83]\n" \
    "s_add_u32 s97, s60, 32768\n" \
    "buffer_load_dwordx4 v[48:51], v35, %[ws], s97 offen sc1\n" \
    "s_waitcnt vmcnt(7)\n" \
    "v_mfma_f32_16x16x32_bf16 v[68:71], a[192:195], v[52:55], v[68:71]\n" \
    "v_mfma_f32_16x16x32_bf16 v[72:75], a[196:199], v[52:55], v[72:75]\n" \
    "v_mfma_f32_16x16x32_bf16 v[76:79], a[200:203], v[52:55], v[76:79]\n" \
    "v_mfma_f32_16x16x32_bf16 v[80:83], a[204:207], v[52:55], v[80:83]\n" \
    "s_add_u32 s97, s60, 36864\n" \
    "buffer_load_dwordx4 v[52:55], v32, %[ws], s97 offen sc1\n" \
    "s_waitcnt vmcnt(7)\n" \
    "v_mfma_f32_16x16x32_bf16 v[68:71], a[208:211], v[56:59], v[68:71]\n" \
    "v_mfma_f32_16x16x32_bf16 v[72:75], a[212:215], v[56:59], v[72:75]\n" \
    "v_mfma_f32_16x16x32_bf16 v[76:79], a[216:219], v[56:59], v[76:79]\n" \
    "v_mfma_f32_16x16x32_bf16 v[80:83], a[220:223], v[56:59], v[80:83]\n" \
    "s_add_u32 s97, s60, 36864\n" \
    "buffer_load_dwordx4 v[56:59], v33, %[ws], s97 offen sc1\n" \
    "s_waitcnt vmcnt(7)\n" \
    "v_mfma_f32_16x16x32_bf16 v[68:71], a[224:227], v[60:63], v[68:71]\n" \
    "v_mfma_f32_16x16x32_bf16 v[72:75], a[228:231], v[60:63], v[72:75]\n" \
    "v_mfma_f32_16x16x32_bf16 v[76:79], a[232:235], v[60:63], v[76:79]\n" \
    "v_mfma_f32_16x16x32_bf16 v[80:83], a[236:239], v[60:63], v[80:83]\n" \
    "s_add_u32 s97, s60, 36864\n" \
    "buffer_load_dwordx4 v[60:63], v34, %[ws], s97 offen sc1\n" \
    "s_waitcnt vmcnt(7)\n" \
    "v_mfma_f32_16x16x32_bf16 v[68:71], a[240:243], v[64:67], v[68:71]\n" \
    "v_mfma_f32_16x16x32_bf16 v[72:75], a[244:247], v[64:67], v[72:75]\n" \
    "v_mfma_f32_16x16x32_bf16 v[76:79], a[248:251], v[64:67], v[76:79]\n" \
    "v_mfma_f32_16x16x32_bf16 v[80:83], a[252:255], v[64:67], v[80:83]\n" \
    "s_add_u32 s97, s60, 36864\n" \
    "buffer_load_dwordx4 v[64:67], v35, %[ws], s97 offen sc1\n" \
    "s_waitcnt vmcnt(7)\n" \
    "v_mfma_f32_16x16x32_bf16 v[68:71], v[96:99], v[36:39], v[68:71]\n" \
    "v_mfma_f32_16x16x32_bf16 v[72:75], v[100:103], v[36:39], v[72:75]\n" \
    "v_mfma_f32_16x16x32_bf16 v[76:79], v[104:107], v[36:39], v[76:79]\n" \
    "v_mfma_f32_16x16x32_bf16 v[80:83], v[108:111], v[36:39], v[80:83]\n" \
    "s_add_u32 s97, s60, 49152\n" \
    "buffer_load_dwordx4 v[36:39], v32, %[ws], s97 offen sc1\n" \
    "s_waitcnt vmcnt(7)\n" \
    "v_mfma_f32_16x16x32_bf16 v[68:71], v[112:115], v[40:43], v[68:71]\n" \
    "v_mfma_f32_16x16x32_bf16 v[72:75], v[116:119], v[40:43], v[72:75]\n" \
    "v_mfma_f32_16x16x32_bf16 v[76:79], v[120:123], v[40:43], v[76:79]\n" \
    "v_mfma_f32_16x16x32_bf16 v[80:83], v[124:127], v[40:43], v[80:83]\n" \
    "s_add_u32 s97, s60, 49152\n" \
    "buffer_load_dwordx4 v[40:43], v33, %[ws], s97 offen sc1\n" \
    "s_waitcnt vmcnt(7)\n" \
    "v_mfma_f32_16x16x32_bf16 v[68:71], v[128:131], v[44:47], v[68:71]\n" \
    "v_mfma_f32_16x16x32_bf16 v[72:75], v[132:135], v[44:47], v[72:75]\n" \
    "v_mfma_f32_16x16x32_bf16 v[76:79], v[136:139], v[44:47], v[76:79]\n" \
    "v_mfma_f32_16x16x32_bf16 v[80:83], v[140:143], v[44:47], v[80:83]\n" \
    "s_add_u32 s97, s60, 49152\n" \
    "buffer_load_dwordx4 v[44:47], v34, %[ws], s97 offen sc1\n" \
    "s_waitcnt vmcnt(7)\n" \
    "v_mfma_f32_16x16x32_bf16 v[68:71], v[160:163], v[48:51], v[68:71]\n" \
    "v_mfma_f32_16x16x32_bf16 v[72:75], v[164:167], v[48:51], v[72:75]\n" \
    "v_mfma_f32_16x16x32_bf16 v[76:79], v[168:171], v[48:51], v[76:79]\n" \
    "v_mfma_f32_16x16x32_bf16 v[80:83], v[172:175], v[48:51], v[80:83]\n" \
    "s_add_u32 s97, s60, 49152\n" \
    "buffer_load_dwordx4 v[48:51], v35, %[ws], s97 offen sc1\n" \
    "s_waitcnt vmcnt(7)\n" \
    "v_mfma_f32_16x16x32_bf16 v[68:71], v[176:179], v[52:55], v[68:71]\n" \
    "v_mfma_f32_16x16x32_bf16 v[72:75], v[180:183], v[52:55], v[72:75]\n" \
    "v_mfma_f32_16x16x32_bf16 v[76:79], v[184:187], v[52:55], v[76:79]\n" \
    "v_mfma_f32_16x16x32_bf16 v[80:83], v[188:191], v[52:55], v[80:83]\n" \
    "s_add_u32 s97, s60, 53248\n" \
    "buffer_load_dwordx4 v[52:55], v32, %[ws], s97 offen sc1\n" \
    "s_waitcnt vmcnt(7)\n" \
    "v_mfma_f32_16x16x32_bf16 v[68:71], v[192:195], v[56:59], v[68:71]\n" \
    "v_mfma_f32_16x16x32_bf16 v[72:75], v[196:199], v[56:59], v[72:75]\n" \
    "v_mfma_f32_16x16x32_bf16 v[76:79], v[200:203], v[56:59], v[76:79]\n" \
    "v_mfma_f32_16x16x32_bf16 v[80:83], v[204:207], v[56:59], v[80:83]\n" \
    "s_add_u32 s97, s60, 53248\n" \
    "buffer_load_dwordx4 v[56:59], v33, %[ws], s97 offen sc1\n" \
    "s_waitcnt vmcnt(7)\n" \
    "v_mfma_f32_16x16x32_bf16 v[68:71], v[208:211], v[60:63], v[68:71]\n" \
    "v_mfma_f32_16x16x32_bf16 v[72:75], v[212:215], v[60:63], v[72:75]\n" \
    "v_mfma_f32_16x16x32_bf16 v[76:79], v[216:219], v[60:63], v[76:79]\n" \
    "v_mfma_f32_16x16x32_bf16 v[80:83], v[220:223], v[60:63], v[80:83]\n" \
    "s_add_u32 s97, s60, 53248\n" \
    "buffer_load_dwordx4 v[60:63], v34, %[ws], s97 offen sc1\n" \
    "s_waitcnt vmcnt(7)\n" \
    "v_mfma_f32_16x16x32_bf16 v[68:71], v[224:227], v[64:67], v[68:71]\n" \
    "s_waitcnt vmcnt(26)\n" \
    "v_cmp_gt_i32 vcc, s63, v84\n" \
    "v_mfma_f32_16x16x32_bf16 v[72:75], v[228:231], v[64:67], v[72:75]\n" \
    "s_mul_i32 s61, s63, 0x202000\n" \
    "s_and_b32 s61, s61, 0xffe02000\n" \
    "v_mfma_f32_16x16x32_bf16 v[76:79], v[232:235], v[64:67], v[76:79]\n" \
    "s_xor_b32 s61, s61, %[pol]\n" \
    "s_cbranch_vccnz LC_c1r_%=\n" \
    "LC_c1_%=:\n" \
    "v_mfma_f32_16x16x32_bf16 v[80:83], v[236:239], v[64:67], v[80:83]\n" \
    "s_add_u32 s97, s60, 53248\n" \
    "buffer_load_dwordx4 v[64:67], v35, %[ws], s97 offen sc1\n" \
    "s_waitcnt vmcnt(7)\n" \
    "v_mfma_f32_16x16x32_bf16 v[68:71], v[240:243], v[36:39], v[68:71]\n" \
    "ds_read_b128 v[144:147], %[w25] offset:0\n" \
    "v_mfma_f32_16x16x32_bf16 v[72:75], v[244:247], v[36:39], v[72:75]\n" \
    "ds_read_b128 v[148:151], %[w25] offset:1024\n" \
    "v_mfma_f32_16x16x32_bf16 v[76:79], v[248:251], v[36:39], v[76:79]\n" \
    "ds_read_b128 v[152:155], %[w25] offset:32768\n" \
    "v_mfma_f32_16x16x32_bf16 v[80:83], v[252:255], v[36:39], v[80:83]\n" \
    "ds_read_b128 v[156:159], %[w25] offset:33792\n" \
    "s_waitcnt lgkmcnt(3)\n" \
    "s_add_u32 s97, s61, 0\n" \
    "buffer_load_dwordx4 v[36:39], v32, %[ws], s97 offen sc1\n" \
    "s_waitcnt vmcnt(7)\n" \
    "v_mfma_f32_16x16x32_bf16 v[68:71], v[144:147], v[40:43], v[68:71]\n" \
    "ds_read_b64_tr_b16 v[144:145], v88 offset:8192\n" \
    "ds_read_b64_tr_b16 v[146:147], v88 offset:10240\n" \
    "s_waitcnt lgkmcnt(4)\n" \
    "v_mfma_f32_16x16x32_bf16 v[72:75], v[148:151], v[40:43], v[72:75]\n" \
    "ds_read_b64_tr_b16 v[148:149], v89 offset:8192\n" \
    "ds_read_b64_tr_b16 v[150:151], v89 offset:10240\n" \
    "s_waitcnt lgkmcnt(5)\n" \
    "v_mfma_f32_16x16x32_bf16 v[76:79], v[152:155], v[40:43], v[76:79]\n" \
    "ds_read_b64_tr_b16 v[152:153], v90 offset:8192\n" \
    "ds_read_b64_tr_b16 v[154:155], v90 offset:10240\n" \
    "s_waitcnt lgkmcnt(6)\n" \
    "v_mfma_f32_16x16x32_bf16 v[80:83], v[156:159], v[40:43], v[80:83]\n" \
    "ds_read_b64_tr_b16 v[156:157], v91 offset:8192\n" \
    "ds_read_b64_tr_b16 v[158:159], v91 offset:10240\n" \
    "s_waitcnt lgkmcnt(6)\n" \
    "s_add_u32 s97, s61, 0\n" \
    "buffer_load_dwordx4 v[40:43], v33, %[ws], s97 offen sc1\n" \
    "s_waitcnt vmcnt(7)\n" \
    "v_mfma_f32_16x16x32_bf16 v[68:71], v[144:147], v[44:47], v[68:71]\n" \
    "ds_read_b64_tr_b16 v[144:145], v88 offset:12288\n" \
    "ds_read_b64_tr_b16 v[146:147], v88 offset:14336\n" \
    "s_waitcnt lgkmcnt(6)\n" \
    "v_mfma_f32_16x16x32_bf16 v[72:75], v[148:151], v[44:47], v[72:75]\n" \
    "ds_read_b64_tr_b16 v[148:149], v89 offset:12288\n" \
    "ds_read_b64_tr_b16 v[150:151], v89 offset:14336\n" \
    "s_waitcnt lgkmcnt(6)\n" \
    "v_mfma_f32_16x16x32_bf16 v[76:79], v[152:155], v[44:47], v[76:79]\n" \
    "ds_read_b64_tr_b16 v[152:153], v90 offset:12288\n" \
    "ds_read_b64_tr_b16 v[154:155], v90 offset:14336\n" \
    "s_waitcnt lgkmcnt(6)\n" \
    "v_mfma_f32_16x16x32_bf16 v[80:83], v[156:159], v[44:47], v[80:83]\n" \
    "ds_read_b64_tr_b16 v[156:157], v91 offset:12288\n" \
    "ds_read_b64_tr_b16 v[158:159], v91 offset:14336\n" \
    "s_waitcnt lgkmcnt(6)\n" \
    "s_add_u32 s97, s61, 0\n" \
    "buffer_load_dwordx4 v[44:47], v34, %[ws], s97 offen sc1\n" \
    "s_waitcnt vmcnt(7)\n" \
    "v_mfma_f32_16x16x32_bf16 v[68:71], v[144:147], v[48:51], v[68:71]\n" \
    "ds_read_b64_tr_b16 v[144:145], v88 offset:16384\n" \
    "ds_read_b64_tr_b16 v[146:147], v88 offset:18432\n" \
    "s_waitcnt lgkmcnt(6)\n" \
    "v_mfma_f32_16x16x32_bf16 v[72:75], v[148:151], v[48:51], v[72:75]\n" \
    "ds_read_b64_tr_b16 v[148:149], v89 offset:16384\n" \
    "ds_read_b64_tr_b16 v[150:151], v89 offset:18432\n" \
    "s_waitcnt lgkmcnt(6)\n" \
    "v_mfma_f32_16x16x32_bf16 v[76:79], v[152:155], v[48:51], v[76:79]\n" \
    "ds_read_b64_tr_b16 v[152:153], v90 offset:16384\n" \
    "ds_read_b64_tr_b16 v[154:155], v90 offset:18432\n" \
    "s_waitcnt lgkmcnt(6)\n" \
    "v_mfma_f32_16x16x32_bf16 v[80:83], v[156:159], v[48:51], v[80:83]\n" \
    "ds_read_b64_tr_b16 v[156:157], v91 offset:16384\n" \
    "ds_read_b64_tr_b16 v[158:159], v91 offset:18432\n" \
    "s_waitcnt lgkmcnt(6)\n" \
    "s_add_u32 s97, s61, 0\n" \
    "buffer_load_dwordx4 v[48:51], v35, %[ws], s97 offen sc1\n" \
    "s_waitcnt vmcnt(7)\n" \
    "v_mfma_f32_16x16x32_bf16 v[68:71], v[144:147], v[52:55], v[68:71]\n" \
    "ds_read_b64_tr_b16 v[144:145], v88 offset:20480\n" \
    "ds_read_b64_tr_b16 v[146:147], v88 offset:22528\n" \
    "s_waitcnt lgkmcnt(6)\n" \
    "v_mfma_f32_16x16x32_bf16 v[72:75], v[148:151], v[52:55], v[72:75]\n" \
    "ds_read_b64_tr_b16 v[148:149], v89 offset:20480\n" \
    "ds_read_b64_tr_b16 v[150:151], v89 offset:22528\n" \
    "s_waitcnt lgkmcnt(6)\n" \
    "v_mfma_f32_16x16x32_bf16 v[76:79], v[152:155], v[52:55], v[76:79]\n" \
    "ds_read_b64_tr_b16 v[152:153], v90 offset:20480\n" \
    "ds_read_b64_tr_b16 v[154:155], v90 offset:22528\n" \
    "s_waitcnt lgkmcnt(6)\n" \
    "v_mfma_f32_16x16x32_bf16 v[80:83], v[156:159], v[52:55], v[80:83]\n" \
    "ds_read_b64_tr_b16 v[156:157], v91 offset:20480\n" \
    "ds_read_b64_tr_b16 v[158:159], v91 offset:22528\n" \
    "s_waitcnt lgkmcnt(6)\n" \
    "s_add_u32 s97, s61, 4096\n" \
    "buffer_load_dwordx4 v[52:55], v32, %[ws], s97 offen sc1\n" \
    "s_waitcnt vmcnt(7)\n" \
    "v_mfma_f32_16x16x32_bf16 v[68:71], v[144:147], v[56:59], v[68:71]\n" \
    "ds_read_b64_tr_b16 v[144:145], v88 offset:0\n" \
    "ds_read_b64_tr_b16 v[146:147], v88 offset:2048\n" \
    "s_waitcnt lgkmcnt(6)\n" \
    "v_mfma_f32_16x16x32_bf16 v[72:75], v[148:151], v[56:59], v[72:75]\n" \
    "ds_read_b64_tr_b16 v[148:149], v89 offset:0\n" \
    "ds_read_b64_tr_b16 v[150:151], v89 offset:2048\n" \
    "s_waitcnt lgkmcnt(6)\n" \
    "v_mfma_f32_16x16x32_bf16 v[76:79], v[152:155], v[56:59], v[76:79]\n" \
    "ds_read_b64_tr_b16 v[152:153], v90 offset:0\n" \
    "ds_read_b64_tr_b16 v[154:155], v90 offset:2048\n" \
    "s_waitcnt lgkmcnt(6)\n" \
    "v_mfma_f32_16x16x32_bf16 v[80:83], v[156:159], v[56:59], v[80:83]\n" \
    "ds_read_b64_tr_b16 v[156:157], v91 offset:0\n" \
    "ds_read_b64_tr_b16 v[158:159], v91 offset:2048\n" \
    "s_waitcnt lgkmcnt(6)\n" \
    "s_add_u32 s97, s61, 4096\n" \
    "buffer_load_dwordx4 v[56:59], v33, %[ws], s97 offen sc1\n" \
    "s_waitcnt vmcnt(7)\n" \
    "v_mfma_f32_16x16x32_bf16 v[68:71], v[144:147], v[60:63], v[68:71]\n" \
    "ds_read_b64_tr_b16 v[144:145], v88 offset:4096\n" \
    "ds_read_b64_tr_b16 v[146:147], v88 offset:6144\n" \
    "s_waitcnt lgkmcnt(6)\n" \
    "buffer_load_dword v84, %[vfl], %[ws], %[pol] offen sc1\n" \
    "v_mfma_f32_16x16x32_bf16 v[72:75], v[148:151], v[60:63], v[72:75]\n" \
    "ds_read_b64_tr_b16 v[148:149], v89 offset:4096\n" \
    "ds_read_b64_tr_b16 v[150:151], v89 offset:6144\n" \
    "s_waitcnt lgkmcnt(6)\n" \
    "v_mfma_f32_16x16x32_bf16 v[76:79], v[152:155], v[60:63], v[76:79]\n" \
    "ds_read_b64_tr_b16 v[152:153], v90 offset:4096\n" \
    "ds_read_b64_tr_b16 v[154:155], v90 offset:6144\n" \
    "s_waitcnt lgkmcnt(6)\n" \
    "v_mfma_f32_16x16x32_bf16 v[80:83], v[156:159], v[60:63], v[80:83]\n" \
    "ds_read_b64_tr_b16 v[156:157], v91 offset:4096\n" \
    "ds_read_b64_tr_b16 v[158:159], v91 offset:6144\n" \
    "s_waitcnt lgkmcnt(6)\n" \
    "s_add_u32 s97, s61, 4096\n" \
    "buffer_load_dwordx4 v[60:63], v34, %[ws], s97 offen sc1\n" \
    "s_waitcnt vmcnt(8)\n" \
    "v_mfma_f32_16x16x32_bf16 v[68:71], v[144:147], v[64:67], v[68:71]\n" \
    "s_waitcnt lgkmcnt(4)\n" \
    "v_mfma_f32_16x16x32_bf16 v[72:75], v[148:151], v[64:67], v[72:75]\n" \
    "s_waitcnt lgkmcnt(2)\n" \
    "v_mfma_f32_16x16x32_bf16 v[76:79], v[152:155], v[64:67], v[76:79]\n" \
    "s_waitcnt lgkmcnt(0)\n" \
    "v_mfma_f32_16x16x32_bf16 v[80:83], v[156:159], v[64:67], v[80:83]\n" \
    "s_add_u32 s97, s61, 4096\n" \
    "buffer_load_dwordx4 v[64:67], v35, %[ws], s97 offen sc1\n" \
    "s_nop 1\n" \
    "ds_write_b128 v86, v[68:71] offset:0\n" \
    "s_nop 1\n" \
    "ds_write_b128 v86, v[72:75] offset:64\n" \
    "s_nop 1\n" \
    "ds_write_b128 v86, v[76:79] offset:128\n" \
    "s_nop 1\n" \
    "ds_write_b128 v86, v[80:83] offset:192\n" \
    "s_nop 1\n" \
    "v_xor_b32 v86, 32768, v86\n" \
    "s_mul_i32 s62, s95, 262144\n" \
    "s_add_u32 s62, s62, 0\n" \
    "s_cmp_lg_u32 s63, s95\n" \
    "s_mov_b32 s95, s63\n" \
    "s_mov_b32 s60, s61\n" \
    "s_cbranch_scc1 LC_trip_%=\n" \
    "s_waitcnt vmcnt(0)\n" \
    "s_waitcnt lgkmcnt(0)\n" \
    "s_barrier\n" \
    "ds_read_b128 v[144:147], v87 offset:0\n" \
    "ds_read_b128 v[148:151], v87 offset:4352\n" \
    "ds_read_b128 v[152:155], v87 offset:8704\n" \
    "ds_read_b128 v[156:159], v87 offset:13056\n" \
    "v_xor_b32 v87, 32768, v87\n" \
    "s_nop 0\n" \
    "s_nop 0\n" \
    "s_nop 0\n" \
    "s_nop 0\n" \
    "s_nop 0\n" \
    "s_waitcnt lgkmcnt(0)\n" \
    "v_pk_add_f32 v[144:145], v[144:145], v[148:149]\n" \
    "v_pk_add_f32 v[146:147], v[146:147], v[150:151]\n" \
    "v_pk_add_f32 v[152:153], v[152:153], v[156:157]\n" \
    "v_pk_add_f32 v[154:155], v[154:155], v[158:159]\n" \
    "v_pk_add_f32 v[144:145], v[144:145], v[152:153]\n" \
    "v_pk_add_f32 v[146:147], v[146:147], v[154:155]\n" \
    "buffer_store_dwordx4 v[144:147], %[vout], %[od], s62 offen sc1\n" \
    "s_branch LC_end_%=\n" \
    "LC_hsr_%=:\n" \
    "s_sleep 2\n" \
    "s_add_u32 s98, s98, 1\n" \
    "s_cmp_gt_u32 s98, 131072\n" \
    "s_cbranch_scc1 LC_fail_%=\n" \
    "s_branch LC_hs_%=\n" \
    "LC_redo_%=:\n" \
    "s_waitcnt vmcnt(0) lgkmcnt(0)\n" \
    "s_barrier\n" \
    "s_or_b32 %[cfg], s97, 256\n" \
    "s_branch LC_end_%=\n" \
    "LC_c0r_%=:\n" \
    "buffer_load_dword v84, %[vfl], %[ws], %[pol] offen sc1\n" \
    "s_waitcnt vmcnt(0)\n" \
    "v_cmp_gt_i32 vcc, s63, v84\n" \
    "s_cbranch_vccz LC_c0_%=\n" \
    "s_sleep 1\n" \
    "s_add_u32 s98, s98, 1\n" \
    "s_cmp_gt_u32 s98, 131072\n" \
    "s_cbranch_scc1 LC_fail_%=\n" \
    "s_branch LC_c0r_%=\n" \
    "LC_c1r_%=:\n" \
    "buffer_load_dword v84, %[vfl], %[ws], %[pol] offen sc1\n" \
    "s_waitcnt vmcnt(0)\n" \
    "v_cmp_gt_i32 vcc, s63, v84\n" \
    "s_cbranch_vccz LC_c1_%=\n" \
    "s_sleep 1\n" \
    "s_add_u32 s98, s98, 1\n" \
    "s_cmp_gt_u32 s98, 131072\n" \
    "s_cbranch_scc1 LC_fail_%=\n" \
    "s_branch LC_c1r_%=\n" \
    "LC_first_%=:\n" \
    "v_mfma_f32_16x16x32_bf16 v[68:71], a[0:3], v[36:39], 0\n" \
    "v_mfma_f32_16x16x32_bf16 v[72:75], a[4:7], v[36:39], 0\n" \
    "v_mfma_f32_16x16x32_bf16 v[76:79], a[8:11], v[36:39], 0\n" \
    "v_mfma_f32_16x16x32_bf16 v[80:83], a[12:15], v[36:39], 0\n" \
    "s_add_u32 s97, s60, 16384\n" \
    "buffer_load_dwordx4 v[36:39], v32, %[ws], s97 offen sc1\n" \
    "v_mfma_f32_16x16x32_bf16 v[68:71], a[16:19], v[40:43], v[68:71]\n" \
    "v_mfma_f32_16x16x32_bf16 v[72:75], a[20:23], v[40:43], v[72:75]\n" \
    "v_mfma_f32_16x16x32_bf16 v[76:79], a[24:27], v[40:43], v[76:79]\n" \
    "v_mfma_f32_16x16x32_bf16 v[80:83], a[28:31], v[40:43], v[80:83]\n" \
    "s_add_u32 s97, s60, 16384\n" \
    "buffer_load_dwordx4 v[40:43], v33, %[ws], s97 offen sc1\n" \
    "v_mfma_f32_16x16x32_bf16 v[68:71], a[32:35], v[44:47], v[68:71]\n" \
    "v_mfma_f32_16x16x32_bf16 v[72:75], a[36:39], v[44:47], v[72:75]\n" \
    "v_mfma_f32_16x16x32_bf16 v[76:79], a[40:43], v[44:47], v[76:79]\n" \
    "v_mfma_f32_16x16x32_bf16 v[80:83], a[44:47], v[44:47], v[80:83]\n" \
    "s_add_u32 s97, s60, 16384\n" \
    "buffer_load_dwordx4 v[44:47], v34, %[ws], s97 offen sc1\n" \
    "v_mfma_f32_16x16x32_bf16 v[68:71], a[48:51], v[48:51], v[68:71]\n" \
    "v_mfma_f32_16x16x32_bf16 v[72:75], a[52:55], v[48:51], v[72:75]\n" \
    "v_mfma_f32_16x16x32_bf16 v[76:79], a[56:59], v[48:51], v[76:79]\n" \
    "v_mfma_f32_16x16x32_bf16 v[80:83], a[60:63], v[48:51], v[80:83]\n" \
    "s_add_u32 s97, s60, 16384\n" \
    "buffer_load_dwordx4 v[48:51], v35, %[ws], s97 offen sc1\n" \
    "v_mfma_f32_16x16x32_bf16 v[68:71], a[64:67], v[52:55], v[68:71]\n" \
    "v_mfma_f32_16x16x32_bf16 v[72:75], a[68:71], v[52:55], v[72:75]\n" \
    "v_mfma_f32_16x16x32_bf16 v[76:79], a[72:75], v[52:55], v[76:79]\n" \
    "v_mfma_f32_16x16x32_bf16 v[80:83], a[76:79], v[52:55], v[80:83]\n" \
    "s_add_u32 s97, s60, 20480\n" \
    "buffer_load_dwordx4 v[52:55], v32, %[ws], s97 offen sc1\n" \
    "v_mfma_f32_16x16x32_bf16 v[68:71], a[80:83], v[56:59], v[68:71]\n" \
    "v_mfma_f32_16x16x32_bf16 v[72:75], a[84:87], v[56:59], v[72:75]\n" \
    "v_mfma_f32_16x16x32_bf16 v[76:79], a[88:91], v[56:59], v[76:79]\n" \
    "v_mfma_f32_16x16x32_bf16 v[80:83], a[92:95], v[56:59], v[80:83]\n" \
    "s_add_u32 s97, s60, 20480\n" \
    "buffer_load_dwordx4 v[56:59], v33, %[ws], s97 offen sc1\n" \
    "v_mfma_f32_16x16x32_bf16 v[68:71], a[96:99], v[60:63], v[68:71]\n" \
    "v_mfma_f32_16x16x32_bf16 v[72:75], a[100:103], v[60:63], v[72:75]\n" \
    "v_mfma_f32_16x16x32_bf16 v[76:79], a[104:107], v[60:63], v[76:79]\n" \
    "v_mfma_f32_16x16x32_bf16 v[80:83], a[108:111], v[60:63], v[80:83]\n" \
    "s_add_u32 s97, s60, 20480\n" \
    "buffer_load_dwordx4 v[60:63], v34, %[ws], s97 offen sc1\n" \
    "v_mfma_f32_16x16x32_bf16 v[68:71], a[112:115], v[64:67], v[68:71]\n" \
    "v_mfma_f32_16x16x32_bf16 v[72:75], a[116:119], v[64:67], v[72:75]\n" \
    "v_mfma_f32_16x16x32_bf16 v[76:79], a[120:123], v[64:67], v[76:79]\n" \
    "v_mfma_f32_16x16x32_bf16 v[80:83], a[124:127], v[64:67], v[80:83]\n" \
    "s_add_u32 s97, s60, 20480\n" \
    "buffer_load_dwordx4 v[64:67], v35, %[ws], s97 offen sc1\n" \
    "s_waitcnt vmcnt(0)\n" \
    "s_branch LC_join_%=\n" \
    "LC_fail_%=:\n" \
    "v_mov_b32 v148, 2043904\n" \
    "v_mov_b32 v149, 1\n" \
    "buffer_store_dword v149, v148, %[ws], 0 offen sc1\n" \
    "s_endpgm\n" \
    "LC_end_%=:\n"
#define ASM_ROLE_S2 \
    "s_nop 4\n" \
    "s_mov_b32 s98, 0\n" \
    "s_mov_b32 s95, 0\n" \
    "s_add_u32 s97, %[hsa], 32768\n" \
    "buffer_load_dword v31, off, %[ws], s97 sc1\n" \
    "buffer_load_dwordx2 v[88:89], %[vd], %[dd], 0 offen\n" \
    "buffer_store_dwordx4 v[92:95], %[vpr], %[ws], 0 offen sc1\n" \
    "s_waitcnt vmcnt(0)\n" \
    "v_readfirstlane_b32 %[cfg], v31\n" \
    "s_nop 1\n" \
    "s_add_u32 %[cfg], %[cfg], 256\n" \
    "s_and_b32 %[cfg], %[cfg], 0xffffff00\n" \
    "v_mov_b32 v31, %[cfg]\n" \
    "buffer_store_dword v31, off, %[ws], %[hsa] sc1\n" \
    "s_mov_b32 s61, 0xfff\n" \
    "s_mov_b32 s62, 0xfff\n" \
    "s_waitcnt vmcnt(0)\n" \
    "s_barrier\n" \
    "s_cmp_lg_u32 %[w], 0\n" \
    "s_cbranch_scc1 LS2_pr2_%=\n" \
    "v_readlane_b32 s60, %[vpr], 0\n" \
    "s_memtime s[64:65]\n" \
    "s_nop 2\n" \
    "s_waitcnt lgkmcnt(0)\n" \
    "buffer_load_dword v31, off, %[ws], s60 sc1\n" \
    "s_waitcnt vmcnt(0)\n" \
    "s_memtime s[92:93]\n" \
    "s_waitcnt lgkmcnt(0)\n" \
    "s_sub_u32 s64, s92, s64\n" \
    "s_min_u32 s61, s61, s64\n" \
    "v_readlane_b32 s60, %[vpr], 8\n" \
    "s_memtime s[64:65]\n" \
    "s_nop 2\n" \
    "s_waitcnt lgkmcnt(0)\n" \
    "buffer_load_dword v31, off, %[ws], s60 sc1\n" \
    "s_waitcnt vmcnt(0)\n" \
    "s_memtime s[92:93]\n" \
    "s_waitcnt lgkmcnt(0)\n" \
    "s_sub_u32 s64, s92, s64\n" \
    "s_min_u32 s62, s62, s64\n" \
    "v_readlane_b32 s60, %[vpr], 16\n" \
    "s_memtime s[64:65]\n" \
    "s_nop 2\n" \
    "s_waitcnt lgkmcnt(0)\n" \
    "buffer_load_dword v31, off, %[ws], s60 sc1\n" \
    "s_waitcnt vmcnt(0)\n" \
    "s_memtime s[92:93]\n" \
    "s_waitcnt lgkmcnt(0)\n" \
    "s_sub_u32 s64, s92, s64\n" \
    "s_min_u32 s61, s61, s64\n" \
    "v_readlane_b32 s60, %[vpr], 24\n" \
    "s_memtime s[64:65]\n" \
    "s_nop 2\n" \
    "s_waitcnt lgkmcnt(0)\n" \
    "buffer_load_dword v31, off, %[ws], s60 sc1\n" \
    "s_waitcnt vmcnt(0)\n" \
    "s_memtime s[92:93]\n" \
    "s_waitcnt lgkmcnt(0)\n" \
    "s_sub_u32 s64, s92, s64\n" \
    "s_min_u32 s62, s62, s64\n" \
    "LS2_pr2_%=:\n" \
    "s_barrier\n" \
    "s_lshl_b32 s61, s61, 12\n" \
    "s_or_b32 s61, s61, s62\n" \
    "LS2_h1_%=:\n" \
    "s_add_u32 s97, %[hsa], 32768\n" \
    "buffer_load_dword v31, off, %[ws], s97 sc1\n" \
    "s_waitcnt vmcnt(0)\n" \
    "v_readfirstlane_b32 s97, v31\n" \
    "s_nop 1\n" \
    "s_xor_b32 s97, s97, %[cfg]\n" \
    "s_cmp_gt_u32 s97, 255\n" \
    "s_cbranch_scc1 LS2_h1r_%=\n" \
    "s_and_b32 s96, s97, 3\n" \
    "s_cmp_lg_u32 s96, %[est]\n" \
    "s_cbranch_scc1 LS2_redo_%=\n" \
    "s_waitcnt vmcnt(0)\n" \
    "v_mov_b32 v31, s61\n" \
    "s_add_u32 s97, %[hsa], 69632\n" \
    "buffer_store_dword v31, off, %[ws], s97 sc1\n" \
    "LS2_hs_%=:\n" \
    "s_add_u32 s97, %[hsa], 32768\n" \
    "buffer_load_dword v31, off, %[ws], s97 sc1\n" \
    "s_waitcnt vmcnt(0)\n" \
    "v_readfirstlane_b32 s97, v31\n" \
    "s_nop 1\n" \
    "s_xor_b32 s97, s97, %[cfg]\n" \
    "s_cmp_gt_u32 s97, 255\n" \
    "s_cbranch_scc1 LS2_hsr_%=\n" \
    "s_and_b32 s96, s97, 3\n" \
    "s_cmp_lg_u32 s96, %[est]\n" \
    "s_cbranch_scc1 LS2_redo_%=\n" \
    "s_bitcmp0_b32 s97, 3\n" \
    "s_cbranch_scc1 LS2_hsr_%=\n" \
    "s_mov_b32 %[cfg], s97\n" \
    "LS2_hsok_%=:\n" \
    "s_and_b32 %[pol], %[cfg], 4\n" \
    "s_lshl_b32 %[pol], %[pol], 11\n" \
    "s_xor_b32 %[npol], %[pol], 8192\n" \
    "s_add_u32 s60, %[npol], 2084880\n" \
    "LS2_gw_%=:\n" \
    "s_sleep 4\n" \
    "s_add_u32 s98, s98, 1\n" \
    "s_cmp_gt_u32 s98, 131072\n" \
    "s_cbranch_scc1 LS2_fail_%=\n" \
    "buffer_load_dword v31, off, %[ws], s60 sc1\n" \
    "s_waitcnt vmcnt(0)\n" \
    "v_cmp_gt_i32 vcc, 0, v31\n" \
    "s_cbranch_vccnz LS2_gw_%=\n" \
    "v_add_u32 v40, 16384, %[vow]\n" \
    "v_xor_b32 v40, 64, v40\n" \
    "s_lshl_b32 s64, %[w], 15\n" \
    "v_add_u32 v41, s64, %[fo]\n" \
    "v_xor_b32 v42, 64, v41\n" \
    "s_add_u32 m0, s64, 0\n" \
    "s_mov_b32 s97, 0\n" \
    "buffer_load_dwordx4 %[vow], %[dr], s97 offen nt lds\n" \
    "s_add_u32 m0, s64, 1024\n" \
    "s_mov_b32 s97, 0\n" \
    "buffer_load_dwordx4 v40, %[dr], s97 offen nt lds\n" \
    "s_add_u32 m0, s64, 2048\n" \
    "s_mov_b32 s97, 128\n" \
    "buffer_load_dwordx4 %[vow], %[dr], s97 offen nt lds\n" \
    "s_add_u32 m0, s64, 3072\n" \
    "s_mov_b32 s97, 128\n" \
    "buffer_load_dwordx4 v40, %[dr], s97 offen nt lds\n" \
    "s_add_u32 m0, s64, 4096\n" \
    "s_mov_b32 s97, 256\n" \
    "buffer_load_dwordx4 %[vow], %[dr], s97 offen nt lds\n" \
    "s_add_u32 m0, s64, 5120\n" \
    "s_mov_b32 s97, 256\n" \
    "buffer_load_dwordx4 v40, %[dr], s97 offen nt lds\n" \
    "s_add_u32 m0, s64, 6144\n" \
    "s_mov_b32 s97, 384\n" \
    "buffer_load_dwordx4 %[vow], %[dr], s97 offen nt lds\n" \
    "s_add_u32 m0, s64, 7168\n" \
    "s_mov_b32 s97, 384\n" \
    "buffer_load_dwordx4 v40, %[dr], s97 offen nt lds\n" \
    "s_add_u32 m0, s64, 8192\n" \
    "s_mov_b32 s97, 0\n" \
    "buffer_load_dwordx4 %[vow], %[di], s97 offen nt lds\n" \
    "s_add_u32 m0, s64, 9216\n" \
    "s_mov_b32 s97, 0\n" \
    "buffer_load_dwordx4 v40, %[di], s97 offen nt lds\n" \
    "s_add_u32 m0, s64, 10240\n" \
    "s_mov_b32 s97, 128\n" \
    "buffer_load_dwordx4 %[vow], %[di], s97 offen nt lds\n" \
    "s_add_u32 m0, s64, 11264\n" \
    "s_mov_b32 s97, 128\n" \
    "buffer_load_dwordx4 v40, %[di], s97 offen nt lds\n" \
    "s_add_u32 m0, s64, 12288\n" \
    "s_mov_b32 s97, 256\n" \
    "buffer_load_dwordx4 %[vow], %[di], s97 offen nt lds\n" \
    "s_add_u32 m0, s64, 13312\n" \
    "s_mov_b32 s97, 256\n" \
    "buffer_load_dwordx4 v40, %[di], s97 offen nt lds\n" \
    "s_add_u32 m0, s64, 14336\n" \
    "s_mov_b32 s97, 384\n" \
    "buffer_load_dwordx4 %[vow], %[di], s97 offen nt lds\n" \
    "s_add_u32 m0, s64, 15360\n" \
    "s_mov_b32 s97, 384\n" \
    "buffer_load_dwordx4 v40, %[di], s97 offen nt lds\n" \
    "s_add_u32 m0, s64, 16384\n" \
    "s_mov_b32 s97, 32768\n" \
    "buffer_load_dwordx4 %[vow], %[dr], s97 offen nt lds\n" \
    "s_add_u32 m0, s64, 17408\n" \
    "s_mov_b32 s97, 32768\n" \
    "buffer_load_dwordx4 v40, %[dr], s97 offen nt lds\n" \
    "s_add_u32 m0, s64, 18432\n" \
    "s_mov_b32 s97, 32896\n" \
    "buffer_load_dwordx4 %[vow], %[dr], s97 offen nt lds\n" \
    "s_add_u32 m0, s64, 19456\n" \
    "s_mov_b32 s97, 32896\n" \
    "buffer_load_dwordx4 v40, %[dr], s97 offen nt lds\n" \
    "s_add_u32 m0, s64, 20480\n" \
    "s_mov_b32 s97, 33024\n" \
    "buffer_load_dwordx4 %[vow], %[dr], s97 offen nt lds\n" \
    "s_add_u32 m0, s64, 21504\n" \
    "s_mov_b32 s97, 33024\n" \
    "buffer_load_dwordx4 v40, %[dr], s97 offen nt lds\n" \
    "s_add_u32 m0, s64, 22528\n" \
    "s_mov_b32 s97, 33152\n" \
    "buffer_load_dwordx4 %[vow], %[dr], s97 offen nt lds\n" \
    "s_add_u32 m0, s64, 23552\n" \
    "s_mov_b32 s97, 33152\n" \
    "buffer_load_dwordx4 v40, %[dr], s97 offen nt lds\n" \
    "s_add_u32 m0, s64, 24576\n" \
    "s_mov_b32 s97, 32768\n" \
    "buffer_load_dwordx4 %[vow], %[di], s97 offen nt lds\n" \
    "s_add_u32 m0, s64, 25600\n" \
    "s_mov_b32 s97, 32768\n" \
    "buffer_load_dwordx4 v40, %[di], s97 offen nt lds\n" \
    "s_add_u32 m0, s64, 26624\n" \
    "s_mov_b32 s97, 32896\n" \
    "buffer_load_dwordx4 %[vow], %[di], s97 offen nt lds\n" \
    "s_add_u32 m0, s64, 27648\n" \
    "s_mov_b32 s97, 32896\n" \
    "buffer_load_dwordx4 v40, %[di], s97 offen nt lds\n" \
    "s_add_u32 m0, s64, 28672\n" \
    "s_mov_b32 s97, 33024\n" \
    "buffer_load_dwordx4 %[vow], %[di], s97 offen nt lds\n" \
    "s_add_u32 m0, s64, 29696\n" \
    "s_mov_b32 s97, 33024\n" \
    "buffer_load_dwordx4 v40, %[di], s97 offen nt lds\n" \
    "s_add_u32 m0, s64, 30720\n" \
    "s_mov_b32 s97, 33152\n" \
    "buffer_load_dwordx4 %[vow], %[di], s97 offen nt lds\n" \
    "s_add_u32 m0, s64, 31744\n" \
    "s_mov_b32 s97, 33152\n" \
    "buffer_load_dwordx4 v40, %[di], s97 offen nt lds\n" \
    "s_waitcnt vmcnt(24)\n" \
    "ds_read_b64 a[0:1], v41 offset:0\n" \
    "ds_read_b64 a[16:17], v41 offset:8\n" \
    "ds_read_b64 a[32:33], v42 offset:0\n" \
    "ds_read_b64 a[48:49], v42 offset:8\n" \
    "ds_read_b64 a[64:65], v41 offset:2048\n" \
    "ds_read_b64 a[80:81], v41 offset:2056\n" \
    "ds_read_b64 a[96:97], v42 offset:2048\n" \
    "ds_read_b64 a[112:113], v42 offset:2056\n" \
    "ds_read_b64 a[128:129], v41 offset:4096\n" \
    "ds_read_b64 a[144:145], v41 offset:4104\n" \
    "ds_read_b64 a[160:161], v42 offset:4096\n" \
    "ds_read_b64 a[176:177], v42 offset:4104\n" \
    "ds_read_b64 a[192:193], v41 offset:6144\n" \
    "ds_read_b64 a[208:209], v41 offset:6152\n" \
    "ds_read_b64 a[224:225], v42 offset:6144\n" \
    "ds_read_b64 a[240:241], v42 offset:6152\n" \
    "s_waitcnt lgkmcnt(0)\n" \
    "s_add_u32 m0, s64, 0\n" \
    "s_mov_b32 s97, 65536\n" \
    "buffer_load_dwordx4 %[vow], %[dr], s97 offen nt lds\n" \
    "s_add_u32 m0, s64, 1024\n" \
    "s_mov_b32 s97, 65536\n" \
    "buffer_load_dwordx4 v40, %[dr], s97 offen nt lds\n" \
    "s_add_u32 m0, s64, 2048\n" \
    "s_mov_b32 s97, 65664\n" \
    "buffer_load_dwordx4 %[vow], %[dr], s97 offen nt lds\n" \
    "s_add_u32 m0, s64, 3072\n" \
    "s_mov_b32 s97, 65664\n" \
    "buffer_load_dwordx4 v40, %[dr], s97 offen nt lds\n" \
    "s_add_u32 m0, s64, 4096\n" \
    "s_mov_b32 s97, 65792\n" \
    "buffer_load_dwordx4 %[vow], %[dr], s97 offen nt lds\n" \
    "s_add_u32 m0, s64, 5120\n" \
    "s_mov_b32 s97, 65792\n" \
    "buffer_load_dwordx4 v40, %[dr], s97 offen nt lds\n" \
    "s_add_u32 m0, s64, 6144\n" \
    "s_mov_b32 s97, 65920\n" \
    "buffer_load_dwordx4 %[vow], %[dr], s97 offen nt lds\n" \
    "s_add_u32 m0, s64, 7168\n" \
    "s_mov_b32 s97, 65920\n" \
    "buffer_load_dwordx4 v40, %[dr], s97 offen nt lds\n" \
    "s_waitcnt vmcnt(24)\n" \
    "ds_read_b64 a[2:3], v41 offset:8192\n" \
    "ds_read_b64 a[18:19], v41 offset:8200\n" \
    "ds_read_b64 a[34:35], v42 offset:8192\n" \
    "ds_read_b64 a[50:51], v42 offset:8200\n" \
    "ds_read_b64 a[66:67], v41 offset:10240\n" \
    "ds_read_b64 a[82:83], v41 offset:10248\n" \
    "ds_read_b64 a[98:99], v42 offset:10240\n" \
    "ds_read_b64 a[114:115], v42 offset:10248\n" \
    "ds_read_b64 a[130:131], v41 offset:12288\n" \
    "ds_read_b64 a[146:147], v41 offset:12296\n" \
    "ds_read_b64 a[162:163], v42 offset:12288\n" \
    "ds_read_b64 a[178:179], v42 offset:12296\n" \
    "ds_read_b64 a[194:195], v41 offset:14336\n" \
    "ds_read_b64 a[210:211], v41 offset:14344\n" \
    "ds_read_b64 a[226:227], v42 offset:14336\n" \
    "ds_read_b64 a[242:243], v42 offset:14344\n" \
    "s_waitcnt lgkmcnt(0)\n" \
    "s_add_u32 m0, s64, 8192\n" \
    "s_mov_b32 s97, 65536\n" \
    "buffer_load_dwordx4 %[vow], %[di], s97 offen nt lds\n" \
    "s_add_u32 m0, s64, 9216\n" \
    "s_mov_b32 s97, 65536\n" \
    "buffer_load_dwordx4 v40, %[di], s97 offen nt lds\n" \
    "s_add_u32 m0, s64, 10240\n" \
    "s_mov_b32 s97, 65664\n" \
    "buffer_load_dwordx4 %[vow], %[di], s97 offen nt lds\n" \
    "s_add_u32 m0, s64, 11264\n" \
    "s_mov_b32 s97, 65664\n" \
    "buffer_load_dwordx4 v40, %[di], s97 offen nt lds\n" \
    "s_add_u32 m0, s64, 12288\n" \
    "s_mov_b32 s97, 65792\n" \
    "buffer_load_dwordx4 %[vow], %[di], s97 offen nt lds\n" \
    "s_add_u32 m0, s64, 13312\n" \
    "s_mov_b32 s97, 65792\n" \
    "buffer_load_dwordx4 v40, %[di], s97 offen nt lds\n" \
    "s_add_u32 m0, s64, 14336\n" \
    "s_mov_b32 s97, 65920\n" \
    "buffer_load_dwordx4 %[vow], %[di], s97 offen nt lds\n" \
    "s_add_u32 m0, s64, 15360\n" \
    "s_mov_b32 s97, 65920\n" \
    "buffer_load_dwordx4 v40, %[di], s97 offen nt lds\n" \
    "s_waitcnt vmcnt(24)\n" \
    "ds_read_b64 a[4:5], v41 offset:16384\n" \
    "ds_read_b64 a[20:21], v41 offset:16392\n" \
    "ds_read_b64 a[36:37], v42 offset:16384\n" \
    "ds_read_b64 a[52:53], v42 offset:16392\n" \
    "ds_read_b64 a[68:69], v41 offset:18432\n" \
    "ds_read_b64 a[84:85], v41 offset:18440\n" \
    "ds_read_b64 a[100:101], v42 offset:18432\n" \
    "ds_read_b64 a[116:117], v42 offset:18440\n" \
    "ds_read_b64 a[132:133], v41 offset:20480\n" \
    "ds_read_b64 a[148:149], v41 offset:20488\n" \
    "ds_read_b64 a[164:165], v42 offset:20480\n" \
    "ds_read_b64 a[180:181], v42 offset:20488\n" \
    "ds_read_b64 a[196:197], v41 offset:22528\n" \
    "ds_read_b64 a[212:213], v41 offset:22536\n" \
    "ds_read_b64 a[228:229], v42 offset:22528\n" \
    "ds_read_b64 a[244:245], v42 offset:22536\n" \
    "s_waitcnt lgkmcnt(0)\n" \
    "s_add_u32 m0, s64, 16384\n" \
    "s_mov_b32 s97, 98304\n" \
    "buffer_load_dwordx4 %[vow], %[dr], s97 offen nt lds\n" \
    "s_add_u32 m0, s64, 17408\n" \
    "s_mov_b32 s97, 98304\n" \
    "buffer_load_dwordx4 v40, %[dr], s97 offen nt lds\n" \
    "s_add_u32 m0, s64, 18432\n" \
    "s_mov_b32 s97, 98432\n" \
    "buffer_load_dwordx4 %[vow], %[dr], s97 offen nt lds\n" \
    "s_add_u32 m0, s64, 19456\n" \
    "s_mov_b32 s97, 98432\n" \
    "buffer_load_dwordx4 v40, %[dr], s97 offen nt lds\n" \
    "s_add_u32 m0, s64, 20480\n" \
    "s_mov_b32 s97, 98560\n" \
    "buffer_load_dwordx4 %[vow], %[dr], s97 offen nt lds\n" \
    "s_add_u32 m0, s64, 21504\n" \
    "s_mov_b32 s97, 98560\n" \
    "buffer_load_dwordx4 v40, %[dr], s97 offen nt lds\n" \
    "s_add_u32 m0, s64, 22528\n" \
    "s_mov_b32 s97, 98688\n" \
    "buffer_load_dwordx4 %[vow], %[dr], s97 offen nt lds\n" \
    "s_add_u32 m0, s64, 23552\n" \
    "s_mov_b32 s97, 98688\n" \
    "buffer_load_dwordx4 v40, %[dr], s97 offen nt lds\n" \
    "s_waitcnt vmcnt(24)\n" \
    "ds_read_b64 a[6:7], v41 offset:24576\n" \
    "ds_read_b64 a[22:23], v41 offset:24584\n" \
    "ds_read_b64 a[38:39], v42 offset:24576\n" \
    "ds_read_b64 a[54:55], v42 offset:24584\n" \
    "ds_read_b64 a[70:71], v41 offset:26624\n" \
    "ds_read_b64 a[86:87], v41 offset:26632\n" \
    "ds_read_b64 a[102:103], v42 offset:26624\n" \
    "ds_read_b64 a[118:119], v42 offset:26632\n" \
    "ds_read_b64 a[134:135], v41 offset:28672\n" \
    "ds_read_b64 a[150:151], v41 offset:28680\n" \
    "ds_read_b64 a[166:167], v42 offset:28672\n" \
    "ds_read_b64 a[182:183], v42 offset:28680\n" \
    "ds_read_b64 a[198:199], v41 offset:30720\n" \
    "ds_read_b64 a[214:215], v41 offset:30728\n" \
    "ds_read_b64 a[230:231], v42 offset:30720\n" \
    "ds_read_b64 a[246:247], v42 offset:30728\n" \
    "s_waitcnt lgkmcnt(0)\n" \
    "s_add_u32 m0, s64, 24576\n" \
    "s_mov_b32 s97, 98304\n" \
    "buffer_load_dwordx4 %[vow], %[di], s97 offen nt lds\n" \
    "s_add_u32 m0, s64, 25600\n" \
    "s_mov_b32 s97, 98304\n" \
    "buffer_load_dwordx4 v40, %[di], s97 offen nt lds\n" \
    "s_add_u32 m0, s64, 26624\n" \
    "s_mov_b32 s97, 98432\n" \
    "buffer_load_dwordx4 %[vow], %[di], s97 offen nt lds\n" \
    "s_add_u32 m0, s64, 27648\n" \
    "s_mov_b32 s97, 98432\n" \
    "buffer_load_dwordx4 v40, %[di], s97 offen nt lds\n" \
    "s_add_u32 m0, s64, 28672\n" \
    "s_mov_b32 s97, 98560\n" \
    "buffer_load_dwordx4 %[vow], %[di], s97 offen nt lds\n" \
    "s_add_u32 m0, s64, 29696\n" \
    "s_mov_b32 s97, 98560\n" \
    "buffer_load_dwordx4 v40, %[di], s97 offen nt lds\n" \
    "s_add_u32 m0, s64, 30720\n" \
    "s_mov_b32 s97, 98688\n" \
    "buffer_load_dwordx4 %[vow], %[di], s97 offen nt lds\n" \
    "s_add_u32 m0, s64, 31744\n" \
    "s_mov_b32 s97, 98688\n" \
    "buffer_load_dwordx4 v40, %[di], s97 offen nt lds\n" \
    "s_waitcnt vmcnt(24)\n" \
    "ds_read_b64 a[8:9], v41 offset:0\n" \
    "ds_read_b64 a[24:25], v41 offset:8\n" \
    "ds_read_b64 a[40:41], v42 offset:0\n" \
    "ds_read_b64 a[56:57], v42 offset:8\n" \
    "ds_read_b64 a[72:73], v41 offset:2048\n" \
    "ds_read_b64 a[88:89], v41 offset:2056\n" \
    "ds_read_b64 a[104:105], v42 offset:2048\n" \
    "ds_read_b64 a[120:121], v42 offset:2056\n" \
    "ds_read_b64 a[136:137], v41 offset:4096\n" \
    "ds_read_b64 a[152:153], v41 offset:4104\n" \
    "ds_read_b64 a[168:169], v42 offset:4096\n" \
    "ds_read_b64 a[184:185], v42 offset:4104\n" \
    "ds_read_b64 a[200:201], v41 offset:6144\n" \
    "ds_read_b64 a[216:217], v41 offset:6152\n" \
    "ds_read_b64 a[232:233], v42 offset:6144\n" \
    "ds_read_b64 a[248:249], v42 offset:6152\n" \
    "s_waitcnt lgkmcnt(0)\n" \
    "s_waitcnt vmcnt(16)\n" \
    "ds_read_b64 a[10:11], v41 offset:8192\n" \
    "ds_read_b64 a[26:27], v41 offset:8200\n" \
    "ds_read_b64 a[42:43], v42 offset:8192\n" \
    "ds_read_b64 a[58:59], v42 offset:8200\n" \
    "ds_read_b64 a[74:75], v41 offset:10240\n" \
    "ds_read_b64 a[90:91], v41 offset:10248\n" \
    "ds_read_b64 a[106:107], v42 offset:10240\n" \
    "ds_read_b64 a[122:123], v42 offset:10248\n" \
    "ds_read_b64 a[138:139], v41 offset:12288\n" \
    "ds_read_b64 a[154:155], v41 offset:12296\n" \
    "ds_read_b64 a[170:171], v42 offset:12288\n" \
    "ds_read_b64 a[186:187], v42 offset:12296\n" \
    "ds_read_b64 a[202:203], v41 offset:14336\n" \
    "ds_read_b64 a[218:219], v41 offset:14344\n" \
    "ds_read_b64 a[234:235], v42 offset:14336\n" \
    "ds_read_b64 a[250:251], v42 offset:14344\n" \
    "s_waitcnt lgkmcnt(0)\n" \
    "s_waitcnt vmcnt(8)\n" \
    "ds_read_b64 a[12:13], v41 offset:16384\n" \
    "ds_read_b64 a[28:29], v41 offset:16392\n" \
    "ds_read_b64 a[44:45], v42 offset:16384\n" \
    "ds_read_b64 a[60:61], v42 offset:16392\n" \
    "ds_read_b64 a[76:77], v41 offset:18432\n" \
    "ds_read_b64 a[92:93], v41 offset:18440\n" \
    "ds_read_b64 a[108:109], v42 offset:18432\n" \
    "ds_read_b64 a[124:125], v42 offset:18440\n" \
    "ds_read_b64 a[140:141], v41 offset:20480\n" \
    "ds_read_b64 a[156:157], v41 offset:20488\n" \
    "ds_read_b64 a[172:173], v42 offset:20480\n" \
    "ds_read_b64 a[188:189], v42 offset:20488\n" \
    "ds_read_b64 a[204:205], v41 offset:22528\n" \
    "ds_read_b64 a[220:221], v41 offset:22536\n" \
    "ds_read_b64 a[236:237], v42 offset:22528\n" \
    "ds_read_b64 a[252:253], v42 offset:22536\n" \
    "s_waitcnt lgkmcnt(0)\n" \
    "s_waitcnt vmcnt(0)\n" \
    "ds_read_b64 a[14:15], v41 offset:24576\n" \
    "ds_read_b64 a[30:31], v41 offset:24584\n" \
    "ds_read_b64 a[46:47], v42 offset:24576\n" \
    "ds_read_b64 a[62:63], v42 offset:24584\n" \
    "ds_read_b64 a[78:79], v41 offset:26624\n" \
    "ds_read_b64 a[94:95], v41 offset:26632\n" \
    "ds_read_b64 a[110:111], v42 offset:26624\n" \
    "ds_read_b64 a[126:127], v42 offset:26632\n" \
    "ds_read_b64 a[142:143], v41 offset:28672\n" \
    "ds_read_b64 a[158:159], v41 offset:28680\n" \
    "ds_read_b64 a[174:175], v42 offset:28672\n" \
    "ds_read_b64 a[190:191], v42 offset:28680\n" \
    "ds_read_b64 a[206:207], v41 offset:30720\n" \
    "ds_read_b64 a[222:223], v41 offset:30728\n" \
    "ds_read_b64 a[238:239], v42 offset:30720\n" \
    "ds_read_b64 a[254:255], v42 offset:30728\n" \
    "s_waitcnt lgkmcnt(0)\n" \
    "s_barrier\n" \
    "v_lshlrev_b32 v92, 16, v88\n" \
    "v_and_b32 v93, 0xffff0000, v88\n" \
    "v_lshlrev_b32 v94, 16, v89\n" \
    "v_and_b32 v95, 0xffff0000, v89\n" \
    "s_add_u32 s97, %[w], 0\n" \
    "s_and_b32 s97, s97, 3\n" \
    "s_lshl_b32 s97, s97, 10\n" \
    "v_add_u32 v32, s97, %[vo]\n" \
    "s_add_u32 s97, %[w], 1\n" \
    "s_and_b32 s97, s97, 3\n" \
    "s_lshl_b32 s97, s97, 10\n" \
    "v_add_u32 v33, s97, %[vo]\n" \
    "s_add_u32 s97, %[w], 2\n" \
    "s_and_b32 s97, s97, 3\n" \
    "s_lshl_b32 s97, s97, 10\n" \
    "v_add_u32 v34, s97, %[vo]\n" \
    "s_add_u32 s97, %[w], 3\n" \
    "s_and_b32 s97, s97, 3\n" \
    "s_lshl_b32 s97, s97, 10\n" \
    "v_add_u32 v35, s97, %[vo]\n" \
    "v_mov_b32 v86, %[wp]\n" \
    "v_mov_b32 v87, %[rp]\n" \
    "v_mov_b32 v84, -1\n" \
    "s_mov_b32 s63, 0\n" \
    "s_nop 0\n" \
    "v_cmp_gt_i32 vcc, s63, v84\n" \
    "s_mul_i32 s61, s63, 0x202000\n" \
    "s_and_b32 s61, s61, 0xffe02000\n" \
    "s_xor_b32 s61, s61, %[pol]\n" \
    "s_cbranch_vccnz LS2_c0r_%=\n" \
    "LS2_c0_%=:\n" \
    "s_add_u32 s97, s61, 0\n" \
    "buffer_load_dwordx4 v[144:147], v32, %[ws], s97 offen sc1 ;F0\n" \
    "s_add_u32 s97, s61, 0\n" \
    "buffer_load_dwordx4 v[148:151], v33, %[ws], s97 offen sc1 ;F1\n" \
    "s_add_u32 s97, s61, 0\n" \
    "buffer_load_dwordx4 v[152:155], v34, %[ws], s97 offen sc1 ;F2\n" \
    "s_add_u32 s97, s61, 0\n" \
    "buffer_load_dwordx4 v[156:159], v35, %[ws], s97 offen sc1 ;F3\n" \
    "s_add_u32 s97, s61, 4096\n" \
    "buffer_load_dwordx4 v[160:163], v32, %[ws], s97 offen sc1 ;F4\n" \
    "s_add_u32 s97, s61, 4096\n" \
    "buffer_load_dwordx4 v[164:167], v33, %[ws], s97 offen sc1 ;F5\n" \
    "s_add_u32 s97, s61, 4096\n" \
    "buffer_load_dwordx4 v[168:171], v34, %[ws], s97 offen sc1 ;F6\n" \
    "s_add_u32 s97, s61, 4096\n" \
    "buffer_load_dwordx4 v[172:175], v35, %[ws], s97 offen sc1 ;F7\n" \
    "s_add_u32 s97, s61, 16384\n" \
    "buffer_load_dwordx4 v[176:179], v32, %[ws], s97 offen sc1 ;F8\n" \
    "s_add_u32 s97, s61, 16384\n" \
    "buffer_load_dwordx4 v[180:183], v33, %[ws], s97 offen sc1 ;F9\n" \
    "s_add_u32 s97, s61, 16384\n" \
    "buffer_load_dwordx4 v[184:187], v34, %[ws], s97 offen sc1 ;F10\n" \
    "s_add_u32 s97, s61, 16384\n" \
    "buffer_load_dwordx4 v[188:191], v35, %[ws], s97 offen sc1 ;F11\n" \
    "s_add_u32 s97, s61, 20480\n" \
    "buffer_load_dwordx4 v[192:195], v32, %[ws], s97 offen sc1 ;F12\n" \
    "s_add_u32 s97, s61, 20480\n" \
    "buffer_load_dwordx4 v[196:199], v33, %[ws], s97 offen sc1 ;F13\n" \
    "s_add_u32 s97, s61, 20480\n" \
    "buffer_load_dwordx4 v[200:203], v34, %[ws], s97 offen sc1 ;F14\n" \
    "s_add_u32 s97, s61, 20480\n" \
    "buffer_load_dwordx4 v[204:207], v35, %[ws], s97 offen sc1 ;F15\n" \
    "s_mov_b32 s95, s63\n" \
    "s_mov_b32 s60, s61\n" \
    "s_waitcnt vmcnt(0)\n" \
    "s_branch LS2_first_%=\n" \
    "LS2_trip_%=:\n" \
    "s_waitcnt vmcnt(17)\n" \
    "v_mfma_f32_16x16x32_bf16 v[68:71], a[0:3], v[144:147], 0\n" \
    "s_nop 0\n" \
    "s_nop 0\n" \
    "v_mfma_f32_16x16x32_bf16 v[72:75], a[4:7], v[144:147], 0\n" \
    "ds_write_b128 v86, v[128:131] offset:0\n" \
    "s_nop 0\n" \
    "v_mfma_f32_16x16x32_bf16 v[76:79], a[8:11], v[144:147], 0\n" \
    "ds_write_b128 v86, v[132:135] offset:64\n" \
    "s_nop 0\n" \
    "v_mfma_f32_16x16x32_bf16 v[80:83], a[12:15], v[144:147], 0\n" \
    "s_add_u32 s97, s60, 131072\n" \
    "buffer_load_dwordx4 v[144:147], v32, %[ws], s97 offen sc1\n" \
    "s_waitcnt vmcnt(17)\n" \
    "v_mfma_f32_16x16x32_bf16 v[68:71], a[16:19], v[148:151], v[68:71]\n" \
    "ds_write_b128 v86, v[136:139] offset:128\n" \
    "s_nop 0\n" \
    "v_mfma_f32_16x16x32_bf16 v[72:75], a[20:23], v[148:151], v[72:75]\n" \
    "ds_write_b128 v86, v[140:143] offset:192\n" \
    "s_nop 0\n" \
    "v_mfma_f32_16x16x32_bf16 v[76:79], a[24:27], v[148:151], v[76:79]\n" \
    "v_xor_b32 v86, 32768, v86\n" \
    "s_nop 0\n" \
    "v_mfma_f32_16x16x32_bf16 v[80:83], a[28:31], v[148:151], v[80:83]\n" \
    "s_add_u32 s97, s60, 131072\n" \
    "buffer_load_dwordx4 v[148:151], v33, %[ws], s97 offen sc1\n" \
    "s_waitcnt vmcnt(17)\n" \
    "v_mfma_f32_16x16x32_bf16 v[68:71], a[32:35], v[152:155], v[68:71]\n" \
    "s_nop 0\n" \
    "s_nop 0\n" \
    "v_mfma_f32_16x16x32_bf16 v[72:75], a[36:39], v[152:155], v[72:75]\n" \
    "s_nop 0\n" \
    "s_nop 0\n" \
    "v_mfma_f32_16x16x32_bf16 v[76:79], a[40:43], v[152:155], v[76:79]\n" \
    "s_nop 0\n" \
    "s_nop 0\n" \
    "v_mfma_f32_16x16x32_bf16 v[80:83], a[44:47], v[152:155], v[80:83]\n" \
    "s_add_u32 s97, s60, 131072\n" \
    "buffer_load_dwordx4 v[152:155], v34, %[ws], s97 offen sc1\n" \
    "s_waitcnt vmcnt(17)\n" \
    "v_mfma_f32_16x16x32_bf16 v[68:71], a[48:51], v[156:159], v[68:71]\n" \
    "s_waitcnt lgkmcnt(0)\n" \
    "s_barrier\n" \
    "v_mfma_f32_16x16x32_bf16 v[72:75], a[52:55], v[156:159], v[72:75]\n" \
    "ds_read_b128 v[96:99], v87 offset:0\n" \
    "ds_read_b128 v[100:103], v87 offset:4352\n" \
    "v_mfma_f32_16x16x32_bf16 v[76:79], a[56:59], v[156:159], v[76:79]\n" \
    "ds_read_b128 v[104:107], v87 offset:8704\n" \
    "ds_read_b128 v[108:111], v87 offset:13056\n" \
    "v_mfma_f32_16x16x32_bf16 v[80:83], a[60:63], v[156:159], v[80:83]\n" \
    "s_add_u32 s97, s60, 131072\n" \
    "buffer_load_dwordx4 v[156:159], v35, %[ws], s97 offen sc1\n" \
    "s_waitcnt vmcnt(17)\n" \
    "v_mfma_f32_16x16x32_bf16 v[68:71], a[64:67], v[160:163], v[68:71]\n" \
    "v_xor_b32 v87, 32768, v87\n" \
    "s_nop 0\n" \
    "v_mfma_f32_16x16x32_bf16 v[72:75], a[68:71], v[160:163], v[72:75]\n" \
    "s_nop 0\n" \
    "s_nop 0\n" \
    "v_mfma_f32_16x16x32_bf16 v[76:79], a[72:75], v[160:163], v[76:79]\n" \
    "s_nop 0\n" \
    "s_nop 0\n" \
    "v_mfma_f32_16x16x32_bf16 v[80:83], a[76:79], v[160:163], v[80:83]\n" \
    "s_add_u32 s97, s60, 135168\n" \
    "buffer_load_dwordx4 v[160:163], v32, %[ws], s97 offen sc1\n" \
    "s_waitcnt vmcnt(17)\n" \
    "v_mfma_f32_16x16x32_bf16 v[68:71], a[80:83], v[164:167], v[68:71]\n" \
    "s_waitcnt lgkmcnt(0)\n" \
    "v_pk_add_f32 v[96:97], v[96:97], v[100:101]\n" \
    "v_mfma_f32_16x16x32_bf16 v[72:75], a[84:87], v[164:167], v[72:75]\n" \
    "v_pk_add_f32 v[98:99], v[98:99], v[102:103]\n" \
    "v_pk_add_f32 v[104:105], v[104:105], v[108:109]\n" \
    "v_mfma_f32_16x16x32_bf16 v[76:79], a[88:91], v[164:167], v[76:79]\n" \
    "v_pk_add_f32 v[106:107], v[106:107], v[110:111]\n" \
    "v_pk_add_f32 v[96:97], v[96:97], v[104:105]\n" \
    "v_mfma_f32_16x16x32_bf16 v[80:83], a[92:95], v[164:167], v[80:83]\n" \
    "s_add_u32 s97, s60, 135168\n" \
    "buffer_load_dwordx4 v[164:167], v33, %[ws], s97 offen sc1\n" \
    "s_waitcnt vmcnt(17)\n" \
    "v_mfma_f32_16x16x32_bf16 v[68:71], a[96:99], v[168:171], v[68:71]\n" \
    "v_pk_add_f32 v[98:99], v[98:99], v[106:107]\n" \
    "s_waitcnt vmcnt(29)\n" \
    "v_mfma_f32_16x16x32_bf16 v[72:75], a[100:103], v[168:171], v[72:75]\n" \
    "v_lshlrev_b32 v112, 16, v90\n" \
    "v_and_b32 v113, 0xffff0000, v90\n" \
    "v_mfma_f32_16x16x32_bf16 v[76:79], a[104:107], v[168:171], v[76:79]\n" \
    "v_lshlrev_b32 v114, 16, v91\n" \
    "v_and_b32 v115, 0xffff0000, v91\n" \
    "v_mfma_f32_16x16x32_bf16 v[80:83], a[108:111], v[168:171], v[80:83]\n" \
    "s_add_u32 s97, s60, 135168\n" \
    "buffer_load_dwordx4 v[168:171], v34, %[ws], s97 offen sc1\n" \
    "s_waitcnt vmcnt(17)\n" \
    "v_mfma_f32_16x16x32_bf16 v[68:71], a[112:115], v[172:175], v[68:71]\n" \
    "v_mul_f32 v112, v112, v92\n" \
    "v_mul_f32 v113, v113, v93\n" \
    "v_mfma_f32_16x16x32_bf16 v[72:75], a[116:119], v[172:175], v[72:75]\n" \
    "v_mul_f32 v114, v114, v94\n" \
    "v_mul_f32 v115, v115, v95\n" \
    "v_mfma_f32_16x16x32_bf16 v[76:79], a[120:123], v[172:175], v[76:79]\n" \
    "v_cvt_pk_bf16_f32 v116, v112, v113\n" \
    "v_cvt_pk_bf16_f32 v117, v114, v115\n" \
    "v_mfma_f32_16x16x32_bf16 v[80:83], a[124:127], v[172:175], v[80:83]\n" \
    "s_add_u32 s97, s60, 135168\n" \
    "buffer_load_dwordx4 v[172:175], v35, %[ws], s97 offen sc1\n" \
    "s_waitcnt vmcnt(17)\n" \
    "v_mfma_f32_16x16x32_bf16 v[68:71], a[128:131], v[176:179], v[68:71]\n" \
    "v_lshlrev_b32 v112, 16, v116\n" \
    "v_and_b32 v113, 0xffff0000, v116\n" \
    "v_mfma_f32_16x16x32_bf16 v[72:75], a[132:135], v[176:179], v[72:75]\n" \
    "v_lshlrev_b32 v114, 16, v117\n" \
    "v_and_b32 v115, 0xffff0000, v117\n" \
    "v_mfma_f32_16x16x32_bf16 v[76:79], a[136:139], v[176:179], v[76:79]\n" \
    "v_fma_f32 v96, 2.0, v96, v112\n" \
    "v_fma_f32 v97, 2.0, v97, v113\n" \
    "v_mfma_f32_16x16x32_bf16 v[80:83], a[140:143], v[176:179], v[80:83]\n" \
    "s_add_u32 s97, s60, 147456\n" \
    "buffer_load_dwordx4 v[176:179], v32, %[ws], s97 offen sc1\n" \
    "s_waitcnt vmcnt(16)\n" \
    "v_mfma_f32_16x16x32_bf16 v[68:71], a[144:147], v[180:183], v[68:71]\n" \
    "v_fma_f32 v98, 2.0, v98, v114\n" \
    "v_fma_f32 v99, 2.0, v99, v115\n" \
    "v_mfma_f32_16x16x32_bf16 v[72:75], a[148:151], v[180:183], v[72:75]\n" \
    "buffer_store_dwordx4 v[96:99], %[vout], %[od], s62 offen sc1\n" \
    "v_mfma_f32_16x16x32_bf16 v[76:79], a[152:155], v[180:183], v[76:79]\n" \
    "v_mfma_f32_16x16x32_bf16 v[80:83], a[156:159], v[180:183], v[80:83]\n" \
    "s_add_u32 s97, s60, 147456\n" \
    "buffer_load_dwordx4 v[180:183], v33, %[ws], s97 offen sc1\n" \
    "s_waitcnt vmcnt(17)\n" \
    "v_mfma_f32_16x16x32_bf16 v[68:71], a[160:163], v[184:187], v[68:71]\n" \
    "v_mfma_f32_16x16x32_bf16 v[72:75], a[164:167], v[184:187], v[72:75]\n" \
    "v_mfma_f32_16x16x32_bf16 v[76:79], a[168:171], v[184:187], v[76:79]\n" \
    "v_mfma_f32_16x16x32_bf16 v[80:83], a[172:175], v[184:187], v[80:83]\n" \
    "s_add_u32 s97, s60, 147456\n" \
    "buffer_load_dwordx4 v[184:187], v34, %[ws], s97 offen sc1\n" \
    "LS2_join_%=:\n" \
    "s_waitcnt vmcnt(17)\n" \
    "v_mfma_f32_16x16x32_bf16 v[68:71], a[176:179], v[188:191], v[68:71]\n" \
    "s_lshl_b32 s97, s95, 16\n" \
    "buffer_load_dwordx2 v[88:89], %[vx], %[dx], s97 offen\n" \
    "v_mfma_f32_16x16x32_bf16 v[72:75], a[180:183], v[188:191], v[72:75]\n" \
    "s_lshl_b32 s97, s95, 16\n" \
    "v_mfma_f32_16x16x32_bf16 v[76:79], a[184:187], v[188:191], v[76:79]\n" \
    "buffer_load_dwordx2 v[90:91], %[vx], %[dx], s97 offen offset:2048\n" \
    "v_mfma_f32_16x16x32_bf16 v[80:83], a[188:191], v[188:191], v[80:83]\n" \
    "s_add_u32 s97, s60, 147456\n" \
    "buffer_load_dwordx4 v[188:191], v35, %[ws], s97 offen sc1\n" \
    "s_waitcnt vmcnt(19)\n" \
    "v_mfma_f32_16x16x32_bf16 v[68:71], a[192:195], v[192:195], v[68:71]\n" \
    "s_add_u32 s63, s95, 1\n" \
    "v_mfma_f32_16x16x32_bf16 v[72:75], a[196:199], v[192:195], v[72:75]\n" \
    "s_cmp_lt_u32 s63, 64\n" \
    "v_mfma_f32_16x16x32_bf16 v[76:79], a[200:203], v[192:195], v[76:79]\n" \
    "s_cselect_b32 s63, s63, s95\n" \
    "v_mfma_f32_16x16x32_bf16 v[80:83], a[204:207], v[192:195], v[80:83]\n" \
    "s_add_u32 s97, s60, 151552\n" \
    "buffer_load_dwordx4 v[192:195], v32, %[ws], s97 offen sc1\n" \
    "s_waitcnt vmcnt(19)\n" \
    "v_mfma_f32_16x16x32_bf16 v[68:71], a[208:211], v[196:199], v[68:71]\n" \
    "v_mfma_f32_16x16x32_bf16 v[72:75], a[212:215], v[196:199], v[72:75]\n" \
    "v_mfma_f32_16x16x32_bf16 v[76:79], a[216:219], v[196:199], v[76:79]\n" \
    "v_mfma_f32_16x16x32_bf16 v[80:83], a[220:223], v[196:199], v[80:83]\n" \
    "s_add_u32 s97, s60, 151552\n" \
    "buffer_load_dwordx4 v[196:199], v33, %[ws], s97 offen sc1\n" \
    "s_waitcnt vmcnt(18)\n" \
    "v_mfma_f32_16x16x32_bf16 v[68:71], a[224:227], v[200:203], v[68:71]\n" \
    "v_mfma_f32_16x16x32_bf16 v[72:75], a[228:231], v[200:203], v[72:75]\n" \
    "v_mfma_f32_16x16x32_bf16 v[76:79], a[232:235], v[200:203], v[76:79]\n" \
    "v_mfma_f32_16x16x32_bf16 v[80:83], a[236:239], v[200:203], v[80:83]\n" \
    "s_add_u32 s97, s60, 151552\n" \
    "buffer_load_dwordx4 v[200:203], v34, %[ws], s97 offen sc1\n" \
    "s_waitcnt vmcnt(18)\n" \
    "v_mfma_f32_16x16x32_bf16 v[68:71], a[240:243], v[204:207], v[68:71]\n" \
    "s_waitcnt vmcnt(20)\n" \
    "v_cmp_gt_i32 vcc, s63, v84\n" \
    "v_mfma_f32_16x16x32_bf16 v[72:75], a[244:247], v[204:207], v[72:75]\n" \
    "s_mul_i32 s61, s63, 0x202000\n" \
    "s_and_b32 s61, s61, 0xffe02000\n" \
    "v_mfma_f32_16x16x32_bf16 v[76:79], a[248:251], v[204:207], v[76:79]\n" \
    "s_xor_b32 s61, s61, %[pol]\n" \
    "s_cbranch_vccnz LS2_c1r_%=\n" \
    "LS2_c1_%=:\n" \
    "v_mfma_f32_16x16x32_bf16 v[80:83], a[252:255], v[204:207], v[80:83]\n" \
    "s_add_u32 s97, s60, 151552\n" \
    "buffer_load_dwordx4 v[204:207], v35, %[ws], s97 offen sc1\n" \
    "s_mul_i32 s62, s95, 262144\n" \
    "s_add_u32 s62, s62, 0\n" \
    "s_waitcnt vmcnt(18)\n" \
    "v_mfma_f32_16x16x32_bf16 v[128:131], a[0:3], v[144:147], 0\n" \
    "s_nop 0\n" \
    "s_nop 0\n" \
    "v_mfma_f32_16x16x32_bf16 v[132:135], a[4:7], v[144:147], 0\n" \
    "ds_write_b128 v86, v[68:71] offset:0\n" \
    "s_nop 0\n" \
    "v_mfma_f32_16x16x32_bf16 v[136:139], a[8:11], v[144:147], 0\n" \
    "ds_write_b128 v86, v[72:75] offset:64\n" \
    "s_nop 0\n" \
    "v_mfma_f32_16x16x32_bf16 v[140:143], a[12:15], v[144:147], 0\n" \
    "s_add_u32 s97, s61, 0\n" \
    "buffer_load_dwordx4 v[144:147], v32, %[ws], s97 offen sc1\n" \
    "s_waitcnt vmcnt(18)\n" \
    "v_mfma_f32_16x16x32_bf16 v[128:131], a[16:19], v[148:151], v[128:131]\n" \
    "ds_write_b128 v86, v[76:79] offset:128\n" \
    "s_nop 0\n" \
    "v_mfma_f32_16x16x32_bf16 v[132:135], a[20:23], v[148:151], v[132:135]\n" \
    "ds_write_b128 v86, v[80:83] offset:192\n" \
    "s_nop 0\n" \
    "v_mfma_f32_16x16x32_bf16 v[136:139], a[24:27], v[148:151], v[136:139]\n" \
    "v_xor_b32 v86, 32768, v86\n" \
    "s_nop 0\n" \
    "v_mfma_f32_16x16x32_bf16 v[140:143], a[28:31], v[148:151], v[140:143]\n" \
    "s_add_u32 s97, s61, 0\n" \
    "buffer_load_dwordx4 v[148:151], v33, %[ws], s97 offen sc1\n" \
    "s_waitcnt vmcnt(18)\n" \
    "v_mfma_f32_16x16x32_bf16 v[128:131], a[32:35], v[152:155], v[128:131]\n" \
    "s_nop 0\n" \
    "s_nop 0\n" \
    "v_mfma_f32_16x16x32_bf16 v[132:135], a[36:39], v[152:155], v[132:135]\n" \
    "s_nop 0\n" \
    "s_nop 0\n" \
    "v_mfma_f32_16x16x32_bf16 v[136:139], a[40:43], v[152:155], v[136:139]\n" \
    "s_nop 0\n" \
    "s_nop 0\n" \
    "v_mfma_f32_16x16x32_bf16 v[140:143], a[44:47], v[152:155], v[140:143]\n" \
    "s_add_u32 s97, s61, 0\n" \
    "buffer_load_dwordx4 v[152:155], v34, %[ws], s97 offen sc1\n" \
    "s_waitcnt vmcnt(18)\n" \
    "v_mfma_f32_16x16x32_bf16 v[128:131], a[48:51], v[156:159], v[128:131]\n" \
    "s_waitcnt lgkmcnt(0)\n" \
    "s_barrier\n" \
    "v_mfma_f32_16x16x32_bf16 v[132:135], a[52:55], v[156:159], v[132:135]\n" \
    "ds_read_b128 v[96:99], v87 offset:0\n" \
    "ds_read_b128 v[100:103], v87 offset:4352\n" \
    "v_mfma_f32_16x16x32_bf16 v[136:139], a[56:59], v[156:159], v[136:139]\n" \
    "ds_read_b128 v[104:107], v87 offset:8704\n" \
    "ds_read_b128 v[108:111], v87 offset:13056\n" \
    "v_mfma_f32_16x16x32_bf16 v[140:143], a[60:63], v[156:159], v[140:143]\n" \
    "s_add_u32 s97, s61, 0\n" \
    "buffer_load_dwordx4 v[156:159], v35, %[ws], s97 offen sc1\n" \
    "s_waitcnt vmcnt(18)\n" \
    "v_mfma_f32_16x16x32_bf16 v[128:131], a[64:67], v[160:163], v[128:131]\n" \
    "v_xor_b32 v87, 32768, v87\n" \
    "s_nop 0\n" \
    "v_mfma_f32_16x16x32_bf16 v[132:135], a[68:71], v[160:163], v[132:135]\n" \
    "s_nop 0\n" \
    "s_nop 0\n" \
    "v_mfma_f32_16x16x32_bf16 v[136:139], a[72:75], v[160:163], v[136:139]\n" \
    "s_nop 0\n" \
    "s_nop 0\n" \
    "v_mfma_f32_16x16x32_bf16 v[140:143], a[76:79], v[160:163], v[140:143]\n" \
    "s_add_u32 s97, s61, 4096\n" \
    "buffer_load_dwordx4 v[160:163], v32, %[ws], s97 offen sc1\n" \
    "s_waitcnt vmcnt(18)\n" \
    "v_mfma_f32_16x16x32_bf16 v[128:131], a[80:83], v[164:167], v[128:131]\n" \
    "s_waitcnt lgkmcnt(0)\n" \
    "v_pk_add_f32 v[96:97], v[96:97], v[100:101]\n" \
    "v_mfma_f32_16x16x32_bf16 v[132:135], a[84:87], v[164:167], v[132:135]\n" \
    "v_pk_add_f32 v[98:99], v[98:99], v[102:103]\n" \
    "v_pk_add_f32 v[104:105], v[104:105], v[108:109]\n" \
    "v_mfma_f32_16x16x32_bf16 v[136:139], a[88:91], v[164:167], v[136:139]\n" \
    "v_pk_add_f32 v[106:107], v[106:107], v[110:111]\n" \
    "v_pk_add_f32 v[96:97], v[96:97], v[104:105]\n" \
    "v_mfma_f32_16x16x32_bf16 v[140:143], a[92:95], v[164:167], v[140:143]\n" \
    "s_add_u32 s97, s61, 4096\n" \
    "buffer_load_dwordx4 v[164:167], v33, %[ws], s97 offen sc1\n" \
    "s_waitcnt vmcnt(18)\n" \
    "v_mfma_f32_16x16x32_bf16 v[128:131], a[96:99], v[168:171], v[128:131]\n" \
    "v_pk_add_f32 v[98:99], v[98:99], v[106:107]\n" \
    "s_waitcnt vmcnt(12)\n" \
    "v_mfma_f32_16x16x32_bf16 v[132:135], a[100:103], v[168:171], v[132:135]\n" \
    "v_lshlrev_b32 v112, 16, v88\n" \
    "v_and_b32 v113, 0xffff0000, v88\n" \
    "v_mfma_f32_16x16x32_bf16 v[136:139], a[104:107], v[168:171], v[136:139]\n" \
    "v_lshlrev_b32 v114, 16, v89\n" \
    "v_and_b32 v115, 0xffff0000, v89\n" \
    "v_mfma_f32_16x16x32_bf16 v[140:143], a[108:111], v[168:171], v[140:143]\n" \
    "s_add_u32 s97, s61, 4096\n" \
    "buffer_load_dwordx4 v[168:171], v34, %[ws], s97 offen sc1\n" \
    "s_waitcnt vmcnt(18)\n" \
    "v_mfma_f32_16x16x32_bf16 v[128:131], a[112:115], v[172:175], v[128:131]\n" \
    "v_mul_f32 v112, v112, v92\n" \
    "v_mul_f32 v113, v113, v93\n" \
    "v_mfma_f32_16x16x32_bf16 v[132:135], a[116:119], v[172:175], v[132:135]\n" \
    "v_mul_f32 v114, v114, v94\n" \
    "v_mul_f32 v115, v115, v95\n" \
    "v_mfma_f32_16x16x32_bf16 v[136:139], a[120:123], v[172:175], v[136:139]\n" \
    "v_cvt_pk_bf16_f32 v116, v112, v113\n" \
    "v_cvt_pk_bf16_f32 v117, v114, v115\n" \
    "v_mfma_f32_16x16x32_bf16 v[140:143], a[124:127], v[172:175], v[140:143]\n" \
    "s_add_u32 s97, s61, 4096\n" \
    "buffer_load_dwordx4 v[172:175], v35, %[ws], s97 offen sc1\n" \
    "s_waitcnt vmcnt(18)\n" \
    "v_mfma_f32_16x16x32_bf16 v[128:131], a[128:131], v[176:179], v[128:131]\n" \
    "v_lshlrev_b32 v112, 16, v116\n" \
    "v_and_b32 v113, 0xffff0000, v116\n" \
    "v_mfma_f32_16x16x32_bf16 v[132:135], a[132:135], v[176:179], v[132:135]\n" \
    "v_lshlrev_b32 v114, 16, v117\n" \
    "v_and_b32 v115, 0xffff0000, v117\n" \
    "v_mfma_f32_16x16x32_bf16 v[136:139], a[136:139], v[176:179], v[136:139]\n" \
    "v_fma_f32 v96, 2.0, v96, v112\n" \
    "v_fma_f32 v97, 2.0, v97, v113\n" \
    "v_mfma_f32_16x16x32_bf16 v[140:143], a[140:143], v[176:179], v[140:143]\n" \
    "s_add_u32 s97, s61, 16384\n" \
    "buffer_load_dwordx4 v[176:179], v32, %[ws], s97 offen sc1\n" \
    "s_waitcnt vmcnt(17)\n" \
    "v_mfma_f32_16x16x32_bf16 v[128:131], a[144:147], v[180:183], v[128:131]\n" \
    "v_fma_f32 v98, 2.0, v98, v114\n" \
    "v_fma_f32 v99, 2.0, v99, v115\n" \
    "v_mfma_f32_16x16x32_bf16 v[132:135], a[148:151], v[180:183], v[132:135]\n" \
    "buffer_store_dwordx4 v[96:99], %[vout], %[od], s62 offen sc1\n" \
    "v_mfma_f32_16x16x32_bf16 v[136:139], a[152:155], v[180:183], v[136:139]\n" \
    "v_mfma_f32_16x16x32_bf16 v[140:143], a[156:159], v[180:183], v[140:143]\n" \
    "s_add_u32 s97, s61, 16384\n" \
    "buffer_load_dwordx4 v[180:183], v33, %[ws], s97 offen sc1\n" \
    "s_waitcnt vmcnt(18)\n" \
    "v_mfma_f32_16x16x32_bf16 v[128:131], a[160:163], v[184:187], v[128:131]\n" \
    "v_mfma_f32_16x16x32_bf16 v[132:135], a[164:167], v[184:187], v[132:135]\n" \
    "v_mfma_f32_16x16x32_bf16 v[136:139], a[168:171], v[184:187], v[136:139]\n" \
    "v_mfma_f32_16x16x32_bf16 v[140:143], a[172:175], v[184:187], v[140:143]\n" \
    "s_add_u32 s97, s61, 16384\n" \
    "buffer_load_dwordx4 v[184:187], v34, %[ws], s97 offen sc1\n" \
    "s_waitcnt vmcnt(16)\n" \
    "v_mfma_f32_16x16x32_bf16 v[128:131], a[176:179], v[188:191], v[128:131]\n" \
    "v_mfma_f32_16x16x32_bf16 v[132:135], a[180:183], v[188:191], v[132:135]\n" \
    "v_mfma_f32_16x16x32_bf16 v[136:139], a[184:187], v[188:191], v[136:139]\n" \
    "v_mfma_f32_16x16x32_bf16 v[140:143], a[188:191], v[188:191], v[140:143]\n" \
    "s_add_u32 s97, s61, 16384\n" \
    "buffer_load_dwordx4 v[188:191], v35, %[ws], s97 offen sc1\n" \
    "s_waitcnt vmcnt(16)\n" \
    "v_mfma_f32_16x16x32_bf16 v[128:131], a[192:195], v[192:195], v[128:131]\n" \
    "v_mfma_f32_16x16x32_bf16 v[132:135], a[196:199], v[192:195], v[132:135]\n" \
    "v_mfma_f32_16x16x32_bf16 v[136:139], a[200:203], v[192:195], v[136:139]\n" \
    "v_mfma_f32_16x16x32_bf16 v[140:143], a[204:207], v[192:195], v[140:143]\n" \
    "s_add_u32 s97, s61, 20480\n" \
    "buffer_load_dwordx4 v[192:195], v32, %[ws], s97 offen sc1\n" \
    "s_waitcnt vmcnt(16)\n" \
    "v_mfma_f32_16x16x32_bf16 v[128:131], a[208:211], v[196:199], v[128:131]\n" \
    "v_mfma_f32_16x16x32_bf16 v[132:135], a[212:215], v[196:199], v[132:135]\n" \
    "v_mfma_f32_16x16x32_bf16 v[136:139], a[216:219], v[196:199], v[136:139]\n" \
    "v_mfma_f32_16x16x32_bf16 v[140:143], a[220:223], v[196:199], v[140:143]\n" \
    "s_add_u32 s97, s61, 20480\n" \
    "buffer_load_dwordx4 v[196:199], v33, %[ws], s97 offen sc1\n" \
    "s_waitcnt vmcnt(16)\n" \
    "v_mfma_f32_16x16x32_bf16 v[128:131], a[224:227], v[200:203], v[128:131]\n" \
    "buffer_load_dword v84, %[vfl], %[ws], %[pol] offen sc1\n" \
    "v_mfma_f32_16x16x32_bf16 v[132:135], a[228:231], v[200:203], v[132:135]\n" \
    "v_mfma_f32_16x16x32_bf16 v[136:139], a[232:235], v[200:203], v[136:139]\n" \
    "v_mfma_f32_16x16x32_bf16 v[140:143], a[236:239], v[200:203], v[140:143]\n" \
    "s_add_u32 s97, s61, 20480\n" \
    "buffer_load_dwordx4 v[200:203], v34, %[ws], s97 offen sc1\n" \
    "s_waitcnt vmcnt(17)\n" \
    "v_mfma_f32_16x16x32_bf16 v[128:131], a[240:243], v[204:207], v[128:131]\n" \
    "v_mfma_f32_16x16x32_bf16 v[132:135], a[244:247], v[204:207], v[132:135]\n" \
    "v_mfma_f32_16x16x32_bf16 v[136:139], a[248:251], v[204:207], v[136:139]\n" \
    "v_mfma_f32_16x16x32_bf16 v[140:143], a[252:255], v[204:207], v[140:143]\n" \
    "s_add_u32 s97, s61, 20480\n" \
    "buffer_load_dwordx4 v[204:207], v35, %[ws], s97 offen sc1\n" \
    "s_mul_i32 s62, s95, 262144\n" \
    "s_add_u32 s62, s62, 8192\n" \
    "s_cmp_lg_u32 s63, s95\n" \
    "s_mov_b32 s95, s63\n" \
    "s_mov_b32 s60, s61\n" \
    "s_cbranch_scc1 LS2_trip_%=\n" \
    "s_nop 7\n" \
    "s_nop 1\n" \
    "ds_write_b128 v86, v[128:131] offset:0\n" \
    "s_nop 1\n" \
    "ds_write_b128 v86, v[132:135] offset:64\n" \
    "s_nop 1\n" \
    "ds_write_b128 v86, v[136:139] offset:128\n" \
    "s_nop 1\n" \
    "ds_write_b128 v86, v[140:143] offset:192\n" \
    "s_nop 1\n" \
    "v_xor_b32 v86, 32768, v86\n" \
    "s_waitcnt vmcnt(0)\n" \
    "s_waitcnt lgkmcnt(0)\n" \
    "s_barrier\n" \
    "ds_read_b128 v[96:99], v87 offset:0\n" \
    "ds_read_b128 v[100:103], v87 offset:4352\n" \
    "ds_read_b128 v[104:107], v87 offset:8704\n" \
    "ds_read_b128 v[108:111], v87 offset:13056\n" \
    "v_xor_b32 v87, 32768, v87\n" \
    "s_nop 0\n" \
    "s_nop 0\n" \
    "s_nop 0\n" \
    "s_nop 0\n" \
    "s_nop 0\n" \
    "s_waitcnt lgkmcnt(0)\n" \
    "v_pk_add_f32 v[96:97], v[96:97], v[100:101]\n" \
    "v_pk_add_f32 v[98:99], v[98:99], v[102:103]\n" \
    "v_pk_add_f32 v[104:105], v[104:105], v[108:109]\n" \
    "v_pk_add_f32 v[106:107], v[106:107], v[110:111]\n" \
    "v_pk_add_f32 v[96:97], v[96:97], v[104:105]\n" \
    "v_pk_add_f32 v[98:99], v[98:99], v[106:107]\n" \
    "v_lshlrev_b32 v112, 16, v90\n" \
    "v_and_b32 v113, 0xffff0000, v90\n" \
    "v_lshlrev_b32 v114, 16, v91\n" \
    "v_and_b32 v115, 0xffff0000, v91\n" \
    "v_mul_f32 v112, v112, v92\n" \
    "v_mul_f32 v113, v113, v93\n" \
    "v_mul_f32 v114, v114, v94\n" \
    "v_mul_f32 v115, v115, v95\n" \
    "v_cvt_pk_bf16_f32 v116, v112, v113\n" \
    "v_cvt_pk_bf16_f32 v117, v114, v115\n" \
    "v_lshlrev_b32 v112, 16, v116\n" \
    "v_and_b32 v113, 0xffff0000, v116\n" \
    "v_lshlrev_b32 v114, 16, v117\n" \
    "v_and_b32 v115, 0xffff0000, v117\n" \
    "v_fma_f32 v96, 2.0, v96, v112\n" \
    "v_fma_f32 v97, 2.0, v97, v113\n" \
    "v_fma_f32 v98, 2.0, v98, v114\n" \
    "v_fma_f32 v99, 2.0, v99, v115\n" \
    "buffer_store_dwordx4 v[96:99], %[vout], %[od], s62 offen sc1\n" \
    "s_branch LS2_end_%=\n" \
    "LS2_h1r_%=:\n" \
    "s_sleep 2\n" \
    "s_add_u32 s98, s98, 1\n" \
    "s_cmp_gt_u32 s98, 131072\n" \
    "s_cbranch_scc1 LS2_fail_%=\n" \
    "s_branch LS2_h1_%=\n" \
    "LS2_hsr_%=:\n" \
    "s_sleep 2\n" \
    "s_add_u32 s98, s98, 1\n" \
    "s_cmp_gt_u32 s98, 131072\n" \
    "s_cbranch_scc1 LS2_fail_%=\n" \
    "s_branch LS2_hs_%=\n" \
    "LS2_redo_%=:\n" \
    "s_waitcnt vmcnt(0) lgkmcnt(0)\n" \
    "s_barrier\n" \
    "s_or_b32 %[cfg], s97, 256\n" \
    "s_branch LS2_end_%=\n" \
    "LS2_c0r_%=:\n" \
    "buffer_load_dword v84, %[vfl], %[ws], %[pol] offen sc1\n" \
    "s_waitcnt vmcnt(0)\n" \
    "v_cmp_gt_i32 vcc, s63, v84\n" \
    "s_cbranch_vccz LS2_c0_%=\n" \
    "s_sleep 1\n" \
    "s_add_u32 s98, s98, 1\n" \
    "s_cmp_gt_u32 s98, 131072\n" \
    "s_cbranch_scc1 LS2_fail_%=\n" \
    "s_branch LS2_c0r_%=\n" \
    "LS2_c1r_%=:\n" \
    "buffer_load_dword v84, %[vfl], %[ws], %[pol] offen sc1\n" \
    "s_waitcnt vmcnt(0)\n" \
    "v_cmp_gt_i32 vcc, s63, v84\n" \
    "s_cbranch_vccz LS2_c1_%=\n" \
    "s_sleep 1\n" \
    "s_add_u32 s98, s98, 1\n" \
    "s_cmp_gt_u32 s98, 131072\n" \
    "s_cbranch_scc1 LS2_fail_%=\n" \
    "s_branch LS2_c1r_%=\n" \
    "LS2_first_%=:\n" \
    "v_mfma_f32_16x16x32_bf16 v[68:71], a[0:3], v[144:147], 0\n" \
    "v_mfma_f32_16x16x32_bf16 v[72:75], a[4:7], v[144:147], 0\n" \
    "v_mfma_f32_16x16x32_bf16 v[76:79], a[8:11], v[144:147], 0\n" \
    "v_mfma_f32_16x16x32_bf16 v[80:83], a[12:15], v[144:147], 0\n" \
    "s_add_u32 s97, s60, 131072\n" \
    "buffer_load_dwordx4 v[144:147], v32, %[ws], s97 offen sc1\n" \
    "v_mfma_f32_16x16x32_bf16 v[68:71], a[16:19], v[148:151], v[68:71]\n" \
    "v_mfma_f32_16x16x32_bf16 v[72:75], a[20:23], v[148:151], v[72:75]\n" \
    "v_mfma_f32_16x16x32_bf16 v[76:79], a[24:27], v[148:151], v[76:79]\n" \
    "v_mfma_f32_16x16x32_bf16 v[80:83], a[28:31], v[148:151], v[80:83]\n" \
    "s_add_u32 s97, s60, 131072\n" \
    "buffer_load_dwordx4 v[148:151], v33, %[ws], s97 offen sc1\n" \
    "v_mfma_f32_16x16x32_bf16 v[68:71], a[32:35], v[152:155], v[68:71]\n" \
    "v_mfma_f32_16x16x32_bf16 v[72:75], a[36:39], v[152:155], v[72:75]\n" \
    "v_mfma_f32_16x16x32_bf16 v[76:79], a[40:43], v[152:155], v[76:79]\n" \
    "v_mfma_f32_16x16x32_bf16 v[80:83], a[44:47], v[152:155], v[80:83]\n" \
    "s_add_u32 s97, s60, 131072\n" \
    "buffer_load_dwordx4 v[152:155], v34, %[ws], s97 offen sc1\n" \
    "v_mfma_f32_16x16x32_bf16 v[68:71], a[48:51], v[156:159], v[68:71]\n" \
    "v_mfma_f32_16x16x32_bf16 v[72:75], a[52:55], v[156:159], v[72:75]\n" \
    "v_mfma_f32_16x16x32_bf16 v[76:79], a[56:59], v[156:159], v[76:79]\n" \
    "v_mfma_f32_16x16x32_bf16 v[80:83], a[60:63], v[156:159], v[80:83]\n" \
    "s_add_u32 s97, s60, 131072\n" \
    "buffer_load_dwordx4 v[156:159], v35, %[ws], s97 offen sc1\n" \
    "v_mfma_f32_16x16x32_bf16 v[68:71], a[64:67], v[160:163], v[68:71]\n" \
    "v_mfma_f32_16x16x32_bf16 v[72:75], a[68:71], v[160:163], v[72:75]\n" \
    "v_mfma_f32_16x16x32_bf16 v[76:79], a[72:75], v[160:163], v[76:79]\n" \
    "v_mfma_f32_16x16x32_bf16 v[80:83], a[76:79], v[160:163], v[80:83]\n" \
    "s_add_u32 s97, s60, 135168\n" \
    "buffer_load_dwordx4 v[160:163], v32, %[ws], s97 offen sc1\n" \
    "v_mfma_f32_16x16x32_bf16 v[68:71], a[80:83], v[164:167], v[68:71]\n" \
    "v_mfma_f32_16x16x32_bf16 v[72:75], a[84:87], v[164:167], v[72:75]\n" \
    "v_mfma_f32_16x16x32_bf16 v[76:79], a[88:91], v[164:167], v[76:79]\n" \
    "v_mfma_f32_16x16x32_bf16 v[80:83], a[92:95], v[164:167], v[80:83]\n" \
    "s_add_u32 s97, s60, 135168\n" \
    "buffer_load_dwordx4 v[164:167], v33, %[ws], s97 offen sc1\n" \
    "v_mfma_f32_16x16x32_bf16 v[68:71], a[96:99], v[168:171], v[68:71]\n" \
    "v_mfma_f32_16x16x32_bf16 v[72:75], a[100:103], v[168:171], v[72:75]\n" \
    "v_mfma_f32_16x16x32_bf16 v[76:79], a[104:107], v[168:171], v[76:79]\n" \
    "v_mfma_f32_16x16x32_bf16 v[80:83], a[108:111], v[168:171], v[80:83]\n" \
    "s_add_u32 s97, s60, 135168\n" \
    "buffer_load_dwordx4 v[168:171], v34, %[ws], s97 offen sc1\n" \
    "v_mfma_f32_16x16x32_bf16 v[68:71], a[112:115], v[172:175], v[68:71]\n" \
    "v_mfma_f32_16x16x32_bf16 v[72:75], a[116:119], v[172:175], v[72:75]\n" \
    "v_mfma_f32_16x16x32_bf16 v[76:79], a[120:123], v[172:175], v[76:79]\n" \
    "v_mfma_f32_16x16x32_bf16 v[80:83], a[124:127], v[172:175], v[80:83]\n" \
    "s_add_u32 s97, s60, 135168\n" \
    "buffer_load_dwordx4 v[172:175], v35, %[ws], s97 offen sc1\n" \
    "v_mfma_f32_16x16x32_bf16 v[68:71], a[128:131], v[176:179], v[68:71]\n" \
    "v_mfma_f32_16x16x32_bf16 v[72:75], a[132:135], v[176:179], v[72:75]\n" \
    "v_mfma_f32_16x16x32_bf16 v[76:79], a[136:139], v[176:179], v[76:79]\n" \
    "v_mfma_f32_16x16x32_bf16 v[80:83], a[140:143], v[176:179], v[80:83]\n" \
    "s_add_u32 s97, s60, 147456\n" \
    "buffer_load_dwordx4 v[176:179], v32, %[ws], s97 offen sc1\n" \
    "v_mfma_f32_16x16x32_bf16 v[68:71], a[144:147], v[180:183], v[68:71]\n" \
    "v_mfma_f32_16x16x32_bf16 v[72:75], a[148:151], v[180:183], v[72:75]\n" \
    "v_mfma_f32_16x16x32_bf16 v[76:79], a[152:155], v[180:183], v[76:79]\n" \
    "v_mfma_f32_16x16x32_bf16 v[80:83], a[156:159], v[180:183], v[80:83]\n" \
    "s_add_u32 s97, s60, 147456\n" \
    "buffer_load_dwordx4 v[180:183], v33, %[ws], s97 offen sc1\n" \
    "v_mfma_f32_16x16x32_bf16 v[68:71], a[160:163], v[184:187], v[68:71]\n" \
    "v_mfma_f32_16x16x32_bf16 v[72:75], a[164:167], v[184:187], v[72:75]\n" \
    "v_mfma_f32_16x16x32_bf16 v[76:79], a[168:171], v[184:187], v[76:79]\n" \
    "v_mfma_f32_16x16x32_bf16 v[80:83], a[172:175], v[184:187], v[80:83]\n" \
    "s_add_u32 s97, s60, 147456\n" \
    "buffer_load_dwordx4 v[184:187], v34, %[ws], s97 offen sc1\n" \
    "s_waitcnt vmcnt(0)\n" \
    "s_branch LS2_join_%=\n" \
    "LS2_fail_%=:\n" \
    "v_mov_b32 v100, 2043904\n" \
    "v_mov_b32 v101, 1\n" \
    "buffer_store_dword v101, v100, %[ws], 0 offen sc1\n" \
    "s_endpgm\n" \
    "LS2_end_%=:\n"
#define ASM_CLOBBERS "memory", "vcc", "scc", \
    "v31", "v32", "v33", "v34", "v35", "v36", "v37", "v38", "v39", "v40", "v41", "v42", "v43", "v44", "v45", "v46", "v47", "v48", "v49", "v50", "v51", "v52", "v53", "v54", \
    "v55", "v56", "v57", "v58", "v59", "v60", "v61", "v62", "v63", "v64", "v65", "v66", "v67", "v68", "v69", "v70", "v71", "v72", "v73", "v74", "v75", "v76", "v77", "v78", \
    "v79", "v80", "v81", "v82", "v83", "v84", "v85", "v86", "v87", "v88", "v89", "v90", "v91", "v92", "v93", "v94", "v95", "v96", "v97", "v98", "v99", "v100", "v101", "v102", \
    "v103", "v104", "v105", "v106", "v107", "v108", "v109", "v110", "v111", "v112", "v113", "v114", "v115", "v116", "v117", "v118", "v119", "v120", "v121", "v122", "v123", "v124", "v125", "v126", \
    "v127", "v128", "v129", "v130", "v131", "v132", "v133", "v134", "v135", "v136", "v137", "v138", "v139", "v140", "v141", "v142", "v143", "v144", "v145", "v146", "v147", "v148", "v149", "v150", \
    "v151", "v152", "v153", "v154", "v155", "v156", "v157", "v158", "v159", "v160", "v161", "v162", "v163", "v164", "v165", "v166", "v167", "v168", "v169", "v170", "v171", "v172", "v173", "v174", \
    "v175", "v176", "v177", "v178", "v179", "v180", "v181", "v182", "v183", "v184", "v185", "v186", "v187", "v188", "v189", "v190", "v191", "v192", "v193", "v194", "v195", "v196", "v197", "v198", \
    "v199", "v200", "v201", "v202", "v203", "v204", "v205", "v206", "v207", "v208", "v209", "v210", "v211", "v212", "v213", "v214", "v215", "v216", "v217", "v218", "v219", "v220", "v221", "v222", \
    "v223", "v224", "v225", "v226", "v227", "v228", "v229", "v230", "v231", "v232", "v233", "v234", "v235", "v236", "v237", "v238", "v239", "v240", "v241", "v242", "v243", "v244", "v245", "v246", \
    "v247", "v248", "v249", "v250", "v251", "v252", "v253", "v254", "v255", "a0", "a1", "a2", "a3", "a4", "a5", "a6", "a7", "a8", "a9", "a10", "a11", "a12", "a13", "a14", \
    "a15", "a16", "a17", "a18", "a19", "a20", "a21", "a22", "a23", "a24", "a25", "a26", "a27", "a28", "a29", "a30", "a31", "a32", "a33", "a34", "a35", "a36", "a37", "a38", \
    "a39", "a40", "a41", "a42", "a43", "a44", "a45", "a46", "a47", "a48", "a49", "a50", "a51", "a52", "a53", "a54", "a55", "a56", "a57", "a58", "a59", "a60", "a61", "a62", \
    "a63", "a64", "a65", "a66", "a67", "a68", "a69", "a70", "a71", "a72", "a73", "a74", "a75", "a76", "a77", "a78", "a79", "a80", "a81", "a82", "a83", "a84", "a85", "a86", \
    "a87", "a88", "a89", "a90", "a91", "a92", "a93", "a94", "a95", "a96", "a97", "a98", "a99", "a100", "a101", "a102", "a103", "a104", "a105", "a106", "a107", "a108", "a109", "a110", \
    "a111", "a112", "a113", "a114", "a115", "a116", "a117", "a118", "a119", "a120", "a121", "a122", "a123", "a124", "a125", "a126", "a127", "a128", "a129", "a130", "a131", "a132", "a133", "a134", \
    "a135", "a136", "a137", "a138", "a139", "a140", "a141", "a142", "a143", "a144", "a145", "a146", "a147", "a148", "a149", "a150", "a151", "a152", "a153", "a154", "a155", "a156", "a157", "a158", \
    "a159", "a160", "a161", "a162", "a163", "a164", "a165", "a166", "a167", "a168", "a169", "a170", "a171", "a172", "a173", "a174", "a175", "a176", "a177", "a178", "a179", "a180", "a181", "a182", \
    "a183", "a184", "a185", "a186", "a187", "a188", "a189", "a190", "a191", "a192", "a193", "a194", "a195", "a196", "a197", "a198", "a199", "a200", "a201", "a202", "a203", "a204", "a205", "a206", \
    "a207", "a208", "a209", "a210", "a211", "a212", "a213", "a214", "a215", "a216", "a217", "a218", "a219", "a220", "a221", "a222", "a223", "a224", "a225", "a226", "a227", "a228", "a229", "a230", \
    "a231", "a232", "a233", "a234", "a235", "a236", "a237", "a238", "a239", "a240", "a241", "a242", "a243", "a244", "a245", "a246", "a247", "a248", "a249", "a250", "a251", "a252", "a253", "a254", \
    "a255", "s60", "s61", "s62", "s63", "s64", "s65", "s66", "s88", "s89", "s90", "s91", "s92", "s93", "s94", "s95", "s96", "s97", "s98"
#define ASM_CLOBBERS_CHAIN ASM_CLOBBERS, \
    "s40", "s41", "s42", "s43", "s44", "s45", "s46", "s47", "s48", "s49", "s50", "s51", "s52", "s53", "s54", "s55", "s56", "s57", "s58", "s59", "s67", "s68", "s69", "s70", \
    "s71", "s72", "s73", "s74", "s75", "s76", "s77", "s78", "s79", "s80", "s81", "s82", "s83", "s84", "s85", "s86", "s87", "s99"

#ifndef M_O0
#define M_O0 M_O
#define M_G0 M_G
#endif
#define DBG_OPERAND
#define DBG_OPERAND2

#define HS_OUT [cfg] "=&s"(cfg), [pol] "=&s"(pol), [npol] "=&s"(npol)
#define HS_IN [ws] "s"(mkdesc(p.ws, WS_USED)), [w] "s"(w), [est] "s"(est), [hsa] "s"(hsa)

DEV unsigned head_vo(int h, int lane) {
  const unsigned q = lane >> 4, sl = lane & 15;
  return q * 8192u + h * 256u + (sl & 1) * 16u + (((sl >> 1) ^ q) << 5);
}
DEV unsigned head_rd(int t, int lane) { return (lane >> 2) * 256 + ((t ^ ((lane >> 2) & 7)) << 5) + (lane & 3) * 8; }
DEV unsigned stage_x_col(int w, int lane) { return ((lane & 7) ^ (4 * (w & 1) + (lane >> 4))) << 4; }
DEV unsigned frag_x_off(int lane) {
  const unsigned r = lane & 15, g = lane >> 4;
  return (r >> 3) * 1024 + (r & 7) * 128 + ((g ^ ((r >> 1) & 7)) << 4);
}
DEV unsigned share8(int i, int w, int lane) { return lane < 16 ? ((i & 7) * 64 + w * 16 + lane) * 128u : NOWHERE; }
DEV unsigned vo_mem(int xr, int w, int lane) { return R_MEM + xr * 32768 + (w >> 1) * 16384 + (w & 1) * 4096 + lane * 16; }

DEV unsigned role_V(PRM p, int h, int xr, unsigned est, unsigned hsa, int w, int lane) {
  unsigned cfg, pol, npol;
  asm volatile(ASM_ROLE_V : HS_OUT : HS_IN, [rdy] "s"(RDY + (32 + h) * 16), [wd] "s"(mkdesc(p.Wv, 8u << 20)), [dx] "s"(mkdesc(p.xB, 4u << 20)),

               [vow] "v"(head_vo(h, lane)), [ra] "v"(65536 + head_rd(2 * w, lane)), [vmem] "v"(vo_mem(xr, w, lane)), [lr] "v"(131072 + lane * 16),
               [idx] "v"((lane & 2) ? 0xEEEEu : 0x4444u), [vv] "v"(R_V + phys(h * 2048 + w * 512) + lane * 8), [fo] "v"(frag_x_off(lane)),
               [vox] "v"((8 * w + (lane >> 3)) * 2048u + stage_x_col(w, lane)), [voa] "v"(BLK + h * 49152 + 32768 + w * 2048 + lane * 16),
               [vxt] "v"(share8(h, w, lane)), [vfl] "v"((w * 64 + lane) == 0 ? F_P + h * 128 + 64 : NOWHERE) DBG_OPERAND
               : ASM_CLOBBERS_CHAIN);
  return cfg;
}

DEV unsigned slow_line(const void* m) { return ((3u - ((unsigned)(size_t)m >> 7)) & 7u) * 128u; }

DEV unsigned role_A(PRM p, int h, int xr, unsigned est, unsigned hsa, int w, int lane) {
  const int c16 = lane & 15, g = lane >> 4, tt = w & 1;
  const unsigned vo = c16 < 8 ? R_O + phys((c16 >> 2) * 32768 + rotkb(h, 3)) + (16 * w + 4 * (c16 & 3) + g) * 16 : NOWHERE;
  const unsigned vh = lane < 16 ? (w * 1024 + (h & 7) * 16 + lane) * 2048u : NOWHERE;
  unsigned cfg, pol, npol;
  asm volatile(ASM_ROLE_A : HS_OUT : HS_IN, [rdy] "s"(RDY + h * 16), [dw] "s"(mkdesc(p.Wo, 8u << 20)), [hx] "s"((xr & 1) * 0x100400u + slow_line(p.Wo)), [vh] "v"(vh), [wd] "s"(mkdesc(p.Wq, 8u << 20)),
               [vow] "v"(head_vo(h, lane)),
               [ra] "v"(65536 + head_rd(2 * w, lane)),
               [vmem] "v"(vo_mem(xr, w, lane)), [lr] "v"(lane * 16), [idx] "v"((lane & 2) ? 0xEEEEu : 0x4444u),
               [vk] "v"((w >> 1) * 524288 + R_K + phys(h * 2048) + (w & 1) * 1024 + (lane ^ ((lane >> 2) & 4)) * 16),
               [kr] "v"((lane & 3) * 128 + (((lane >> 2) * 8) ^ ((lane & 2) * 32))), [vva] "v"(R_V + phys(h * 2048 + w * 512) + lane * 8), [vo] "v"(vo),
               [vx] "v"(BLK + h * 49152 + w * 2048 + lane * 16), [vox] "v"(BLK + phys(tt * 131072 + rotkb(4 * h + w, 5)) + lane * 16),
               [vox2] "v"(BLK + phys(tt * 131072 + rotkb(4 * h + (w ^ 1), 5)) + lane * 16),
               [vfp] "v"(F_P + h * 128 + lane % 3 * 32), [vfo] "v"((w * 64 + lane) == 0 ? F_O + h * 128 : NOWHERE),
               [qw] "v"((32 * w + c16) * 16 + g * 4), [qr] "v"((lane >> 2) * 16 + (lane & 1) * 8), [xq] "v"(tt * 4096 + lane * 16),
               [xva] "v"(w * 2048 + lane * 16), [lim] "v"(16 * tt + c16 - 4 * g),
               [vrp] "v"((h & 7) == 0 && (w * 64 + lane) == 0 ? REP + (h >> 3) * 128 : NOWHERE) DBG_OPERAND
               : ASM_CLOBBERS_CHAIN);
  return cfg;
}

DEV unsigned role_B(PRM p, int G, int c, int hk, int Gk, int xr, unsigned est, unsigned hsa, int w, int lane) {
  const unsigned vok = head_vo(hk, lane) + Gk * (4u << 20);
  const unsigned r16 = lane >> 2, s4 = lane & 3, gb = r16 >> 2, q = r16 & 3;
  const unsigned vow = (32 * (gb >> 1) + 16 * (gb & 1) + 4 * (q >> 1) + (q & 1)) * 2048u + c * 64u + (((s4 >> 1) ^ (gb & 1)) << 5) + (s4 & 1) * 16u;
  const unsigned rk = head_rd(4 * (w >> 1) + (w & 1), lane), rw = w * 32768 + r16 * 64 + ((gb & 1) << 5) + s4 * 8;
  const unsigned c16 = lane & 15, g = lane >> 4, a5 = lane >> 5, mm = (lane >> 2) & 7;
  const unsigned vmo = w == 0 || w == 3 ? R_MEM + ((xr ^ w) & 3) * 32768 + phys(rotkb(c >> 1, 2)) + (16 * (2 * (c & 1) + a5) + 8 * G + mm) * 16 + s4 * 4 : NOWHERE;
  const unsigned vm0 = R_MEM + xr * 32768 + phys(rotkb(8 * Gk + 2 * w, 2)) + lane * 16,
                 vm1 = R_MEM + xr * 32768 + phys(rotkb(8 * Gk + 2 * w + 1, 2)) + lane * 16 - 1024;
  const unsigned ow = 16384 + 4 * (w * 192 + g * 48 + c16);
  const unsigned orr = 16384 + 4 * ((2 * (mm >> 2) + (mm & 1)) * 48 + 16 * a5 + 2 * ((mm >> 1) & 1) + 4 * s4);
  const unsigned vse = 2 * ((4 * G + 2 * (mm >> 2) + (mm & 1)) * 1024 + 32 * c + 16 * a5 + 2 * ((mm >> 1) & 1) + 4 * s4);
  unsigned cfg, pol, npol;
  asm volatile(ASM_ROLE_B : HS_OUT : HS_IN, [rdy] "s"(RDY + (64 + 32 * G + c) * 16), [dk] "s"(mkdesc(p.Wk, 8u << 20)), [dw] "s"(mkdesc(p.Wo, 8u << 20)), [xr] "s"(4 * (xr & 1)), [m0p] "s"(p.mem0), [vse] "v"(vse),
               [vok] "v"(vok), [vow] "v"(vow), [rk] "v"(rk), [rw] "v"(rw), [vm0] "v"(vm0), [vm1] "v"(vm1),
               [vk] "v"(Gk * 524288 + R_K + phys(hk * 2048) + w * 512 + lane * 8), [vo] "v"(R_O + (G * 4 + w) * 16384 + lane * 16), [vmo] "v"(vmo),
               [lr] "v"(lane * 16), [ow] "v"(ow), [orr] "v"(orr), [vmsg] "v"(G == 0 && c == 0 && (w * 64 + lane) == 128 ? 0u : NOWHERE),
               [vrl] "v"(G == 0 && c == 0 && lane < 8 ? REP + lane * 128 : NOWHERE),
               [vrp] "v"((c & 15) == 0 && (w * 64 + lane) == 64 ? REP + (4 + 2 * G + (c >> 4)) * 128 : NOWHERE), [idxm] "v"((lane & 2) ? 0xEEEEu : 0x4444u),
               [idxo] "v"((lane & 1) ? 0xEEEEu : 0x4444u) DBG_OPERAND
               : ASM_CLOBBERS_CHAIN);
  return cfg;
}

DEV unsigned role_P(PRM p, int m, int h, unsigned est, unsigned hsa, int w, int lane) {
  unsigned cfg, pol, npol;
  asm volatile(ASM_ROLE_PQ : HS_OUT : HS_IN, [dx] "s"(mkdesc(p.xB, 4u << 20)), [wd] "s"(mkdesc(m ? p.Wk : p.Wq, 8u << 20)),
               [fo] "v"(frag_x_off(lane)), [vox] "v"((8 * w + (lane >> 3)) * 2048u + stage_x_col(w, lane)), [vow] "v"(head_vo(h, lane)),
               [ra] "v"(65536 + head_rd(2 * w, lane)), [voa] "v"(BLK + h * 49152 + m * 16384 + w * 1024 + lane * 16),
               [vxt] "v"((lane >> 3) == m ? share8(h, w, lane) : NOWHERE), [vbp] "v"(F_O + h * 128),
               [vfl] "v"((w * 64 + lane) == 0 ? F_P + h * 128 + m * 32 : NOWHERE) DBG_OPERAND
               : ASM_CLOBBERS);
  return cfg;
}

template <int N> DEV void touch(const void* ptr, unsigned first) {
  const rsrc_t r = mkrs(ptr, N << 20);
  unsigned v[N];
#pragma unroll
  for (int i = 0; i < N; ++i) v[i] = __builtin_amdgcn_raw_buffer_load_b32(r, first, ((i & 3) * 2 + (i >> 2)) << 20, 0);
#pragma unroll
  for (int i = 0; i < N; ++i) asm volatile("" ::"v"(v[i]));
}

DEV void scan_constants(PRM p, lptr L, int n, int w, int lane) {
  const int c16 = lane & 15, g = lane >> 4;
  const int pb = 64 * n + 32 * (w >> 1) + 4 * (w & 1);
  f32x4 k[14];
#pragma unroll
  for (int j = 0; j < 4; ++j) {
    const int q = pb + 8 * g + j;
    const float lr = bf2f(p.lre[q]), li = bf2f(p.lim[q]);
    const float dt = bf2f(pk2(expf(bf2f(p.ldt[q])), 0.f) & 0xffffu);
    const float er = expf(dt * lr);
    float sn, cs;
    sincosf(dt * li, &sn, &cs);
    float br = er * cs, bi = er * sn;
    const float den = 1.f / (lr * lr + li * li);
    k[0][j] = ((br - 1.f) * lr + bi * li) * den;
    k[1][j] = (bi * lr - (br - 1.f) * li) * den;
    k[2][j] = c16 == 0 ? br : 0.f;
    k[3][j] = c16 == 0 ? bi : 0.f;
#pragma unroll
    for (int e = 0; e < 5; ++e) {
      k[4 + 2 * e][j] = br;
      k[5 + 2 * e][j] = bi;
      const float t = br * br - bi * bi;
      bi = 2.f * br * bi;
      br = t;
    }
  }
#pragma unroll
  for (int i = 0; i < 14; ++i) *(LAS f32x4*)(L + i * 4096 + (w * 64 + lane) * 16) = k[i];
}

DEV unsigned role_S1(PRM p, int n, unsigned est, unsigned hsa, int w, int lane) {
  const int pb = 64 * n + 32 * (w >> 1) + 4 * (w & 1);
  unsigned cfg, pol, npol;
  asm volatile(ASM_ROLE_S1 : HS_OUT : HS_IN, [dx] "s"(mkdesc(p.xA, 4u << 20)), [dr] "s"(mkdesc(p.Bre, 2u << 20)),
               [di] "s"(mkdesc(p.Bim, 2u << 20)), [kc] "v"((w * 64 + lane) * 16), [fo] "v"(frag_x_off(lane)),
               [vox] "v"((16 * (w & 1) + 2 * (lane >> 3) + (w >> 1)) * 2048u + stage_x_col(w, lane)),
               [vow] "v"((pb + 8 * (lane >> 5) + ((lane >> 3) & 3)) * 2048u + stage_x_col(0, lane)), [voa] "v"(B_H + phys(rotkb(4 * n + w, 4)) + lane * 16),
               [vxt] "v"(lane < 32 ? ((n & 3) * 128 + w * 32 + lane) * 128u : NOWHERE), [vfl] "v"((w * 64 + lane) == 0 ? F_H + n * 128 : NOWHERE) DBG_OPERAND
               : ASM_CLOBBERS);
  return cfg;
}

DEV unsigned red_wp(int w, int lane) { return 4 * (w * 1088 + (lane & 15) * 68 + 4 * (lane >> 4)); }
DEV unsigned red_rp(int w, int lane) { return 4 * ((4 * w + (lane >> 4)) * 68 + 4 * (lane & 15)); }

DEV unsigned role_C(PRM p, int tt, int cc, int pre, unsigned est, unsigned hsa, int w, int lane) {
  const unsigned r8 = lane >> 3, sl = lane & 7, r16 = lane >> 2;
  const unsigned base = BLK + (tt * 32 + 8 * w) * 8192;
  unsigned cfg, pol, npol;
  asm volatile(ASM_ROLE_C : HS_OUT : HS_IN, [dw] "s"(mkdesc(p.Wo, 8u << 20)), [od] "s"(mkdesc(p.out, 16u << 20)),
               [pre] "s"(pre), [da] "s"(mkdesc(p.xA, 4u << 20)), [db] "s"(mkdesc(p.xB, 4u << 20)), [vxt] "v"(((16 * tt + cc) * 256 + (w * 64 + lane)) * 128),
               [vow] "v"(r8 * 2048u + cc * 128u + (((sl >> 1) ^ ((r8 >> 1) & 3)) << 5) + (sl & 1) * 16u),
               [rt] "v"(65536 + w * 24576 + r16 * 128 + (((r16 >> 1) & 3) << 5) + (lane & 3) * 8), [w25] "v"(20480 + w * 2048 + lane * 16),
               [vo] "v"(base + lane * 16), [vtc] "v"(lane < 32 ? base + phys((cc & 7) * 4096) + lane * 128 : NOWHERE),
               [vfl] "v"(F_O + (8 * w + (lane & 7)) * 128), [wp] "v"(red_wp(w, lane)), [rp] "v"(red_rp(w, lane)),
               [vout] "v"((16 * tt + 4 * w + (lane >> 4)) * 8192 + 4096 + cc * 256 + (lane & 15) * 16) DBG_OPERAND
               : ASM_CLOBBERS);
  return cfg;
}

DEV unsigned role_S2(PRM p, int n, unsigned est, unsigned hsa, int w, int lane) {
  const unsigned row = 2 * (4 * w + (lane >> 4)), col = 64 * n + 4 * (lane & 15);
  unsigned cfg, pol, npol;
  asm volatile(ASM_ROLE_S2 : HS_OUT : HS_IN, [dr] "s"(mkdesc(p.Cre, 2u << 20)), [di] "s"(mkdesc(p.Cim, 2u << 20)),
               [dd] "s"(mkdesc(p.dsk, 2048)), [dx] "s"(mkdesc(p.xA, 4u << 20)), [od] "s"(mkdesc(p.out, 16u << 20)),
               [vow] "v"((64 * n + (lane >> 3)) * 2048u + w * 512u + stage_x_col(0, lane)), [fo] "v"(frag_x_off(lane)), [vd] "v"(col * 2),
               [vpr] "v"(w == 0 ? (hsa >> 7 & 31) * 32768 + (lane & 24) * 1024 + (lane & 32) * 128 + (lane >> 4) * 768 + (lane & 7) * 16 : NOWHERE), [vx] "v"(row * 2048 + col * 2),
               [vo] "v"(B_H + w * 32768 + lane * 16),
               [vtc] "v"(lane < 32 ? B_H + phys((lane >> 4) * 65536 + w * 16384 + (n & 7) * 2048) + (lane & 15) * 128 : NOWHERE),
               [vfl] "v"(F_H + (4 * w + (lane & 3)) * 128), [wp] "v"(red_wp(w, lane)), [rp] "v"(red_rp(w, lane)), [vout] "v"(row * 8192 + col * 4) DBG_OPERAND
               : ASM_CLOBBERS);
  return cfg;
}

DEV int lead(const Params& p, lptr L, unsigned est, int w, int lane) {
  const rsrc_t ws = mkrs(p.ws, WS_USED);
#pragma unroll
  for (int i = 0; i < 13; ++i) st128(v4u{ARMED, ARMED, ARMED, ARMED}, ws, CTL + threadIdx.x * 16, i * 4096);
  drain_bar();
  if (w == 0) {
    unsigned cfg = est, told = ~0u;
    const bool chain = ((lane + est) & 4) != 0;
    v4u seen[4] = {};
    for (unsigned n = 0; n < (1u << 17); ++n) {
      asm volatile("" ::: "memory");
      v4u rq[4], dn[4];
#pragma unroll
      for (int k = 0; k < 4; ++k) {
        rq[k] = ld128(ws, REQ + lane * 128, k * 8192);
        dn[k] = ld128(ws, DONE + lane * 128, k * 8192);
      }
      const bool timer = !chain && lane >= 32;
      if (cfg < 8 && __builtin_amdgcn_ballot_w64(timer && dn[3].x == ARMED) == 0ull) {
        unsigned a = timer ? dn[3].x >> 12 : 0xfffu, b = timer ? dn[3].x & 0xfffu : 0xfffu;
#pragma unroll
        for (int o = 1; o < 64; o <<= 1) {
          a = min(a, (unsigned)__shfl_xor((int)a, o));
          b = min(b, (unsigned)__shfl_xor((int)b, o));
        }
        cfg |= __builtin_amdgcn_readfirstlane(a < b ? 8u : 12u);
      }
      bool open = false;
#pragma unroll
      for (int k = 0; k < 4; ++k) {
        const v4u x = rq[k] ^ seen[k];
        if (told != cfg || (x.x | x.y | x.z | x.w) != 0) st128((rq[k] & 0xFFFFFF00u) | cfg, ws, ACK + lane * 128, k * 8192);
        seen[k] = rq[k];
        if (k * 64 + lane != LEADER) open |= max(max(dn[k].x, dn[k].y), max(dn[k].z, dn[k].w)) == ARMED;
      }
      told = cfg;
      if (__builtin_amdgcn_ballot_w64(open) == 0ull) break;
    }
    if (lane == 0) *(LAS unsigned*)L = cfg;
  }
  drain_bar();
  const int cfg = __builtin_amdgcn_readfirstlane(*(LAS unsigned*)L);
  drain_bar();
  return cfg;
}

DEV unsigned run_role(lptr L, unsigned est, int pre, int w) {
  auto pp = (const __attribute__((address_space(4))) Params*)__builtin_amdgcn_kernarg_segment_ptr();
  unsigned blk = blockIdx.x;
  int lane;
  asm volatile("" : "+s"(pp), "+s"(blk), "+s"(w));
  asm volatile("v_mbcnt_lo_u32_b32 %0, -1, 0\n\tv_mbcnt_hi_u32_b32 %0, -1, %0" : "=v"(lane));
  PRM p = *pp;
  const unsigned hsa = REQ + blk * 128 + w * 4;
  const int g8 = (blk - LEADER + 4 + est) & 7, r = blk >> 3;
  unsigned cfg;
  if (g8 < 4) {
    const int h = 8 * g8 + (r & 7);
    if (r >= 16) cfg = role_B(p, g8 >> 1, 16 * (g8 & 1) + (r - 16), h, (r >> 3) & 1, g8, est, hsa, w, lane);
    else if (r < 8) cfg = role_A(p, h, g8, est, hsa, w, lane);
    else cfg = role_V(p, h, g8, est, hsa, w, lane);
  } else {
    const int x = g8 - 4, n = 4 * x + (r & 3);
    if (r < 16) {
      cfg = role_P(p, r >> 3, 8 * x + (r & 7), est, hsa, w, lane);
    } else {
      if (r < 24) {
        touch<8>(p.Wo, ((8 * x + r - 16) * 256 + w * 64 + lane) * 128);
        cfg = role_C(p, x >> 1, 8 * (x & 1) + (r - 16), pre, est, hsa, w, lane);
      } else if (r < 28) {
        scan_constants(p, L, n, w, lane);
        cfg = role_S1(p, n, est, hsa, w, lane);
      } else {
        cfg = role_S2(p, n, est, hsa, w, lane);
      }
    }
  }
  return cfg;
}

__global__ void __launch_bounds__(256, 1) fused_kernel(Params p0) {
  extern __shared__ __attribute__((aligned(16))) unsigned char smem[];
  const lptr L = (lptr)smem;
  {
    unsigned long long pc;
    unsigned o = ((blockIdx.x >> 3) % 3 * 256 + threadIdx.x) * 128;
    asm volatile("s_getpc_b64 %0\nLct_%=:\n\tv_min_u32 %1, Lsecond_try - Lct_%=, %1\n\tglobal_load_dword v255, %1, %0" : "=&s"(pc), "+v"(o)::"v255");
  }
  unsigned est = (xcc_id() - blockIdx.x + LEADER) & 3u;
  const int w = __builtin_amdgcn_readfirstlane(threadIdx.x >> 6);
  const int pre = blockIdx.x == LEADER ? lead(p0, L, est, w, threadIdx.x & 63) : -1;
  const unsigned cfg = run_role(L, est, pre, w);
  if (__builtin_expect(cfg >= 256, 0)) {
    asm volatile("Lsecond_try:");
    run_role(L, cfg & 3u, -1, w);
  }
}

}

extern "C" void kernel_launch(void* const* d_in, const int* in_sizes, int n_in, void* d_out, int out_size, void* d_ws, size_t ws_size,
                              hipStream_t stream) {
  (void)in_sizes;
  (void)n_in;
  (void)out_size;
  (void)ws_size;
  Params p;
  p.xA = (const u16*)d_in[0];
  p.lre = (const u16*)d_in[1];
  p.lim = (const u16*)d_in[2];
  p.Bre = (const u16*)d_in[3];
  p.Bim = (const u16*)d_in[4];
  p.Cre = (const u16*)d_in[5];
  p.Cim = (const u16*)d_in[6];
  p.ldt = (const u16*)d_in[7];
  p.dsk = (const u16*)d_in[8];
  p.xB = (const u16*)d_in[9];
  p.mem0 = (const u16*)d_in[10];
  p.Wq = (const u16*)d_in[11];
  p.Wk = (const u16*)d_in[12];
  p.Wv = (const u16*)d_in[13];
  p.Wo = (const u16*)d_in[14];
  p.out = (float*)d_out;
  p.ws = (unsigned char*)d_ws;
  (void)hipFuncSetAttribute((const void*)fused_kernel, hipFuncAttributeMaxDynamicSharedMemorySize, LDS_BYTES);
  fused_kernel<<<256, 256, LDS_BYTES, stream>>>(p);
}
```
